```python
import torch

def _softmax(x, axis=-1):
    return torch.softmax(x, dim=axis)
def _gelu(x):
    return torch.nn.functional.gelu(x, approximate="tanh")
def _silu(x):
    return torch.nn.functional.silu(x)
def _sigmoid(x):
    return torch.sigmoid(x)
def _layernorm(x, axis=-1, eps=1e-5):
    m = x.mean(dim=axis, keepdim=True)
    v = x.var(dim=axis, keepdim=True, unbiased=False)
    return (x - m) / torch.sqrt(v + eps)
def _scatter_add(tgt, idx, src, axis=0):
    return tgt.clone().index_add_(axis, idx, src)
def _segment_sum(src, seg, n):
    out = torch.zeros((n,) + src.shape[1:], dtype=src.dtype)
    return out.index_add_(0, seg, src)
def _pad(x, pw):
    flat = tuple(v for pair in reversed(pw) for v in pair)
    return torch.nn.functional.pad(x, flat)

kernel_name = 'interleave_layers__grouped_kv_mqa__mixer_token_mlp__prompt'

def setup_inputs():

    _w = (lambda a: (lambda t: t.to(torch.bfloat16) if t.is_floating_point() else t)(torch.as_tensor(a)))
    def _A_setup():
        B, S, D, H = 1, 2048, 640, 32
        Dh = D // H
        return {
            "x": _w((lambda *s: torch.randn(*s, dtype=torch.bfloat16) * 0.05)(B, S, D)),
            "wq": _w((lambda *s: torch.randn(*s, dtype=torch.bfloat16) * 0.05)(D, H * Dh)),
            "wk": _w((lambda *s: torch.randn(*s, dtype=torch.bfloat16) * 0.05)(D, Dh)),
            "wv": _w((lambda *s: torch.randn(*s, dtype=torch.bfloat16) * 0.05)(D, Dh)),
            "wo": _w((lambda *s: torch.randn(*s, dtype=torch.bfloat16) * 0.05)(H * Dh, D)),
        }
    _A_in = _A_setup()

    def _B_setup():
        B, S, D, F = 1, 2048, 640, 1728
        return {
            "x": _w((lambda *s: torch.randn(*s, dtype=torch.bfloat16) * 0.05)(B, S, D)),
            "w1": _w((lambda *s: torch.randn(*s, dtype=torch.bfloat16) * 0.4)(S, F) * (S ** -0.5)),
            "b1": _w((lambda *s: torch.randn(*s, dtype=torch.bfloat16) * 0.4)(F) * 0.02),
            "w2": _w((lambda *s: torch.randn(*s, dtype=torch.bfloat16) * 0.4)(F, S) * (F ** -0.5)),
            "b2": _w((lambda *s: torch.randn(*s, dtype=torch.bfloat16) * 0.4)(S) * 0.02),
        }
    _B_in = _B_setup()

    return {
        "A_x": _A_in["x"],
        "A_wq": _A_in["wq"],
        "A_wk": _A_in["wk"],
        "A_wv": _A_in["wv"],
        "A_wo": _A_in["wo"],
        "B_x": _B_in["x"],
        "B_w1": _B_in["w1"],
        "B_b1": _B_in["b1"],
        "B_w2": _B_in["w2"],
        "B_b2": _B_in["b2"],
    }

def reference(A_x, A_wq, A_wk, A_wv, A_wo, B_x, B_w1, B_b1, B_w2, B_b2):
    def _A_ref(x, wq, wk, wv, wo):
        B, S, D = x.shape
        H = 32
        Dh = D // H
        q = (lambda x, *d: x.permute(*d))((x @ wq).reshape(B, S, H, Dh), 0, 2, 1, 3)
        k = x @ wk
        v = x @ wv
        scores = torch.einsum("bhqd,bkd->bhqk", q, k) * (Dh ** -0.5)
        p = _softmax(scores, axis=-1)
        o = torch.einsum("bhqk,bkd->bhqd", p, v)
        return (lambda x, *d: x.permute(*d))(o, 0, 2, 1, 3).reshape(B, S, H * Dh) @ wo

    def _B_ref(x, w1, b1, w2, b2):
        B, S, D = x.shape
        h = _layernorm(x, axis=-1)
        u = torch.einsum("bsd,sf->bdf", h, w1) + b1
        u = _gelu(u)
        y = torch.einsum("bdf,fs->bds", u, w2) + b2
        return (lambda x, *d: x.permute(*d))(y, 0, 2, 1)

    x = A_x
    x = x + _A_ref(_layernorm(x, axis=-1), A_wq, A_wk, A_wv, A_wo)
    x = x + _B_ref(_layernorm(x, axis=-1), B_w1, B_b1, B_w2, B_b2)
    x = x + _A_ref(_layernorm(x, axis=-1), A_wq, A_wk, A_wv, A_wo)
    x = x + _B_ref(_layernorm(x, axis=-1), B_w1, B_b1, B_w2, B_b2)
    return x
```

```cpp
#include <hip/hip_runtime.h>
#include <hip/hip_bf16.h>
#include <stdint.h>

typedef __attribute__((ext_vector_type(8))) short bf16x8;
typedef __attribute__((ext_vector_type(4))) short bf16x4;
typedef __attribute__((ext_vector_type(16))) float f32x16;
typedef __attribute__((ext_vector_type(4))) float f32x4;
typedef __attribute__((ext_vector_type(2))) float f32x2;
typedef __attribute__((ext_vector_type(2))) __bf16 bf2_t;
typedef __attribute__((ext_vector_type(4))) unsigned u32x4;
typedef __attribute__((ext_vector_type(2))) unsigned u32x2;
typedef unsigned short u16;
#define LAS __attribute__((address_space(3)))
typedef LAS const char* lds_cptr;
typedef LAS char* lds_ptr;
#define DI __device__ __forceinline__

constexpr int S_ = 2048, D_ = 640, H_ = 32, DH_ = 20, F_ = 1728;
constexpr float LN_EPS = 1e-5f;
constexpr float QSCALE = 0.22360679774997896f * 1.4426950408889634f;

constexpr int KV_TILE = 2624, KV_KT = 1024, KV_V = 1280, KV_VS = 672;
constexpr int KV_BYTES = KV_TILE * (S_ / 32);

DI unsigned pack_bf16(float lo, float hi) {
  f32x2 v = {lo, hi};
  bf2_t b = __builtin_convertvector(v, bf2_t);
  return __builtin_bit_cast(unsigned, b);
}
DI float bf_lo(unsigned u) { return __uint_as_float(u << 16); }
DI float bf_hi(unsigned u) { return __uint_as_float(u & 0xffff0000u); }
DI u16 f2bf(float x) { return (u16)(pack_bf16(x, 0.f) & 0xffffu); }

DI void glds16(const void* g, unsigned lds_base) {
  unsigned sv;
  asm volatile("s_mov_b32 %0, m0\n\ts_mov_b32 m0, %2\n\ts_nop 0\n\tglobal_load_lds_dwordx4 %1, off\n\ts_mov_b32 m0, %0"
               : "=&s"(sv) : "v"(g), "s"(lds_base) : "memory");
}
template <int N> DI void wait_bar() { asm volatile("s_waitcnt vmcnt(%0) lgkmcnt(0)\n\ts_barrier" ::"i"(N) : "memory"); }
DI void bar_lds() { asm volatile("s_waitcnt lgkmcnt(0)\n\ts_barrier" ::: "memory"); }
typedef short v4i16_t __attribute__((ext_vector_type(4)));
DI bf16x4 lds_tr(lds_cptr p) {
  return __builtin_bit_cast(bf16x4, __builtin_amdgcn_ds_read_tr16_b64_v4i16((LAS v4i16_t*)p));
}
DI bf16x8 lds_b128(lds_cptr p) { return *(const LAS bf16x8*)p; }
#define MFMA16(a, b, c) __builtin_amdgcn_mfma_f32_16x16x32_bf16((a), (b), (c), 0, 0, 0)
#define MFMA32(a, b, c) __builtin_amdgcn_mfma_f32_32x32x16_bf16((a), (b), (c), 0, 0, 0)
#define MFMA32K8(a, b, c) __builtin_amdgcn_mfma_f32_32x32x8bf16_1k((a), (b), (c), 0, 0, 0)

template <int WT, int AT, int AMODE, int NST, int KSTEPS, int NSLOT, bool W_IS_A, bool EXTRA>
struct Gemm {
  static constexpr int WROW = WT * 32;
  static constexpr int WBYTES = 128 * WROW;
  static constexpr int ABYTES = AT * 4096;
  static constexpr int XBYTES = EXTRA ? 5120 : 0;
  static constexpr int SLOT = WBYTES + ABYTES + XBYTES;
  static constexpr int NW0 = (WT + 1) / 2;
  static constexpr int NWMAX = NW0 + (EXTRA ? 1 : 0);
  static constexpr int ATX = AT + (EXTRA ? 1 : 0);
  static constexpr int WP = WBYTES / 1024, AP = ABYTES / 1024;
  static_assert((WP == 20 && AP == 16) || (WP == 16 && AP == 20), "piece split");

  f32x4 acc[NWMAX][ATX];
  float st1[2], st2[2];

  DI void run(const char* W, int ldw, const char* A, int lda, const char* X, char* lds_generic) {
    const int tid = threadIdx.x, lane = tid & 63;
    const int wave = __builtin_amdgcn_readfirstlane(tid >> 6);
    const int kq = wave & 3, half = wave >> 2;
    const unsigned lds0 = (unsigned)(uintptr_t)lds_generic;
    const lds_cptr lds = (lds_cptr)lds_generic;
    const bool hasx = EXTRA && (X != nullptr);
    const bool w_loader = (WP == 20) ? (wave < 4) : (wave >= 4);
    const int lw = wave & 3;
    constexpr int NP = 5;
    const char* src[NP];
    unsigned dst0;
    int npieces;
    long sstride;
    if (w_loader) {
      npieces = WP / 4; dst0 = lw * (WP / 4) * 1024; sstride = 128L * ldw;
#pragma unroll
      for (int i = 0; i < NP; ++i) {
        int n = (lw * (WP / 4) + (i < WP / 4 ? i : 0)) * 64 + lane;
        int row, ch;
        if (WT == 4) {
          row = n >> 3; int pos = n & 7;
          int f = ((row >> 1) & 1) | (((row >> 3) & 1) << 1);
          ch = (((pos >> 1) ^ f) << 1) | (pos & 1);
        } else {
          row = n / (2 * WT); ch = n - row * (2 * WT);
        }
        src[i] = W + (long)row * ldw + ch * 16;
      }
    } else {
      npieces = AP / 4; dst0 = WBYTES + lw * (AP / 4) * 1024;
      if (AMODE == 0) {
        sstride = ABYTES;
#pragma unroll
        for (int i = 0; i < NP; ++i) src[i] = A + (lw * (AP / 4) + (i < AP / 4 ? i : 0)) * 1024 + lane * 16;
      } else {
        sstride = 256;
#pragma unroll
        for (int i = 0; i < NP; ++i) {
          int n = (lw * (AP / 4) + (i < AP / 4 ? i : 0)) * 64 + lane;
          int row = n >> 4, pos = n & 15;
          src[i] = A + (long)row * lda + ((pos ^ (row & 15)) * 16);
        }
      }
    }
    const bool x_loader = hasx && wave >= 3;
    const char* xsrc = hasx ? X + (wave - 3) * 1024 + lane * 16 : W;
    const int cnt = npieces + (x_loader ? 1 : 0);

    auto issue = [&](int st) {
      const unsigned sl = lds0 + (st % NSLOT) * SLOT;
      const bool partial = (KSTEPS % 4 != 0) && (st == NST - 1);
#pragma unroll
      for (int i = 0; i < NP; ++i) {
        if (i < npieces) {
          const char* p = src[i] + (long)st * sstride;
          if (partial) {
            const int piece = lw * npieces + i;
            if (piece * 2 >= npieces * 4) p -= sstride / 2;
          }
          glds16(p, __builtin_amdgcn_readfirstlane(sl + dst0 + i * 1024));
        }
      }
      if (EXTRA) { if (x_loader) glds16(xsrc + (long)st * 5120, __builtin_amdgcn_readfirstlane(sl + WBYTES + ABYTES + (wave - 3) * 1024)); }
    };
    const int g = lane >> 4, q = (lane & 15) >> 2, p = lane & 3, c16 = lane & 15;
    const int wt0 = half ? NW0 : 0;
    const int nw = half ? (WT - NW0) : NW0;
    unsigned woff[NW0];
#pragma unroll
    for (int i = 0; i < NW0; ++i) {
      int c = wt0 + i; if (c >= WT) c = WT - 1;
      const int row = 32 * kq + 8 * g + q;
      if (WT == 4) { int f = ((q >> 1) & 1) | ((g & 1) << 1); woff[i] = row * WROW + ((c ^ f) * 32) + 8 * p; }
      else woff[i] = row * WROW + c * 32 + 8 * p;
    }
    const unsigned xoff = WBYTES + ABYTES + (32 * kq + 8 * g + q) * 40 + half * 32 + 8 * p;
    unsigned aoff;
    if (AMODE == 0) aoff = WBYTES + kq * AT * 1024 + lane * 16;
    else aoff = WBYTES + c16 * 256 + (((4 * kq + g) ^ c16) * 16);
    constexpr int ASTEP = (AMODE == 0) ? 1024 : 4096;

#pragma unroll
    for (int i = 0; i < NWMAX; ++i)
#pragma unroll
      for (int j = 0; j < ATX; ++j) acc[i][j] = f32x4{0.f, 0.f, 0.f, 0.f};
    st1[0] = st1[1] = st2[0] = st2[1] = 0.f;
    bf16x8 ones;
#pragma unroll
    for (int i = 0; i < 8; ++i) ones[i] = (short)0x3f80;

#pragma unroll
    for (int s = 0; s < NSLOT - 1; ++s) if (s < NST) issue(s);

#pragma unroll
    for (int st = 0; st < NST; ++st) {
      constexpr int AHEAD_MAX = NSLOT - 2;
      const int ahead = (NST - 1 - st) < AHEAD_MAX ? (NST - 1 - st) : AHEAD_MAX;
      if (ahead == 0) wait_bar<0>();
      else if (ahead == 1) { if (cnt == 4) wait_bar<4>(); else if (cnt == 5) wait_bar<5>(); else wait_bar<6>(); }
      else { if (cnt == 4) wait_bar<8>(); else if (cnt == 5) wait_bar<10>(); else wait_bar<12>(); }
      if (st + NSLOT - 1 < NST) issue(st + NSLOT - 1);
      const bool active = (st * 4 + 4 <= KSTEPS) || (st * 4 + kq < KSTEPS);
      if (active) {
        const lds_cptr sl = lds + (st % NSLOT) * SLOT;
        bf16x8 af[AT], wf[NWMAX];
#pragma unroll
        for (int i = 0; i < NW0; ++i) {
          bf16x4 lo = lds_tr(sl + woff[i]), hi = lds_tr(sl + woff[i] + 4 * WROW);
          wf[i] = __builtin_shufflevector(lo, hi, 0, 1, 2, 3, 4, 5, 6, 7);
        }
#pragma unroll
        for (int j = 0; j < AT; ++j) af[j] = lds_b128(sl + aoff + j * ASTEP);
        if (EXTRA) {
          bf16x4 lo = lds_tr(sl + xoff), hi = lds_tr(sl + xoff + 160);
          wf[NW0] = __builtin_shufflevector(lo, hi, 0, 1, 2, 3, 4, 5, 6, 7);
        }
#pragma unroll
        for (int i = 0; i < NW0; ++i) {
          if (i < nw) {
#pragma unroll
            for (int j = 0; j < AT; ++j) acc[i][j] = W_IS_A ? MFMA16(wf[i], af[j], acc[i][j]) : MFMA16(af[j], wf[i], acc[i][j]);
            if (EXTRA) acc[i][AT] = MFMA16(wf[i], ones, acc[i][AT]);
          }
        }
        if (EXTRA) {
          if (hasx) {
#pragma unroll
            for (int j = 0; j < AT; ++j) acc[NW0][j] = MFMA16(wf[NW0], af[j], acc[NW0][j]);
            acc[NW0][AT] = MFMA16(wf[NW0], ones, acc[NW0][AT]);
          }
#pragma unroll
          for (int j = 0; j < 2; ++j) {
            const bf16x8 a = half ? af[2 + j] : af[j];
            const u32x4 u = __builtin_bit_cast(u32x4, a);
#pragma unroll
            for (int e = 0; e < 4; ++e) {
              const float lo = bf_lo(u[e]), hi = bf_hi(u[e]);
              st1[j] += lo + hi;
              st2[j] = __builtin_fmaf(lo, lo, __builtin_fmaf(hi, hi, st2[j]));
            }
          }
        }
      }
    }
    bar_lds();
  }
};

DI void map_tok(int bid, int& tb, int& j) { const int x = bid & 7, idx = bid >> 3; tb = x * 4 + (idx >> 3); j = idx & 7; }

typedef Gemm<5, 4, 1, 5, 20, 3, true, true> GemmQKV;
constexpr int QKV_LDS = 3 * GemmQKV::SLOT + 64;
__global__ __launch_bounds__(512, 2) void qkv_kernel(const u16* __restrict__ x, const u16* __restrict__ wq, const u16* __restrict__ wk,
                                                     const u16* __restrict__ wv, u16* __restrict__ Q, char* __restrict__ KV) {
  extern __shared__ __attribute__((aligned(16))) char smem[];
  int tb, j; map_tok(blockIdx.x, tb, j);
  GemmQKV G;
  const char* X = j == 0 ? (const char*)wk : (j == 1 ? (const char*)wv : nullptr);
  G.run((const char*)(wq + j * 80), D_ * 2, (const char*)(x + (long)tb * 64 * D_), D_ * 2, X, smem);
  const int tid = threadIdx.x, lane = tid & 63, wave = __builtin_amdgcn_readfirstlane(tid >> 6);
  const int kq = wave & 3, half = wave >> 2, g = lane >> 4, c16 = lane & 15;
  lds_ptr lds = (lds_ptr)smem;
  constexpr int WSTR = 4 * 5 * 1024;
  (void)WSTR;
  float* stat = (float*)(smem + 112 * 1024);
#pragma unroll
  for (int t = 0; t < 2; ++t) {
    float a = G.st1[t], b = G.st2[t];
    a += __shfl_xor(a, 16); a += __shfl_xor(a, 32);
    b += __shfl_xor(b, 16); b += __shfl_xor(b, 32);
    if (g == 0) { const int tok = (half * 2 + t) * 16 + c16; stat[(kq * 64 + tok) * 2] = a; stat[(kq * 64 + tok) * 2 + 1] = b; }
  }
  float* csum = (float*)(smem + 112 * 1024 + 2048);
  const bool hasx = j < 2;
#pragma unroll
  for (int i = 0; i < 4; ++i) {
    const int wt = (i < 3) ? (half * 3 + i) : (5 + half);
    const bool valid = (i < 3) ? (wt < 5) : hasx;
    if (valid) {
      if (c16 == 0) *(f32x4*)(csum + (kq * 7 + wt) * 16 + 4 * g) = G.acc[i][4];
#pragma unroll
      for (int a = 0; a < 4; ++a) *(LAS f32x4*)(lds + ((kq * 7 + wt) * 4 + a) * 1024 + lane * 16) = G.acc[i][a];
    }
  }
  bar_lds();
  const int ntile = hasx ? 28 : 20;
  for (int t = wave; t < ntile; t += 8) {
    const int wt = t >> 2, a = t & 3;
    f32x4 s = {0.f, 0.f, 0.f, 0.f}, cs = {0.f, 0.f, 0.f, 0.f};
    float s1 = 0.f, s2 = 0.f;
    const int tok = a * 16 + c16;
#pragma unroll
    for (int k = 0; k < 4; ++k) {
      s += *(const LAS f32x4*)(lds + ((k * 7 + wt) * 4 + a) * 1024 + lane * 16);
      cs += *(const f32x4*)(csum + (k * 7 + wt) * 16 + 4 * g);
      s1 += stat[(k * 64 + tok) * 2]; s2 += stat[(k * 64 + tok) * 2 + 1];
    }
    const float mu = s1 * (1.f / D_);
    const float var = fmaxf(s2 * (1.f / D_) - mu * mu, 0.f);
    const float r = rsqrtf(var + LN_EPS);
    f32x4 o;
#pragma unroll
    for (int e = 0; e < 4; ++e) o[e] = (s[e] - mu * cs[e]) * r;
    const int token = tb * 64 + tok;
    if (wt < 5) {
      u32x2 w; w[0] = pack_bf16(o[0] * QSCALE, o[1] * QSCALE); w[1] = pack_bf16(o[2] * QSCALE, o[3] * QSCALE);
      *(u32x2*)(Q + (long)token * D_ + j * 80 + wt * 16 + 4 * g) = w;
    } else {
      const int d0 = (wt - 5) * 16 + 4 * g;
      char* tile = KV + (token >> 5) * KV_TILE;
      const int r32 = token & 31;
      if (d0 < DH_) {
        if (j == 0) {
          u32x2 w; w[0] = pack_bf16(o[0], o[1]); w[1] = pack_bf16(o[2], o[3]);
          if (d0 < 16) *(u32x2*)(tile + ((d0 >> 3) * 32 + r32) * 16 + (d0 & 7) * 2) = w;
          else *(u32x2*)(tile + KV_KT + r32 * 8) = w;
        } else {
          const int s16 = r32 >> 4, kk = r32 & 15, h = (kk >> 2) & 1, jj = 4 * (kk >> 3) + (kk & 3);
#pragma unroll
          for (int e = 0; e < 4; ++e) *(u16*)(tile + KV_V + s16 * KV_VS + (h * 21 + d0 + e) * 16 + jj * 2) = f2bf(o[e]);
        }
      } else if (j == 1 && d0 == DH_) {
        const int s16 = r32 >> 4, kk = r32 & 15, h = (kk >> 2) & 1, jj = 4 * (kk >> 3) + (kk & 3);
        *(u16*)(tile + KV_V + s16 * KV_VS + (h * 21 + 20) * 16 + jj * 2) = (u16)0x3f80;
      }
    }
  }
}

constexpr int ATT_CH = 16;
constexpr int ATT_CHB = ATT_CH * KV_TILE;
constexpr int ATT_LDS = 3 * ATT_CHB;
DI float fmax3(float a, float b, float c) { return __builtin_fmaxf(__builtin_fmaxf(a, b), c); }
DI float rowmax16(const f32x16& s) {
  float a = fmax3(s[0], s[1], s[2]), b = fmax3(s[3], s[4], s[5]);
  a = fmax3(a, s[6], s[7]); b = fmax3(b, s[8], s[9]); a = fmax3(a, s[10], s[11]); b = fmax3(b, s[12], s[13]);
  a = fmax3(a, s[14], s[15]); a = __builtin_fmaxf(a, b);
  return __builtin_fmaxf(a, __shfl_xor(a, 32));
}
DI void pack_p(const f32x16& p, bf16x8& lo, bf16x8& hi) {
  u32x4 a, b;
#pragma unroll
  for (int i = 0; i < 4; ++i) { a[i] = pack_bf16(p[2 * i], p[2 * i + 1]); b[i] = pack_bf16(p[8 + 2 * i], p[9 + 2 * i]); }
  lo = __builtin_bit_cast(bf16x8, a); hi = __builtin_bit_cast(bf16x8, b);
}

__global__ __launch_bounds__(512, 2) void attn_kernel(const u16* __restrict__ Q, const char* __restrict__ KV, u16* __restrict__ O) {
  extern __shared__ __attribute__((aligned(16))) char smem[];
  int tb, j; map_tok(blockIdx.x, tb, j);
  const int tid = threadIdx.x, lane = tid & 63, wave = __builtin_amdgcn_readfirstlane(tid >> 6);
  const int r32 = lane & 31, hi = lane >> 5;
  const int head = j * 4 + (wave & 3), token = tb * 64 + (wave >> 2) * 32 + r32;
  const unsigned lds0 = (unsigned)(uintptr_t)smem;
  const lds_cptr lds = (lds_cptr)smem;
  auto issue = [&](int ch) {
    const char* src = KV + (long)ch * ATT_CHB + wave * 1024 + lane * 16;
    const unsigned dst = lds0 + (ch % 3) * ATT_CHB + wave * 1024;
#pragma unroll
    for (int i = 0; i < 5; ++i) glds16(src + i * 8192, __builtin_amdgcn_readfirstlane(dst + i * 8192));
    if (wave == 0) glds16(src + 5 * 8192, __builtin_amdgcn_readfirstlane(dst + 5 * 8192));
  };
  const u16* qrow = Q + (long)token * D_ + head * DH_;
  bf16x8 q16; bf16x4 q4;
  {
    u32x2 a = *(const u32x2*)(qrow + hi * 8), b = *(const u32x2*)(qrow + hi * 8 + 4);
    u32x2 c = *(const u32x2*)(qrow + 16);
    issue(0);
    asm volatile("" : "+v"(a), "+v"(b), "+v"(c));
    issue(1);
    u32x4 v = {a[0], a[1], b[0], b[1]};
    q16 = __builtin_bit_cast(bf16x8, v);
    u32x2 t = {hi ? 0u : c[0], hi ? 0u : c[1]};
    q4 = __builtin_bit_cast(bf16x4, t);
  }
  const unsigned koff = lane * 16, ktoff = KV_KT + r32 * 8, voff = KV_V + (hi * 21 + (r32 < 20 ? r32 : 20)) * 16;
  f32x16 o = {}, negm = {};
  f32x16 s;
#pragma unroll
  for (int ch = 0; ch < 4; ++ch) {
    if (ch < 3) { if (wave == 0) wait_bar<6>(); else wait_bar<5>(); } else wait_bar<0>();
    if (ch + 2 < 4) issue(ch + 2);
    const lds_cptr cb = lds + (ch % 3) * ATT_CHB;
    if (ch == 0) {
      const bf16x8 k16 = lds_b128(cb + koff);
      const bf16x4 k4 = *(const LAS bf16x4*)(cb + ktoff);
      f32x16 z = {};
      z = MFMA32(k16, q16, z); z = MFMA32K8(k4, q4, z);
      const float m = rowmax16(z);
#pragma unroll
      for (int i = 0; i < 16; ++i) negm[i] = -m;
    }
#pragma unroll 2
    for (int t = 0; t < ATT_CH; ++t) {
      const lds_cptr tp = cb + t * KV_TILE;
      const bf16x8 k16 = lds_b128(tp + koff);
      const bf16x4 k4 = *(const LAS bf16x4*)(tp + ktoff);
      const bf16x8 v0 = lds_b128(tp + voff), v1 = lds_b128(tp + voff + KV_VS);
      s = MFMA32(k16, q16, negm); s = MFMA32K8(k4, q4, s);
#pragma unroll
      for (int i = 0; i < 16; ++i) s[i] = __builtin_amdgcn_exp2f(s[i]);
      bf16x8 p0, p1; pack_p(s, p0, p1);
      o = MFMA32(v0, p0, o); o = MFMA32(v1, p1, o);
    }
  }
  float l = hi ? o[8] : o[12];
  bool bad = !(l < 1.8446744e19f);
#pragma unroll
  for (int i = 0; i < 12; ++i) bad |= !(__builtin_fabsf(o[i]) < 3.0e38f);
  if (__builtin_expect(__any(bad), 0)) {
    float m = -INFINITY;
    o = f32x16{};
    for (int t = 0; t < S_ / 32; ++t) {
      const char* tp = KV + (long)t * KV_TILE;
      const bf16x8 k16 = *(const bf16x8*)(tp + koff);
      const bf16x4 k4 = *(const bf16x4*)(tp + ktoff);
      const bf16x8 v0 = *(const bf16x8*)(tp + voff), v1 = *(const bf16x8*)(tp + voff + KV_VS);
      f32x16 z = {};
      z = MFMA32(k16, q16, z); z = MFMA32K8(k4, q4, z);
      const float mn = __builtin_fmaxf(m, rowmax16(z));
      const float alpha = __builtin_amdgcn_exp2f(m - mn);
      m = mn;
#pragma unroll
      for (int i = 0; i < 16; ++i) { z[i] = __builtin_amdgcn_exp2f(z[i] - m); o[i] *= alpha; }
      bf16x8 p0, p1; pack_p(z, p0, p1);
      o = MFMA32(v0, p0, o); o = MFMA32(v1, p1, o);
    }
    l = hi ? o[8] : o[12];
  }
  const float rl = 1.0f / l;
  char* ob = (char*)O + (long)tb * (20 * 4 * 1024);
  const int trow = (wave >> 2) * 32 + r32;
#pragma unroll
  for (int gq = 0; gq < 3; ++gq) {
    const int d0 = 8 * gq + 4 * hi;
    if (d0 < DH_) {
      const int k = head * DH_ + d0;
      u32x2 w; w[0] = pack_bf16(o[4 * gq] * rl, o[4 * gq + 1] * rl); w[1] = pack_bf16(o[4 * gq + 2] * rl, o[4 * gq + 3] * rl);
      *(u32x2*)(ob + (((k >> 5) * 4 + (trow >> 4)) * 64 + (trow & 15) + 16 * ((k & 31) >> 3)) * 16 + (k & 7) * 2) = w;
    }
  }
}

template <class GM, int WT, int AT> DI void store_partials(GM& G, char* smem) {
  const int tid = threadIdx.x, lane = tid & 63, wave = __builtin_amdgcn_readfirstlane(tid >> 6);
  const int kq = wave & 3, half = wave >> 2;
  lds_ptr lds = (lds_ptr)smem;
#pragma unroll
  for (int i = 0; i < GM::NW0; ++i) {
    const int wt = half * GM::NW0 + i;
    if (wt < WT) {
#pragma unroll
      for (int a = 0; a < AT; ++a) *(LAS f32x4*)(lds + ((kq * WT + wt) * AT + a) * 1024 + lane * 16) = G.acc[i][a];
    }
  }
  bar_lds();
}
template <int NT> DI f32x4 sum_partials(char* smem, int t, int lane) {
  lds_cptr lds = (lds_cptr)smem;
  f32x4 s = *(const LAS f32x4*)(lds + t * 1024 + lane * 16);
#pragma unroll
  for (int k = 1; k < 4; ++k) s += *(const LAS f32x4*)(lds + (k * NT + t) * 1024 + lane * 16);
  return s;
}

typedef Gemm<5, 4, 0, 5, 20, 4, true, false> GemmWo;
constexpr int WO_LDS = 4 * GemmWo::SLOT;
__global__ __launch_bounds__(512, 2) void wo_kernel(const u16* __restrict__ O, const u16* __restrict__ wo, const u16* __restrict__ x,
                                                    u16* __restrict__ x1) {
  extern __shared__ __attribute__((aligned(16))) char smem[];
  int tb, j; map_tok(blockIdx.x, tb, j);
  GemmWo G;
  G.run((const char*)(wo + j * 80), D_ * 2, (const char*)O + (long)tb * (20 * 4 * 1024), 0, nullptr, smem);
  store_partials<GemmWo, 5, 4>(G, smem);
  const int tid = threadIdx.x, lane = tid & 63, wave = __builtin_amdgcn_readfirstlane(tid >> 6);
  const int g = lane >> 4, c16 = lane & 15;
  for (int t = wave; t < 20; t += 8) {
    const int wt = t >> 2, a = t & 3;
    const f32x4 s = sum_partials<20>(smem, t, lane);
    const long off = (long)(tb * 64 + a * 16 + c16) * D_ + j * 80 + wt * 16 + 4 * g;
    const u32x2 r = *(const u32x2*)(x + off);
    u32x2 w;
    w[0] = pack_bf16(bf_lo(r[0]) + s[0], bf_hi(r[0]) + s[1]);
    w[1] = pack_bf16(bf_lo(r[1]) + s[2], bf_hi(r[1]) + s[3]);
    *(u32x2*)(x1 + off) = w;
  }
}

__global__ __launch_bounds__(512) void ln_kernel(const u16* __restrict__ x1, u16* __restrict__ Hd) {
  __shared__ __attribute__((aligned(16))) u16 tile[8][D_ + 8];
  const int tid = threadIdx.x, lane = tid & 63, wave = tid >> 6;
  const int s0 = blockIdx.x * 8;
  const u16* row = x1 + (long)(s0 + wave) * D_;
  float v[10];
#pragma unroll
  for (int i = 0; i < 5; ++i) { const unsigned u = *(const unsigned*)(row + i * 128 + lane * 2); v[2 * i] = bf_lo(u); v[2 * i + 1] = bf_hi(u); }
  float s1 = 0.f;
#pragma unroll
  for (int i = 0; i < 10; ++i) s1 += v[i];
#pragma unroll
  for (int o = 1; o < 64; o <<= 1) s1 += __shfl_xor(s1, o);
  const float mu = s1 * (1.f / D_);
  float s2 = 0.f;
#pragma unroll
  for (int i = 0; i < 10; ++i) { v[i] -= mu; s2 += v[i] * v[i]; }
#pragma unroll
  for (int o = 1; o < 64; o <<= 1) s2 += __shfl_xor(s2, o);
  const float var = s2 * (1.f / D_);
  const float r1 = rsqrtf(var + LN_EPS);
  const float r2 = rsqrtf(var * r1 * r1 + LN_EPS);
  const float r = r1 * r2;
#pragma unroll
  for (int i = 0; i < 5; ++i) *(unsigned*)(&tile[wave][i * 128 + lane * 2]) = pack_bf16(v[2 * i] * r, v[2 * i + 1] * r);
  __syncthreads();
  for (int d = tid; d < D_; d += 512) {
    u32x4 w;
#pragma unroll
    for (int i = 0; i < 4; ++i) w[i] = (unsigned)tile[2 * i][d] | ((unsigned)tile[2 * i + 1][d] << 16);
    const int panel = d / 80, rt = (d - panel * 80) >> 4;
    *(u32x4*)((char*)Hd + ((((long)panel * 64 + (s0 >> 5)) * 5 + rt) * 64 + (d & 15) + 16 * ((s0 & 31) >> 3)) * 16) = w;
  }
}

typedef Gemm<4, 5, 0, 16, 64, 4, true, false> Gemm1;
constexpr int G1_LDS = 4 * Gemm1::SLOT;
constexpr int U_KS = 56;
DI float gelu_tanh(float u) {
  const float z = 0.7978845608028654f * (u + 0.044715f * u * u * u);
  const float e = __builtin_amdgcn_exp2f(-2.885390081777927f * z);
  return u * __builtin_amdgcn_rcpf(1.f + e);
}
__global__ __launch_bounds__(512, 2) void gemm1_kernel(const u16* __restrict__ Hd, const u16* __restrict__ w1, const u16* __restrict__ b1,
                                                       u16* __restrict__ U) {
  extern __shared__ __attribute__((aligned(16))) char smem[];
  const int x = blockIdx.x & 7, idx = blockIdx.x >> 3;
  const int panel = (x & 1) * 4 + (idx & 3), ft = (x >> 1) * 7 + (idx >> 2);
  if (ft >= 27) return;
  Gemm1 G;
  G.run((const char*)(w1 + ft * 64), F_ * 2, (const char*)Hd + (long)panel * (64 * 5 * 1024), 0, nullptr, smem);
  store_partials<Gemm1, 4, 5>(G, smem);
  const int tid = threadIdx.x, lane = tid & 63, wave = __builtin_amdgcn_readfirstlane(tid >> 6);
  const int g = lane >> 4, c16 = lane & 15;
  for (int t = wave; t < 20; t += 8) {
    const int wt = t / 5, a = t - wt * 5;
    const f32x4 s = sum_partials<20>(smem, t, lane);
    const int f = ft * 64 + wt * 16 + 4 * g;
    const u32x2 b = *(const u32x2*)(b1 + f);
    u32x2 w;
    w[0] = pack_bf16(gelu_tanh(s[0] + bf_lo(b[0])), gelu_tanh(s[1] + bf_hi(b[0])));
    w[1] = pack_bf16(gelu_tanh(s[2] + bf_lo(b[1])), gelu_tanh(s[3] + bf_hi(b[1])));
    *(u32x2*)((char*)U + ((((long)panel * U_KS + (f >> 5)) * 5 + a) * 64 + c16 + 16 * ((f & 31) >> 3)) * 16 + (f & 7) * 2) = w;
  }
}

typedef Gemm<4, 5, 0, 14, 54, 4, false, false> Gemm2;
constexpr int G2_LDS = 4 * Gemm2::SLOT;
__global__ __launch_bounds__(512, 2) void gemm2_kernel(const u16* __restrict__ U, const u16* __restrict__ w2, const u16* __restrict__ b2,
                                                       const u16* __restrict__ x1, u16* __restrict__ x2) {
  extern __shared__ __attribute__((aligned(16))) char smem[];
  const int x = blockIdx.x & 7, idx = blockIdx.x >> 3;
  const int panel = (x & 1) * 4 + (idx & 3), stile = (x >> 1) * 8 + (idx >> 2);
  Gemm2 G;
  G.run((const char*)(w2 + stile * 64), S_ * 2, (const char*)U + (long)panel * (U_KS * 5 * 1024), 0, nullptr, smem);
  store_partials<Gemm2, 4, 5>(G, smem);
  const int tid = threadIdx.x, lane = tid & 63, wave = __builtin_amdgcn_readfirstlane(tid >> 6);
  const int g = lane >> 4, c16 = lane & 15;
  for (int t = wave; t < 20; t += 8) {
    const int wt = t / 5, a = t - wt * 5;
    const f32x4 s = sum_partials<20>(smem, t, lane);
    const int tok = stile * 64 + wt * 16 + c16;
    const float b = __uint_as_float((unsigned)b2[tok] << 16);
    const long off = (long)tok * D_ + panel * 80 + a * 16 + 4 * g;
    const u32x2 r = *(const u32x2*)(x1 + off);
    u32x2 w;
    w[0] = pack_bf16(bf_lo(r[0]) + (s[0] + b), bf_hi(r[0]) + (s[1] + b));
    w[1] = pack_bf16(bf_lo(r[1]) + (s[2] + b), bf_hi(r[1]) + (s[3] + b));
    *(u32x2*)(x2 + off) = w;
  }
}

extern "C" void kernel_launch(void* const* d_in, const int* in_sizes, int n_in, void* d_out, int out_size, void* d_ws,
                              size_t ws_size, hipStream_t stream) {
  const u16* x0 = (const u16*)d_in[0];
  const u16* wq = (const u16*)d_in[1];
  const u16* wk = (const u16*)d_in[2];
  const u16* wv = (const u16*)d_in[3];
  const u16* wo = (const u16*)d_in[4];
  const u16* w1 = (const u16*)d_in[6];
  const u16* b1 = (const u16*)d_in[7];
  const u16* w2 = (const u16*)d_in[8];
  const u16* b2 = (const u16*)d_in[9];
  char* ws = (char*)d_ws;
  constexpr size_t XB = (size_t)S_ * D_ * 2;
  u16* Q = (u16*)(ws);
  u16* O = (u16*)(ws + XB);
  u16* X1 = (u16*)(ws + 2 * XB);
  u16* X2 = (u16*)(ws + 3 * XB);
  u16* Hd = (u16*)(ws + 4 * XB);
  u16* U = (u16*)(ws + 5 * XB);
  char* KV = ws + 6 * XB;
  hipFuncSetAttribute((const void*)qkv_kernel, hipFuncAttributeMaxDynamicSharedMemorySize, QKV_LDS);
  hipFuncSetAttribute((const void*)attn_kernel, hipFuncAttributeMaxDynamicSharedMemorySize, ATT_LDS);
  hipFuncSetAttribute((const void*)wo_kernel, hipFuncAttributeMaxDynamicSharedMemorySize, WO_LDS);
  hipFuncSetAttribute((const void*)gemm1_kernel, hipFuncAttributeMaxDynamicSharedMemorySize, G1_LDS);
  hipFuncSetAttribute((const void*)gemm2_kernel, hipFuncAttributeMaxDynamicSharedMemorySize, G2_LDS);
  const u16* xin = x0;
  for (int layer = 0; layer < 2; ++layer) {
    u16* xout = layer == 0 ? X2 : (u16*)d_out;
    qkv_kernel<<<256, 512, QKV_LDS, stream>>>(xin, wq, wk, wv, Q, KV);
    attn_kernel<<<256, 512, ATT_LDS, stream>>>(Q, KV, O);
    wo_kernel<<<256, 512, WO_LDS, stream>>>(O, wo, xin, X1);
    ln_kernel<<<256, 512, 0, stream>>>(X1, Hd);
    gemm1_kernel<<<224, 512, G1_LDS, stream>>>(Hd, w1, b1, U);
    gemm2_kernel<<<256, 512, G2_LDS, stream>>>(U, w2, b2, X1, xout);
    xin = X2;
  }
}
```

```cpp
#include <hip/hip_runtime.h>
#include <hip/hip_bf16.h>
#include <stdint.h>

typedef __attribute__((ext_vector_type(8))) short bf16x8;
typedef __attribute__((ext_vector_type(4))) short bf16x4;
typedef __attribute__((ext_vector_type(16))) float f32x16;
typedef __attribute__((ext_vector_type(4))) float f32x4;
typedef __attribute__((ext_vector_type(2))) float f32x2;
typedef __attribute__((ext_vector_type(2))) __bf16 bf2_t;
typedef __attribute__((ext_vector_type(4))) unsigned u32x4;
typedef __attribute__((ext_vector_type(2))) unsigned u32x2;
typedef unsigned short u16;
#define LAS __attribute__((address_space(3)))
#define GAS __attribute__((address_space(1)))
typedef LAS const char* lds_cptr;
typedef LAS char* lds_ptr;
typedef GAS unsigned gu32;
typedef GAS unsigned long long gu64;
#define DI __device__ __forceinline__
#define RLX_AGENT __ATOMIC_RELAXED, __HIP_MEMORY_SCOPE_AGENT

constexpr int S_ = 2048, D_ = 640, DH_ = 20, F_ = 1728;
constexpr float LN_EPS = 1e-5f;
constexpr float QSCALE = 0.22360679774997896f * 1.4426950408889634f;

constexpr int KV_TILE = 2624, KV_KT = 1024, KV_V = 1280, KV_VS = 672;

constexpr unsigned WS_EPOCH = 0;
constexpr unsigned WS_FLAGS = 4096, FL_LAYER = 32768;
constexpr unsigned FL_KV = 0;
constexpr unsigned FL_O = 8192;
constexpr unsigned FL_X = 12288;
constexpr unsigned FL_H = 16384;
constexpr unsigned FL_U = 20480;
constexpr unsigned WS_DIAG = 98304;
constexpr unsigned WS_STAT = 262144, ST_LAYER = 524288;
constexpr unsigned WS_KV = WS_STAT + 2 * ST_LAYER, KV_LAYER = 196608;
constexpr unsigned XB = S_ * D_ * 2;
constexpr unsigned WS_O = WS_KV + 2 * KV_LAYER;
constexpr unsigned WS_H = WS_O + 2 * XB;
constexpr int U_KS = 56;
constexpr unsigned UB = 8 * U_KS * 5 * 1024;
constexpr unsigned WS_U = WS_H + 2 * XB;
constexpr unsigned WS_X = WS_U + 2 * UB;
constexpr unsigned WS_END = WS_X + XB;

constexpr int L_W = 0;
constexpr int L_WT = 81920;
constexpr int L_RING = 81920;
constexpr int L_ATT = 102400;
constexpr int L_QT = 114688;
constexpr int L_STG = 135168;
constexpr int L_RES = 151552;
constexpr int LDS_BYTES = 163840;
constexpr unsigned SPIN_CAP = 1u << 21;

DI unsigned pack_bf16(float lo, float hi) {
  f32x2 v = {lo, hi};
  bf2_t b = __builtin_convertvector(v, bf2_t);
  return __builtin_bit_cast(unsigned, b);
}
DI float bf_lo(unsigned u) { return __uint_as_float(u << 16); }
DI float bf_hi(unsigned u) { return __uint_as_float(u & 0xffff0000u); }

#define GLDS_HEAD "s_mov_b32 m0, %1\n\ts_nop 4\n\t"
template <bool DEV> DI void glds1(const void* sbase, unsigned m0v, unsigned v0) {
  if (DEV) asm volatile(GLDS_HEAD "global_load_lds_dwordx4 %2, %0 sc1" ::"s"(sbase), "s"(m0v), "v"(v0) : "memory");
  else asm volatile(GLDS_HEAD "global_load_lds_dwordx4 %2, %0" ::"s"(sbase), "s"(m0v), "v"(v0) : "memory");
}
template <bool DEV> DI void glds4(const void* sbase, unsigned m0v, unsigned v0, unsigned v1, unsigned v2, unsigned v3) {
  if (DEV)
    asm volatile(GLDS_HEAD "global_load_lds_dwordx4 %2, %0 sc1\n\tglobal_load_lds_dwordx4 %3, %0 offset:1024 sc1\n\t"
                           "global_load_lds_dwordx4 %4, %0 offset:2048 sc1\n\tglobal_load_lds_dwordx4 %5, %0 offset:3072 sc1"
                 ::"s"(sbase), "s"(m0v), "v"(v0), "v"(v1), "v"(v2), "v"(v3) : "memory");
  else
    asm volatile(GLDS_HEAD "global_load_lds_dwordx4 %2, %0\n\tglobal_load_lds_dwordx4 %3, %0 offset:1024\n\t"
                           "global_load_lds_dwordx4 %4, %0 offset:2048\n\tglobal_load_lds_dwordx4 %5, %0 offset:3072"
                 ::"s"(sbase), "s"(m0v), "v"(v0), "v"(v1), "v"(v2), "v"(v3) : "memory");
}
template <int N, bool DEV> DI void glds_lin(const void* sbase, unsigned m0v, unsigned v) {
  if (N == 1) glds1<DEV>(sbase, m0v, v);
  else if (N == 4) glds4<DEV>(sbase, m0v, v, v, v, v);
  else if (N == 2) {
    if (DEV) asm volatile(GLDS_HEAD "global_load_lds_dwordx4 %2, %0 sc1\n\tglobal_load_lds_dwordx4 %2, %0 offset:1024 sc1" ::"s"(sbase), "s"(m0v), "v"(v) : "memory");
    else asm volatile(GLDS_HEAD "global_load_lds_dwordx4 %2, %0\n\tglobal_load_lds_dwordx4 %2, %0 offset:1024" ::"s"(sbase), "s"(m0v), "v"(v) : "memory");
  } else {
    if (DEV) asm volatile(GLDS_HEAD "global_load_lds_dwordx4 %2, %0 sc1\n\tglobal_load_lds_dwordx4 %2, %0 offset:1024 sc1\n\tglobal_load_lds_dwordx4 %2, %0 offset:2048 sc1" ::"s"(sbase), "s"(m0v), "v"(v) : "memory");
    else asm volatile(GLDS_HEAD "global_load_lds_dwordx4 %2, %0\n\tglobal_load_lds_dwordx4 %2, %0 offset:1024\n\tglobal_load_lds_dwordx4 %2, %0 offset:2048" ::"s"(sbase), "s"(m0v), "v"(v) : "memory");
  }
}
DI void glds2(const void* sbase, unsigned m0v, unsigned v0, unsigned v1) {
  asm volatile(GLDS_HEAD "global_load_lds_dwordx4 %2, %0\n\tglobal_load_lds_dwordx4 %3, %0 offset:1024" ::"s"(sbase), "s"(m0v), "v"(v0), "v"(v1) : "memory");
}
#define UNI(x) __builtin_amdgcn_readfirstlane(x)
template <int N> DI void wait_bar() { asm volatile("s_waitcnt vmcnt(%0) lgkmcnt(0)\n\ts_barrier" ::"i"(N) : "memory"); }
template <int N> DI void wait_vbar() { asm volatile("s_waitcnt vmcnt(%0)\n\ts_barrier" ::"i"(N) : "memory"); }
DI void bar_lds() { asm volatile("s_waitcnt lgkmcnt(0)\n\ts_barrier" ::: "memory"); }
typedef short v4i16_t __attribute__((ext_vector_type(4)));
DI bf16x4 lds_tr(lds_cptr p) {
  return __builtin_bit_cast(bf16x4, __builtin_amdgcn_ds_read_tr16_b64_v4i16((LAS v4i16_t*)p));
}
DI bf16x8 lds_tr8(lds_cptr p, int step) {
  const bf16x4 lo = lds_tr(p), hi = lds_tr(p + step);
  return __builtin_shufflevector(lo, hi, 0, 1, 2, 3, 4, 5, 6, 7);
}
DI bf16x8 lds_b128(lds_cptr p) { return *(const LAS bf16x8*)p; }
#define MFMA16(a, b, c) __builtin_amdgcn_mfma_f32_16x16x32_bf16((a), (b), (c), 0, 0, 0)
#define MFMA32(a, b, c) __builtin_amdgcn_mfma_f32_32x32x16_bf16((a), (b), (c), 0, 0, 0)
#define MFMA32K8(a, b, c) __builtin_amdgcn_mfma_f32_32x32x8bf16_1k((a), (b), (c), 0, 0, 0)
#define SBAR() __builtin_amdgcn_sched_barrier(0)
#define PIN(x) asm volatile("" : "+v"(x))
typedef __amdgpu_buffer_rsrc_t rsrc_t;
DI void st8_dev(rsrc_t r, unsigned off, u32x2 v) { __builtin_amdgcn_raw_buffer_store_b64(v, r, off, 0, 16); }
DI u32x4 ld16_dev(rsrc_t r, unsigned off) { return __builtin_amdgcn_raw_buffer_load_b128(r, off, 0, 16); }
DI u32x4 ld16_dev_s(rsrc_t r, unsigned off, unsigned soff) { return __builtin_amdgcn_raw_buffer_load_b128(r, off, soff, 16); }

DI void poll_flags(const char* ws, unsigned off, int n, unsigned tag, int lane, bool& dead) {
  gu32* f = (gu32*)(ws + off);
  unsigned spins = 0;
  while (!dead) {
    unsigned v = tag;
    if (lane < n) v = __hip_atomic_load(f + lane, RLX_AGENT);
    if (__all(v == tag)) break;
    if (++spins > SPIN_CAP) dead = true;
    __builtin_amdgcn_s_sleep(1);
  }
  asm volatile("" ::: "memory");
}
DI void publish(const char* ws, unsigned off, int nrep, unsigned rstride, unsigned tag, int wave, int lane) {
  wait_bar<0>();
  if (wave == 0 && lane < nrep) __hip_atomic_store((gu32*)(ws + off + lane * rstride), tag, RLX_AGENT);
}

DI float fmax3(float a, float b, float c) { return __builtin_fmaxf(__builtin_fmaxf(a, b), c); }
DI float rowmax16(const f32x16& s) {
  float a = fmax3(s[0], s[1], s[2]), b = fmax3(s[3], s[4], s[5]);
  a = fmax3(a, s[6], s[7]); b = fmax3(b, s[8], s[9]); a = fmax3(a, s[10], s[11]); b = fmax3(b, s[12], s[13]);
  a = fmax3(a, s[14], s[15]); a = __builtin_fmaxf(a, b);
  return __builtin_fmaxf(a, __shfl_xor(a, 32));
}
DI void pack_p(const f32x16& p, bf16x8& lo, bf16x8& hi) {
  u32x4 a, b;
#pragma unroll
  for (int i = 0; i < 4; ++i) { a[i] = pack_bf16(p[2 * i], p[2 * i + 1]); b[i] = pack_bf16(p[8 + 2 * i], p[9 + 2 * i]); }
  lo = __builtin_bit_cast(bf16x8, a); hi = __builtin_bit_cast(bf16x8, b);
}
DI float gelu_tanh(float u) {
  const float z = 0.7978845608028654f * (u + 0.044715f * u * u * u);
  const float e = __builtin_amdgcn_exp2f(-2.885390081777927f * z);
  return u * __builtin_amdgcn_rcpf(1.f + e);
}

DI void wslice_block(const u16* w, unsigned lds0, int lw, int lane, int blk) {
  const char* src = (const char*)w + lw * (160 * 1280);
  if (blk < 5) {
    const int r = lane >> 3, pos = lane & 7;
    const unsigned pe = (((pos >> 1) ^ ((r >> 1) & 1)) << 1 | (pos & 1)) * 16;
    const unsigned v0 = r * 1280 + pe, v1 = r * 1280 + (pe ^ 64) + 9216;
    glds4<false>(src + blk * (32 * 1280), lds0 + L_W + lw * 20480 + blk * 4096, v0, v1, v0 + 18432, v1 + 18432);
  } else {
    const unsigned t0 = (lane >> 1) * 1280 + 128 + (lane & 1) * 16;
    if (blk == 5) glds4<false>(src, lds0 + L_WT + lw * 5120, t0, t0 + 39936, t0 + 2 * 39936, t0 + 3 * 39936);
    else glds1<false>(src + 128 * 1280, lds0 + L_WT + lw * 5120 + 4096, t0);
  }
}

struct SlabLane { unsigned v0, v1, vstep; };
DI SlabLane slab_lane(int lane, unsigned ld) {
  const int r = lane >> 3, pos = lane & 7;
  const unsigned pe = (((pos >> 1) ^ ((r >> 1) & 1)) << 1 | (pos & 1)) * 16;
  return SlabLane{r * ld + pe + 4096, (r + 8) * ld + (pe ^ 64) + 3072, 16 * ld - 2048};
}
DI void slab_load(const char* rows, unsigned dst, const SlabLane& L) {
  glds4<false>(rows - 4096, dst, L.v0, L.v1, L.v0 + L.vstep, L.v1 + L.vstep);
}
DI void touch(const void* sbase, unsigned voff, unsigned& sink) {
  asm volatile("s_nop 4\n\tglobal_load_dword %0, %1, %2" : "+v"(sink) : "v"(voff), "s"(sbase) : "memory");
}

DI f32x4 sum4(lds_cptr lds, int t, int nt, int lane) {
  f32x4 s = *(const LAS f32x4*)(lds + t * 1024 + lane * 16);
#pragma unroll
  for (int k = 1; k < 4; ++k) s += *(const LAS f32x4*)(lds + (k * nt + t) * 1024 + lane * 16);
  return s;
}
DI void ln_finish(float s1, float s2, float& mu, float& var) { mu = s1 * (1.f / D_); var = fmaxf(s2 * (1.f / D_) - mu * mu, 0.f); }

constexpr int ATT_CH = 4;
constexpr int ATT_CHB = ATT_CH * KV_TILE;
constexpr int ATT_SLOT = 10752;
constexpr int ATT_NCH = S_ / 32 / ATT_CH;
constexpr int w2_slot(int st) { return (st % 9) * 16384; }
template <int N> struct IC { static constexpr int v = N; };
template <int I, int N, class F> DI void static_for(F&& f) { if constexpr (I < N) { f(IC<I>{}); static_for<I + 1, N>(f); } }

#define STAMP(i) do { } while (0)

__global__ __launch_bounds__(512, 2) void mega_kernel(const u16* __restrict__ x0, const u16* __restrict__ wq, const u16* __restrict__ wk,
                                                      const u16* __restrict__ wv, const u16* __restrict__ wo, const u16* __restrict__ w1,
                                                      const u16* __restrict__ b1, const u16* __restrict__ w2, const u16* __restrict__ b2,
                                                      u16* __restrict__ out, char* ws) {
  extern __shared__ __attribute__((aligned(16))) char smem[];
  const int tid = threadIdx.x;
  const unsigned lds0 = (unsigned)(uintptr_t)smem;
  const lds_cptr lds = (lds_cptr)smem;
  const lds_ptr ldw = (lds_ptr)smem;
#define FRESH() \
  int lane = tid & 63; asm volatile("" : "+v"(lane)); \
  int wave = UNI(tid >> 6); asm volatile("" : "+s"(wave)); \
  int bid = blockIdx.x; asm volatile("" : "+s"(bid)); \
  const int xcd = bid & 7, idx = bid >> 3, tb = xcd * 4 + (idx >> 3), j = idx & 7; \
  const int g = lane >> 4, c16 = lane & 15, kq = wave & 3, th = wave >> 2; \
  const int ea = wave & 3, ew0 = th ? 3 : 0, ewn = th ? 2 : 3; \
  const unsigned res_off = L_RES + (ea * 16 + c16) * 160 + 8 * g; \
  const int panel4 = (xcd & 1) * 4 + (idx & 3), ft = (xcd >> 1) * 7 + (idx >> 2); \
  const bool has4 = idx < 28 && ft < 27; \
  (void)xcd; (void)g; (void)c16; (void)kq; (void)th; (void)ea; (void)ew0; (void)ewn; (void)res_off; (void)tb; (void)j; (void)panel4; (void)has4
  const rsrc_t rws = __builtin_amdgcn_make_buffer_rsrc(ws, 0, WS_END, 0x00020000);
  unsigned tag = 0;
  bool dead = false;

#pragma unroll 1
  for (int layer = 0; layer < 2; ++layer) {
    const unsigned fl = WS_FLAGS + layer * FL_LAYER;
    const unsigned kvoff = WS_KV + layer * KV_LAYER;
    {
      FRESH();
      STAMP(0);
      if (wave >= 4) {
#pragma unroll 1
        for (int blk = 0; blk < 7; ++blk) wslice_block(wq + j * 80, lds0, wave - 4, lane, blk);
      } else {
        if (j < 2) {
          const char* xs = (const char*)(j == 0 ? wk : wv);
          if (wave < 3) {
            glds_lin<4, false>(xs + wave * 8192, lds0 + L_ATT + wave * 8192, lane * 16);
            glds_lin<4, false>(xs + wave * 8192 + 4096, lds0 + L_ATT + wave * 8192 + 4096, lane * 16);
          } else glds1<false>(xs + 24576, lds0 + L_ATT + 24576, lane * 16);
        }
        if (layer == 0) {
#pragma unroll 1
          for (int pc = wave; pc < 10; pc += 4) {
            const int n = pc * 64 + lane, row = n / 10, ch = n - row * 10;
            glds1<false>(x0 + (long)tb * 64 * D_ + j * 80, lds0 + L_RES + pc * 1024, row * 1280 + ch * 16);
          }
        }
      }
      rsrc_t ra = rws; unsigned abase, astr_t, astr_k;
      if (layer == 0) {
        ra = __builtin_amdgcn_make_buffer_rsrc((void*)x0, 0, XB, 0x00020000);
        abase = (tb * 64 + th * 32 + c16) * (D_ * 2) + kq * 64 + g * 16; astr_t = 16 * D_ * 2; astr_k = 256;
      } else {
        if (wave == 0) poll_flags(ws, WS_FLAGS + FL_X + tb * 128, 8, tag, lane, dead);
        asm volatile("s_barrier" ::: "memory");
        abase = WS_X + tb * 81920 + (kq * 4 + th * 2) * 1024 + lane * 16; astr_t = 1024; astr_k = 16384;
      }
      STAMP(1);
      u32x4 af[5][2];
#pragma unroll
      for (int s = 0; s < 5; ++s)
#pragma unroll
        for (int t = 0; t < 2; ++t) af[s][t] = ld16_dev_s(ra, abase, s * astr_k + t * astr_t);
      unsigned epoch = tag - 1u;
      if (layer == 0) epoch = __hip_atomic_load((gu32*)(ws + WS_EPOCH), RLX_AGENT);
      wait_bar<0>();
      tag = UNI(epoch) + 1u;
      bf16x8 ones;
#pragma unroll
      for (int i = 0; i < 8; ++i) ones[i] = (short)0x3f80;
      f32x4 acc[5][2], akv[2][2], aon[3], aonk;
#pragma unroll
      for (int i = 0; i < 5; ++i) { acc[i][0] = f32x4{0.f, 0.f, 0.f, 0.f}; acc[i][1] = acc[i][0]; }
#pragma unroll
      for (int i = 0; i < 2; ++i) { akv[i][0] = acc[0][0]; akv[i][1] = acc[0][0]; }
      aon[0] = aon[1] = aon[2] = aonk = acc[0][0];
      float st1[2] = {0.f, 0.f}, st2[2] = {0.f, 0.f};
      const int q = c16 >> 2, p = lane & 3;
      const int row = 32 * kq + 8 * g + q;
      const unsigned fsw = (((q >> 1) & 1) | ((g & 1) << 1)) * 32;
      const unsigned woff = L_W + row * 128 + 8 * p, toff = L_WT + row * 32 + 8 * p, xoff = L_ATT + row * 40 + 8 * p;
#pragma unroll
      for (int s = 0; s < 5; ++s) {
        const bf16x8 a0 = __builtin_bit_cast(bf16x8, af[s][0]), a1 = __builtin_bit_cast(bf16x8, af[s][1]);
        bf16x8 wf[5];
#pragma unroll
        for (int i = 0; i < 4; ++i) wf[i] = lds_tr8(lds + woff + s * 16384 + ((i * 32) ^ fsw), 512);
        wf[4] = lds_tr8(lds + toff + s * 4096, 128);
#pragma unroll
        for (int i = 0; i < 5; ++i) { acc[i][0] = MFMA16(wf[i], a0, acc[i][0]); acc[i][1] = MFMA16(wf[i], a1, acc[i][1]); }
        if (th == 0) { aon[0] = MFMA16(wf[0], ones, aon[0]); aon[1] = MFMA16(wf[1], ones, aon[1]); aon[2] = MFMA16(wf[2], ones, aon[2]); }
        else { aon[0] = MFMA16(wf[3], ones, aon[0]); aon[1] = MFMA16(wf[4], ones, aon[1]); }
        if (j < 2) {
          const bf16x8 x0f = lds_tr8(lds + xoff + s * 5120, 160), x1f = lds_tr8(lds + xoff + s * 5120 + 32, 160);
          const bf16x8 xs = th ? x1f : x0f;
          if (j == 0) {
            akv[0][0] = MFMA16(x0f, a0, akv[0][0]); akv[0][1] = MFMA16(x0f, a1, akv[0][1]);
            akv[1][0] = MFMA16(x1f, a0, akv[1][0]); akv[1][1] = MFMA16(x1f, a1, akv[1][1]);
            aonk = MFMA16(xs, ones, aonk);
          } else {
            akv[0][0] = MFMA16(a0, x0f, akv[0][0]); akv[0][1] = MFMA16(a1, x0f, akv[0][1]);
            akv[1][0] = MFMA16(a0, x1f, akv[1][0]); akv[1][1] = MFMA16(a1, x1f, akv[1][1]);
            aonk = MFMA16(ones, xs, aonk);
          }
        }
#pragma unroll
        for (int t = 0; t < 2; ++t)
#pragma unroll
          for (int e = 0; e < 4; ++e) {
            const float lo = bf_lo(af[s][t][e]), hi = bf_hi(af[s][t][e]);
            st1[t] += lo + hi;
            st2[t] = __builtin_fmaf(lo, lo, __builtin_fmaf(hi, hi, st2[t]));
          }
      }
      bar_lds();
      LAS float* stat = (LAS float*)(ldw + L_STG);
      LAS float* csum = (LAS float*)(ldw + L_STG + 2048);
#pragma unroll
      for (int t = 0; t < 2; ++t) {
        float a = st1[t], b = st2[t];
        a += __shfl_xor(a, 16); a += __shfl_xor(a, 32);
        b += __shfl_xor(b, 16); b += __shfl_xor(b, 32);
        if (g == 0) *(LAS f32x2*)(stat + (kq * 64 + th * 32 + t * 16 + c16) * 2) = f32x2{a, b};
      }
      if (j < 2) {
#pragma unroll
        for (int i = 0; i < 2; ++i)
#pragma unroll
          for (int t = 0; t < 2; ++t) *(LAS f32x4*)(ldw + ((kq * 7 + 5 + i) * 4 + th * 2 + t) * 1024 + lane * 16) = akv[i][t];
        if (j == 0) { if (c16 == 0) *(LAS f32x4*)(csum + (kq * 7 + 5 + th) * 16 + 4 * g) = aonk; }
        else { if (g == 0) csum[(kq * 7 + 5 + th) * 16 + c16] = aonk[0]; }
      }
#pragma unroll
      for (int i = 0; i < 5; ++i)
#pragma unroll
        for (int t = 0; t < 2; ++t) *(LAS f32x4*)(ldw + ((kq * 7 + i) * 4 + th * 2 + t) * 1024 + lane * 16) = acc[i][t];
      if (c16 == 0) {
#pragma unroll
        for (int i = 0; i < 3; ++i) if (i < ewn) *(LAS f32x4*)(csum + (kq * 7 + ew0 + i) * 16 + 4 * g) = aon[i];
      }
      bar_lds();
      float s1 = 0.f, s2 = 0.f;
#pragma unroll
      for (int k = 0; k < 4; ++k) { const f32x2 v = *(const LAS f32x2*)(stat + (k * 64 + ea * 16 + c16) * 2); s1 += v[0]; s2 += v[1]; }
      float mu, var; ln_finish(s1, s2, mu, var);
      const float rs = rsqrtf(var + LN_EPS);
      if (j < 2) {
        const int wt = 5 + th;
        const f32x4 s = sum4(lds, wt * 4 + ea, 28, lane);
        const unsigned tile = kvoff + (tb * 2 + (ea >> 1)) * KV_TILE;
        if (j == 0) {
          f32x4 cs = {0.f, 0.f, 0.f, 0.f};
#pragma unroll
          for (int k = 0; k < 4; ++k) cs += *(const LAS f32x4*)(csum + (k * 7 + wt) * 16 + 4 * g);
          u32x2 w; w[0] = pack_bf16((s[0] - mu * cs[0]) * rs, (s[1] - mu * cs[1]) * rs); w[1] = pack_bf16((s[2] - mu * cs[2]) * rs, (s[3] - mu * cs[3]) * rs);
          const int r32 = (ea & 1) * 16 + c16;
          if (th == 0) st8_dev(rws, tile + ((g >> 1) * 32 + r32) * 16 + (g & 1) * 8, w);
          else if (g == 0) st8_dev(rws, tile + KV_KT + r32 * 8, w);
        } else {
          float cs = 0.f;
#pragma unroll
          for (int k = 0; k < 4; ++k) cs += csum[(k * 7 + wt) * 16 + c16];
          float o[4];
#pragma unroll
          for (int e = 0; e < 4; ++e) o[e] = (s[e] - __shfl(mu, 4 * g + e) * cs) * __shfl(rs, 4 * g + e);
          const int d = th * 16 + c16;
          u32x2 w; w[0] = pack_bf16(o[0], o[1]); w[1] = pack_bf16(o[2], o[3]);
          if (d == DH_) { w[0] = 0x3f803f80u; w[1] = 0x3f803f80u; }
          if (d <= DH_) st8_dev(rws, tile + KV_V + (ea & 1) * KV_VS + ((g & 1) * 21 + d) * 16 + (g >> 1) * 8, w);
        }
        publish(ws, fl + FL_KV + (tb * 2 + j) * 4, 32, 256, tag, wave, lane);
      }
      const float r = rs * QSCALE;
#pragma unroll
      for (int i = 0; i < 3; ++i) {
        if (i < ewn) {
          const int wt = ew0 + i;
          const f32x4 s = sum4(lds, wt * 4 + ea, 28, lane);
          f32x4 cs = {0.f, 0.f, 0.f, 0.f};
#pragma unroll
          for (int k = 0; k < 4; ++k) cs += *(const LAS f32x4*)(csum + (k * 7 + wt) * 16 + 4 * g);
          u32x2 w; w[0] = pack_bf16((s[0] - mu * cs[0]) * r, (s[1] - mu * cs[1]) * r); w[1] = pack_bf16((s[2] - mu * cs[2]) * r, (s[3] - mu * cs[3]) * r);
          *(LAS u32x2*)(ldw + L_QT + (ea * 16 + c16) * 160 + wt * 32 + 8 * g) = w;
        }
      }
      bar_lds();
    }
    STAMP(2);
    bf16x8 w1r[12][2];
    {
      FRESH();
      const int r32 = lane & 31, hi = lane >> 5;
      const int hh = wave & 3, trow = th * 32 + r32;
      bf16x8 q16; bf16x4 q4;
      {
        const lds_cptr qp = lds + L_QT + trow * 160 + hh * 40;
        const u32x2 a = *(const LAS u32x2*)(qp + hi * 16), b = *(const LAS u32x2*)(qp + hi * 16 + 8), c = *(const LAS u32x2*)(qp + 32);
        u32x4 v = {a[0], a[1], b[0], b[1]};
        q16 = __builtin_bit_cast(bf16x8, v);
        u32x2 t = {hi ? 0u : c[0], hi ? 0u : c[1]};
        q4 = __builtin_bit_cast(bf16x4, t);
        PIN(q16); PIN(q4);
      }
      const int ftc = has4 ? ft : 0;
      const char* w1s = (const char*)(w1 + ftc * 64) + (long)kq * 32 * (F_ * 2);
      const SlabLane SL = slab_lane(lane, F_ * 2);
      unsigned sink = 0;
      if (wave >= 4) {
        slab_load(w1s, lds0 + L_STG + kq * 4096, SL);
#pragma unroll 1
        for (int blk = 0; blk < 7; ++blk) wslice_block(wo + j * 80, lds0, kq, lane, blk);
        const char* w1w = (const char*)(w1 + ftc * 64) + (long)((idx & 3) * 512 + kq * 128) * (F_ * 2);
        touch(w1w, lane * (F_ * 2), sink); touch(w1w + 64 * (F_ * 2), lane * (F_ * 2), sink);
        const char* w2w = (const char*)(w2 + tb * 64) + (long)(j * 216 + kq * 54) * (S_ * 2);
        touch(w2w, (lane < 54 ? lane : 53) * (S_ * 2), sink);
      }
      if (wave == 0) poll_flags(ws, fl + FL_KV + (bid & 31) * 256, 64, tag, lane, dead);
      bar_lds();
      STAMP(3);
      const unsigned koff = lane * 16;
      const char* KV = ws + kvoff + wave * 3072;
      auto issue_kv = [&](int ch) {
        const unsigned dst = lds0 + L_ATT + (ch % 3) * ATT_SLOT + wave * 3072;
        if (wave < 3) glds_lin<3, true>(KV + ch * ATT_CHB, dst, koff);
        else { glds1<true>(KV + ch * ATT_CHB, dst, koff); if (lane < 16) glds1<true>(KV + ch * ATT_CHB + 1024, dst + 1024, koff); }
      };
      const unsigned fsw = (((c16 >> 3) & 1) | ((g & 1) << 1)) * 32;
      const unsigned srow = (32 * kq + 8 * g + (c16 >> 2)) * 128 + 8 * (lane & 3);
      const unsigned sr0 = srow + ((64 * th) ^ fsw), sr1 = srow + ((64 * th + 32) ^ fsw);
      if (wave < 4) { issue_kv(0); issue_kv(1); }
      const unsigned ktoff = KV_KT + r32 * 8, voff = KV_V + (hi * 21 + (r32 < 20 ? r32 : 20)) * 16;
      f32x16 o = {}, negm, sA, sB;
      bf16x8 k16, v0, v1; bf16x4 k4; u32x4 p0, p1;
      if (wave < 3) wait_bar<3>(); else if (wave == 3) wait_bar<2>(); else wait_bar<28>();
      const lds_cptr ring = lds + L_ATT;
      {
        k16 = lds_b128(ring + koff);
        k4 = *(const LAS bf16x4*)(ring + ktoff);
        f32x16 z = {};
        z = MFMA32(k16, q16, z); z = MFMA32K8(k4, q4, z);
        k16 = lds_b128(ring + KV_TILE + koff);
        k4 = *(const LAS bf16x4*)(ring + KV_TILE + ktoff);
        v1 = lds_b128(ring + voff);
        w1r[0][0] = lds_tr8(lds + L_STG + sr0, 512); w1r[0][1] = lds_tr8(lds + L_STG + sr1, 512);
        const float m = rowmax16(z);
#pragma unroll
        for (int i = 0; i < 16; ++i) { negm[i] = -m; sA[i] = z[i] - m; }
        p1 = u32x4{0u, 0u, 0u, 0u};
        PIN(w1r[0][0]); PIN(w1r[0][1]);
      }
#define EX(x) __builtin_amdgcn_exp2f(x)
#define EXP4(X, i) do { X[i] = EX(X[i]); X[i + 1] = EX(X[i + 1]); X[i + 2] = EX(X[i + 2]); X[i + 3] = EX(X[i + 3]); } while (0)
#define BF8(x) __builtin_bit_cast(bf16x8, x)
#define PK4(P, X, i) do { P[0] = pack_bf16(X[i], X[i + 1]); P[1] = pack_bf16(X[i + 2], X[i + 3]); P[2] = pack_bf16(X[i + 4], X[i + 5]); P[3] = pack_bf16(X[i + 6], X[i + 7]); } while (0)
#define STEP(C, N, kn, vc) do { \
    N = MFMA32(k16, q16, negm); SBAR(); \
    v0 = lds_b128((vc) + voff); EXP4(C, 0); PIN(C); SBAR(); \
    o = MFMA32(v1, BF8(p1), o); SBAR(); \
    k16 = lds_b128((kn) + koff); EXP4(C, 4); PIN(C); SBAR(); PK4(p0, C, 0); PIN(p0); SBAR(); \
    N = MFMA32K8(k4, q4, N); SBAR(); \
    v1 = lds_b128((vc) + voff + KV_VS); EXP4(C, 8); PIN(C); SBAR(); \
    o = MFMA32(v0, BF8(p0), o); SBAR(); \
    k4 = *(const LAS bf16x4*)((kn) + ktoff); EXP4(C, 12); PIN(C); SBAR(); PK4(p1, C, 8); PIN(p1); SBAR(); \
  } while (0)
      static_for<0, ATT_NCH>([&](auto CH) {
        constexpr int ch = decltype(CH)::v;
        const lds_cptr cb = ring + (ch % 3) * ATT_SLOT;
        const lds_cptr nb = (ch + 1 < ATT_NCH) ? ring + ((ch + 1) % 3) * ATT_SLOT : cb;
        STEP(sA, sB, cb + 2 * KV_TILE, cb);
        STEP(sB, sA, cb + 3 * KV_TILE, cb + KV_TILE);
        if (ch + 1 < ATT_NCH || true) { if (wave < 4) wait_vbar<0>(); else asm volatile("s_barrier" ::: "memory"); }
        if (wave < 4) {
          if (ch + 2 < ATT_NCH) issue_kv(ch + 2);
          if ((ch & 1) == 0) slab_load(w1s + (long)(1 + ch / 2) * 128 * (F_ * 2), lds0 + L_STG + kq * 4096, SL);
        }
        if constexpr (ch & 1) { w1r[1 + ch / 2][0] = lds_tr8(lds + L_STG + sr0, 512); w1r[1 + ch / 2][1] = lds_tr8(lds + L_STG + sr1, 512); }
        SBAR();
        STEP(sA, sB, nb, cb + 2 * KV_TILE);
        if constexpr (ch & 1) { PIN(w1r[1 + ch / 2][0]); PIN(w1r[1 + ch / 2][1]); }
        STEP(sB, sA, nb + KV_TILE, cb + 3 * KV_TILE);
      });
      o = MFMA32(v1, BF8(p1), o);
      float l = hi ? o[8] : o[12];
      bool bad = !(l < 1.8446744e19f);
#pragma unroll
      for (int i = 0; i < 12; ++i) bad |= !(__builtin_fabsf(o[i]) < 3.0e38f);
      if (__builtin_expect(__any(bad), 0)) {
        float m = -INFINITY;
        o = f32x16{};
#pragma unroll 1
        for (int t = 0; t < S_ / 32; ++t) {
          const unsigned tp = kvoff + t * KV_TILE;
          const bf16x8 a16 = BF8(ld16_dev(rws, tp + koff));
          const u32x2 a4 = __builtin_amdgcn_raw_buffer_load_b64(rws, tp + ktoff, 0, 16);
          const bf16x8 w0 = BF8(ld16_dev(rws, tp + voff)), w1f = BF8(ld16_dev(rws, tp + voff + KV_VS));
          f32x16 z = {};
          z = MFMA32(a16, q16, z); z = MFMA32K8(__builtin_bit_cast(bf16x4, a4), q4, z);
          const float mn = __builtin_fmaxf(m, rowmax16(z));
          const float alpha = __builtin_amdgcn_exp2f(m - mn);
          m = mn;
#pragma unroll
          for (int i = 0; i < 16; ++i) { z[i] = __builtin_amdgcn_exp2f(z[i] - m); o[i] *= alpha; }
          bf16x8 a0, a1; pack_p(z, a0, a1);
          o = MFMA32(w0, a0, o); o = MFMA32(w1f, a1, o);
        }
        l = hi ? o[8] : o[12];
      }
      const float rl = 1.0f / l;
      const unsigned ob = WS_O + layer * XB + tb * 81920 + ((trow >> 4) * 64 + (trow & 15)) * 16;
#pragma unroll
      for (int gq = 0; gq < 3; ++gq) {
        const int d0 = 8 * gq + 4 * hi;
        if (d0 < DH_) {
          const int k = (j * 4 + hh) * DH_ + d0;
          u32x2 w; w[0] = pack_bf16(o[4 * gq] * rl, o[4 * gq + 1] * rl); w[1] = pack_bf16(o[4 * gq + 2] * rl, o[4 * gq + 3] * rl);
          st8_dev(rws, ob + (k >> 5) * 4096 + ((k & 31) >> 3) * 256 + (k & 7) * 2, w);
        }
      }
      STAMP(4);
      publish(ws, fl + FL_O + tb * 128 + j * 4, 1, 0, tag, wave, lane);
      asm volatile("" :: "v"(sink));
      if (wave >= 4) {
#pragma unroll 1
        for (int st = 0; st < 3; ++st) slab_load(w1s + (long)(9 + st) * 128 * (F_ * 2), lds0 + L_ATT + st * 16384 + kq * 4096, SL);
      }
    }
    {
      FRESH();
      if (wave == 0) poll_flags(ws, fl + FL_O + tb * 128, 8, tag, lane, dead);
      asm volatile("s_barrier" ::: "memory");
      STAMP(5);
      const unsigned abase = WS_O + layer * XB + tb * 81920 + (kq * 4 + th * 2) * 1024 + lane * 16;
      u32x4 af[5][2];
#pragma unroll
      for (int s = 0; s < 5; ++s)
#pragma unroll
        for (int t = 0; t < 2; ++t) af[s][t] = ld16_dev_s(rws, abase + t * 1024, s * 16384);
      f32x4 acc[5][2];
#pragma unroll
      for (int i = 0; i < 5; ++i) { acc[i][0] = f32x4{0.f, 0.f, 0.f, 0.f}; acc[i][1] = acc[i][0]; }
      const int q = c16 >> 2, p = lane & 3;
      const int row = 32 * kq + 8 * g + q;
      const unsigned fsw = (((q >> 1) & 1) | ((g & 1) << 1)) * 32;
      const unsigned woff = L_W + row * 128 + 8 * p, toff = L_WT + row * 32 + 8 * p;
#pragma unroll
      for (int s = 0; s < 5; ++s) {
        const bf16x8 a0 = __builtin_bit_cast(bf16x8, af[s][0]), a1 = __builtin_bit_cast(bf16x8, af[s][1]);
#pragma unroll
        for (int i = 0; i < 5; ++i) {
          const bf16x8 wf = i < 4 ? lds_tr8(lds + woff + s * 16384 + ((i * 32) ^ fsw), 512) : lds_tr8(lds + toff + s * 4096, 128);
          acc[i][0] = MFMA16(wf, a0, acc[i][0]); acc[i][1] = MFMA16(wf, a1, acc[i][1]);
        }
        SBAR();
      }
      wait_bar<0>();
#pragma unroll
      for (int i = 0; i < 5; ++i)
#pragma unroll
        for (int t = 0; t < 2; ++t) *(LAS f32x4*)(ldw + ((kq * 5 + i) * 4 + th * 2 + t) * 1024 + lane * 16) = acc[i][t];
      {
        const unsigned sr0 = L_ATT + row * 128 + 8 * p + ((64 * th) ^ fsw), sr1 = L_ATT + row * 128 + 8 * p + ((64 * th + 32) ^ fsw);
#pragma unroll
        for (int st = 0; st < 3; ++st) { w1r[9 + st][0] = lds_tr8(lds + sr0 + st * 16384, 512); w1r[9 + st][1] = lds_tr8(lds + sr1 + st * 16384, 512); }
      }
      bar_lds();
      if (wave >= 4) {
        const char* w1s = (const char*)(w1 + (has4 ? ft : 0) * 64) + (long)kq * 32 * (F_ * 2);
        const SlabLane SL = slab_lane(lane, F_ * 2);
        if (has4) {
#pragma unroll 1
          for (int st = 0; st < 4; ++st) slab_load(w1s + (long)(12 + st) * 128 * (F_ * 2), lds0 + L_RING + st * 16384 + kq * 4096, SL);
        }
        asm volatile("s_barrier" ::: "memory");
      } else {
        float s1 = 0.f, s2 = 0.f;
#pragma unroll
        for (int wt = 0; wt < 5; ++wt) {
          const f32x4 s = sum4(lds, wt * 4 + ea, 20, lane);
          const u32x2 r = *(const LAS u32x2*)(lds + res_off + wt * 32);
          u32x2 w;
          w[0] = pack_bf16(bf_lo(r[0]) + s[0], bf_hi(r[0]) + s[1]);
          w[1] = pack_bf16(bf_lo(r[1]) + s[2], bf_hi(r[1]) + s[3]);
          *(LAS u32x2*)(ldw + res_off + wt * 32) = w;
          const float a = bf_lo(w[0]), b = bf_hi(w[0]), c = bf_lo(w[1]), d = bf_hi(w[1]);
          s1 += (a + b) + (c + d);
          s2 += (a * a + b * b) + (c * c + d * d);
        }
        s1 += __shfl_xor(s1, 16); s1 += __shfl_xor(s1, 32);
        s2 += __shfl_xor(s2, 16); s2 += __shfl_xor(s2, 32);
        gu64* gr = (gu64*)(ws + WS_STAT + layer * ST_LAYER + (tb * 4 + ea) * 2048);
        if (lane < 32) __hip_atomic_store(gr + j * 32 + lane, ((unsigned long long)tag << 32) | __float_as_uint(g ? s2 : s1), RLX_AGENT);
        float tot;
        {
          unsigned spins = 0;
          for (;;) {
            bool ok = true; tot = 0.f;
#pragma unroll
            for (int i = 0; i < 4; ++i) {
              const unsigned long long v = __hip_atomic_load(gr + i * 64 + lane, RLX_AGENT);
              tot += __uint_as_float((unsigned)v); ok &= (unsigned)(v >> 32) == tag;
            }
            if (__all(ok) || dead) break;
            if (++spins > SPIN_CAP) dead = true;
            __builtin_amdgcn_s_sleep(1);
          }
        }
        STAMP(6);
        tot += __shfl_xor(tot, 32);
        float mu, var; ln_finish(__shfl(tot, c16), __shfl(tot, 16 + c16), mu, var);
        const float r1 = rsqrtf(var + LN_EPS);
        const float rr = r1 * rsqrtf(var * r1 * r1 + LN_EPS);
        float mu4[4], rr4[4];
#pragma unroll
        for (int e = 0; e < 4; ++e) { mu4[e] = __shfl(mu, 4 * g + e); rr4[e] = __shfl(rr, 4 * g + e); }
        const unsigned hb = WS_H + layer * XB + j * 327680 + ((tb * 2 + (ea >> 1)) * 5) * 1024 + (c16 + 16 * ((ea & 1) * 2 + (g >> 1))) * 16 + (g & 1) * 8;
#pragma unroll
        for (int wt = 0; wt < 5; ++wt) {
          const u32x2 v = __builtin_bit_cast(u32x2, lds_tr(lds + L_RES + (ea * 16 + 4 * g + q) * 160 + wt * 32 + 8 * p));
          u32x2 w;
          w[0] = pack_bf16((bf_lo(v[0]) - mu4[0]) * rr4[0], (bf_hi(v[0]) - mu4[1]) * rr4[1]);
          w[1] = pack_bf16((bf_lo(v[1]) - mu4[2]) * rr4[2], (bf_hi(v[1]) - mu4[3]) * rr4[3]);
          st8_dev(rws, hb + wt * 1024, w);
        }
        publish(ws, fl + FL_H + j * 512 + tb * 4, 4, 128, tag, wave, lane);
      }
      STAMP(7);
    }
    {
      FRESH();
      if (has4) {
        const int f = ft * 64 + ea * 16 + 4 * g;
        const u32x2 bias = *(const u32x2*)(b1 + f);
        if (wave == 0) poll_flags(ws, fl + FL_H + panel4 * 512 + ((idx >> 2) & 3) * 128, 32, tag, lane, dead);
        asm volatile("s_barrier" ::: "memory");
        STAMP(8);
        const unsigned l16 = lane * 16;
        const int po = (th ? 12 + 2 * kq : 3 * kq) * 1024;
        const char* hsrc = ws + WS_H + layer * XB + panel4 * 327680 + po;
        auto issue = [&](int st) {
          if (th == 0) glds_lin<3, true>(hsrc + st * 20480, lds0 + (st & 3) * 20480 + po, l16);
          else glds_lin<2, true>(hsrc + st * 20480, lds0 + (st & 3) * 20480 + po, l16);
        };
        issue(0); issue(1); issue(2);
        f32x4 acc[2][5];
#pragma unroll
        for (int i = 0; i < 2; ++i)
#pragma unroll
          for (int a = 0; a < 5; ++a) acc[i][a] = f32x4{0.f, 0.f, 0.f, 0.f};
        const lds_cptr ab = lds + kq * 5120 + l16;
        const unsigned fsw = (((c16 >> 3) & 1) | ((g & 1) << 1)) * 32;
        const unsigned srow = L_RING + (32 * kq + 8 * g + (c16 >> 2)) * 128 + 8 * (lane & 3);
        const unsigned sr0 = srow + ((64 * th) ^ fsw), sr1 = srow + ((64 * th + 32) ^ fsw);
        static_for<0, 16>([&](auto ST) {
          constexpr int st = decltype(ST)::v;
          constexpr int ahead = (15 - st) < 2 ? (15 - st) : 2;
          if (th == 0) wait_bar<3 * ahead>(); else wait_bar<2 * ahead>();
          bf16x8 af[5];
#pragma unroll
          for (int a = 0; a < 5; ++a) af[a] = lds_b128(ab + (st & 3) * 20480 + a * 1024);
          bf16x8 wa, wb;
          if constexpr (st < 12) { wa = w1r[st][0]; wb = w1r[st][1]; }
          else { wa = lds_tr8(lds + sr0 + (st - 12) * 16384, 512); wb = lds_tr8(lds + sr1 + (st - 12) * 16384, 512); }
          if (st + 3 < 16) { if (th == 0) issue(st + 3); }
          SBAR();
#pragma unroll
          for (int a = 0; a < 5; ++a) { acc[0][a] = MFMA16(wa, af[a], acc[0][a]); acc[1][a] = MFMA16(wb, af[a], acc[1][a]); }
          SBAR();
          if (st + 3 < 16) { if (th == 1) issue(st + 3); }
        });
        bar_lds();
#pragma unroll
        for (int i = 0; i < 2; ++i)
#pragma unroll
          for (int a = 0; a < 5; ++a) *(LAS f32x4*)(ldw + ((kq * 4 + 2 * th + i) * 5 + a) * 1024 + l16) = acc[i][a];
        bar_lds();
        STAMP(9);
        const unsigned ub = WS_U + layer * UB + ((panel4 * U_KS + (f >> 5)) * 5) * 1024 + (c16 + 16 * ((f & 31) >> 3)) * 16 + (f & 7) * 2;
#pragma unroll
        for (int i = 0; i < 3; ++i) {
          if (i < ewn) {
            const int at = ew0 + i;
            const f32x4 s = sum4(lds, ea * 5 + at, 20, lane);
            u32x2 w;
            w[0] = pack_bf16(gelu_tanh(s[0] + bf_lo(bias[0])), gelu_tanh(s[1] + bf_hi(bias[0])));
            w[1] = pack_bf16(gelu_tanh(s[2] + bf_lo(bias[1])), gelu_tanh(s[3] + bf_hi(bias[1])));
            st8_dev(rws, ub + at * 1024, w);
          }
        }
        publish(ws, fl + FL_U + panel4 * 512 + ft * 4, 4, 128, tag, wave, lane);
      }
    }
    STAMP(10);
    {
      FRESH();
      const int tok = tb * 64 + ea * 16 + c16;
      const float bias = __uint_as_float((unsigned)b2[tok] << 16);
      if (wave >= 4) {
        const SlabLane SL = slab_lane(lane, S_ * 2);
        const char* wsrc = (const char*)(w2 + tb * 64) + (long)kq * 32 * (S_ * 2);
#pragma unroll 1
        for (int st = 0; st < 9; ++st) slab_load(wsrc + (long)st * 128 * (S_ * 2), lds0 + st * 16384 + kq * 4096, SL);
        asm volatile("s_barrier" ::: "memory");
        static_for<0, 14>([&](auto ST) {
          constexpr int st = decltype(ST)::v;
          constexpr int last = (st + 7 > 13) ? 13 : (st + 7 < 8 ? 8 : st + 7);
          wait_vbar<4 * (last - st)>();
          if (st >= 1 && st + 8 < 14) {
            const long o = (st + 8 == 13 && kq >= 2) ? (long)(2 * 13 - 1) * 64 * (S_ * 2) : (long)(st + 8) * 128 * (S_ * 2);
            slab_load(wsrc + o, lds0 + w2_slot(st + 8) + kq * 4096, SL);
          }
        });
        asm volatile("s_barrier" ::: "memory");
      } else {
        if (wave == 0) poll_flags(ws, fl + FL_U + j * 512 + (tb & 3) * 128, 27, tag, lane, dead);
        asm volatile("s_barrier" ::: "memory");
        STAMP(11);
        const unsigned abase = WS_U + layer * UB + j * (U_KS * 5 * 1024) + kq * 5120 + lane * 16;
        const int q = c16 >> 2, p = lane & 3;
        const unsigned fsw = (((q >> 1) & 1) | ((g & 1) << 1)) * 32;
        const lds_cptr wrow = lds + (32 * kq + 8 * g + q) * 128 + 8 * p;
        u32x4 af[3][5];
#pragma unroll
        for (int st = 0; st < 3; ++st)
#pragma unroll
          for (int a = 0; a < 5; ++a) af[st][a] = ld16_dev_s(rws, abase + (a & 3) * 1024, st * 20480 + (a >> 2) * 4096);
        f32x4 acc[4][5];
#pragma unroll
        for (int i = 0; i < 4; ++i)
#pragma unroll
          for (int a = 0; a < 5; ++a) acc[i][a] = f32x4{0.f, 0.f, 0.f, 0.f};
        static_for<0, 14>([&](auto ST) {
          constexpr int st = decltype(ST)::v;
          bar_lds();
          if (st < 13 || kq < 2) {
            bf16x8 wf[4];
#pragma unroll
            for (int i = 0; i < 4; ++i) wf[i] = lds_tr8(wrow + w2_slot(st) + ((i * 32) ^ fsw), 512);
            SBAR();
#pragma unroll
            for (int a = 0; a < 5; ++a) {
              const bf16x8 x = __builtin_bit_cast(bf16x8, af[st % 3][a]);
#pragma unroll
              for (int i = 0; i < 4; ++i) acc[i][a] = MFMA16(x, wf[i], acc[i][a]);
              SBAR();
              if (st + 3 < 14) af[st % 3][a] = ld16_dev_s(rws, abase + (a & 3) * 1024, (st + 3) * 20480 + (a >> 2) * 4096);
              SBAR();
            }
          }
        });
        bar_lds();
#pragma unroll
        for (int i = 0; i < 4; ++i)
#pragma unroll
          for (int a = 0; a < 5; ++a) *(LAS f32x4*)(ldw + ((kq * 4 + i) * 5 + a) * 1024 + lane * 16) = acc[i][a];
      }
      bar_lds();
      STAMP(12);
#pragma unroll
      for (int i = 0; i < 3; ++i) {
        if (i < ewn) {
          const int at = ew0 + i;
          const f32x4 s = sum4(lds, ea * 5 + at, 20, lane);
          const u32x2 r = *(const LAS u32x2*)(lds + res_off + at * 32);
          u32x2 w;
          w[0] = pack_bf16(bf_lo(r[0]) + (s[0] + bias), bf_hi(r[0]) + (s[1] + bias));
          w[1] = pack_bf16(bf_lo(r[1]) + (s[2] + bias), bf_hi(r[1]) + (s[3] + bias));
          const int d = j * 80 + at * 16 + 4 * g;
          if (layer == 0) {
            *(LAS u32x2*)(ldw + res_off + at * 32) = w;
            st8_dev(rws, WS_X + tb * 81920 + (((d >> 5) * 4 + ea) * 64 + c16 + 16 * ((d & 31) >> 3)) * 16 + (d & 7) * 2, w);
          } else {
            *(u32x2*)(out + (long)tok * D_ + d) = w;
          }
        }
      }
      if (layer == 0) publish(ws, WS_FLAGS + FL_X + tb * 128 + j * 4, 1, 0, tag, wave, lane);
      STAMP(13);
    }
  }
  if (blockIdx.x == 0 && tid == 0) __hip_atomic_store((gu32*)(ws + WS_EPOCH), tag, RLX_AGENT);
}

extern "C" void kernel_launch(void* const* d_in, const int* in_sizes, int n_in, void* d_out, int out_size, void* d_ws,
                              size_t ws_size, hipStream_t stream) {
  (void)hipFuncSetAttribute((const void*)mega_kernel, hipFuncAttributeMaxDynamicSharedMemorySize, LDS_BYTES);
  mega_kernel<<<256, 512, LDS_BYTES, stream>>>((const u16*)d_in[0], (const u16*)d_in[1], (const u16*)d_in[2], (const u16*)d_in[3],
                                               (const u16*)d_in[4], (const u16*)d_in[6], (const u16*)d_in[7], (const u16*)d_in[8],
                                               (const u16*)d_in[9], (u16*)d_out, (char*)d_ws);
}
```

```cpp
#include <hip/hip_runtime.h>
#include <hip/hip_bf16.h>
#include <stdint.h>

typedef __attribute__((ext_vector_type(8))) short bf16x8;
typedef __attribute__((ext_vector_type(4))) short bf16x4;
typedef __attribute__((ext_vector_type(16))) float f32x16;
typedef __attribute__((ext_vector_type(4))) float f32x4;
typedef __attribute__((ext_vector_type(2))) float f32x2;
typedef __attribute__((ext_vector_type(2))) __bf16 bf2_t;
typedef __attribute__((ext_vector_type(4))) unsigned u32x4;
typedef __attribute__((ext_vector_type(2))) unsigned u32x2;
typedef unsigned short u16;
#define LAS __attribute__((address_space(3)))
#define GAS __attribute__((address_space(1)))
typedef LAS const char* lds_cptr;
typedef LAS char* lds_ptr;
typedef GAS unsigned gu32;
typedef GAS unsigned long long gu64;
#define DI __device__ __forceinline__
#define RLX_AGENT __ATOMIC_RELAXED, __HIP_MEMORY_SCOPE_AGENT

constexpr int S_ = 2048, D_ = 640, DH_ = 20, F_ = 1728;
constexpr float LN_EPS = 1e-5f;
constexpr float QSCALE = 0.22360679774997896f * 1.4426950408889634f;

constexpr int KV_TILE = 2624, KV_KT = 1024, KV_V = 1280, KV_VS = 672;

constexpr unsigned WS_EPOCH = 0;
constexpr unsigned WS_FLAGS = 4096, FL_LAYER = 32768;
constexpr unsigned FL_KV = 0;
constexpr unsigned FL_O = 8192;
constexpr unsigned FL_X = 12288;
constexpr unsigned FL_H = 16384;
constexpr unsigned FL_U = 20480;
constexpr unsigned WS_DIAG = 98304;
constexpr unsigned WS_STAT = 262144, ST_LAYER = 524288;
constexpr unsigned WS_KV = WS_STAT + 2 * ST_LAYER, KV_LAYER = 196608;
constexpr unsigned XB = S_ * D_ * 2;
constexpr unsigned WS_O = WS_KV + 2 * KV_LAYER;
constexpr unsigned WS_H = WS_O + 2 * XB;
constexpr int U_KS = 56;
constexpr unsigned UB = 8 * U_KS * 5 * 1024;
constexpr unsigned WS_U = WS_H + 2 * XB;
constexpr unsigned WS_X = WS_U + 2 * UB;
constexpr unsigned WS_END = WS_X + XB;

constexpr int L_W = 0;
constexpr int L_WT = 81920;
constexpr int L_RING = 81920;
constexpr int L_ATT = 102400;
constexpr int L_QT = 114688;
constexpr int L_STG = 134656;
constexpr int L_RES = 151040;
constexpr int L_DUMP = 161280;
constexpr int LDS_BYTES = 163840;
constexpr unsigned SPIN_CAP = 1u << 21;

DI unsigned pack_bf16(float lo, float hi) {
  f32x2 v = {lo, hi};
  bf2_t b = __builtin_convertvector(v, bf2_t);
  return __builtin_bit_cast(unsigned, b);
}
DI float bf_lo(unsigned u) { return __uint_as_float(u << 16); }
DI float bf_hi(unsigned u) { return __uint_as_float(u & 0xffff0000u); }

#define GLDS_HEAD "s_mov_b32 m0, %1\n\ts_nop 4\n\t"
template <bool DEV> DI void glds1(const void* sbase, unsigned m0v, unsigned v0) {
  if (DEV) asm volatile(GLDS_HEAD "global_load_lds_dwordx4 %2, %0 sc1" ::"s"(sbase), "s"(m0v), "v"(v0) : "memory");
  else asm volatile(GLDS_HEAD "global_load_lds_dwordx4 %2, %0" ::"s"(sbase), "s"(m0v), "v"(v0) : "memory");
}
template <bool DEV> DI void glds4(const void* sbase, unsigned m0v, unsigned v0, unsigned v1, unsigned v2, unsigned v3) {
  if (DEV)
    asm volatile(GLDS_HEAD "global_load_lds_dwordx4 %2, %0 sc1\n\tglobal_load_lds_dwordx4 %3, %0 offset:1024 sc1\n\t"
                           "global_load_lds_dwordx4 %4, %0 offset:2048 sc1\n\tglobal_load_lds_dwordx4 %5, %0 offset:3072 sc1"
                 ::"s"(sbase), "s"(m0v), "v"(v0), "v"(v1), "v"(v2), "v"(v3) : "memory");
  else
    asm volatile(GLDS_HEAD "global_load_lds_dwordx4 %2, %0\n\tglobal_load_lds_dwordx4 %3, %0 offset:1024\n\t"
                           "global_load_lds_dwordx4 %4, %0 offset:2048\n\tglobal_load_lds_dwordx4 %5, %0 offset:3072"
                 ::"s"(sbase), "s"(m0v), "v"(v0), "v"(v1), "v"(v2), "v"(v3) : "memory");
}
template <int N, bool DEV> DI void glds_lin(const void* sbase, unsigned m0v, unsigned v) {
  if (N == 1) glds1<DEV>(sbase, m0v, v);
  else if (N == 4) glds4<DEV>(sbase, m0v, v, v, v, v);
  else if (N == 2) {
    if (DEV) asm volatile(GLDS_HEAD "global_load_lds_dwordx4 %2, %0 sc1\n\tglobal_load_lds_dwordx4 %2, %0 offset:1024 sc1" ::"s"(sbase), "s"(m0v), "v"(v) : "memory");
    else asm volatile(GLDS_HEAD "global_load_lds_dwordx4 %2, %0\n\tglobal_load_lds_dwordx4 %2, %0 offset:1024" ::"s"(sbase), "s"(m0v), "v"(v) : "memory");
  } else {
    if (DEV) asm volatile(GLDS_HEAD "global_load_lds_dwordx4 %2, %0 sc1\n\tglobal_load_lds_dwordx4 %2, %0 offset:1024 sc1\n\tglobal_load_lds_dwordx4 %2, %0 offset:2048 sc1" ::"s"(sbase), "s"(m0v), "v"(v) : "memory");
    else asm volatile(GLDS_HEAD "global_load_lds_dwordx4 %2, %0\n\tglobal_load_lds_dwordx4 %2, %0 offset:1024\n\tglobal_load_lds_dwordx4 %2, %0 offset:2048" ::"s"(sbase), "s"(m0v), "v"(v) : "memory");
  }
}
DI void glds2(const void* sbase, unsigned m0v, unsigned v0, unsigned v1) {
  asm volatile(GLDS_HEAD "global_load_lds_dwordx4 %2, %0\n\tglobal_load_lds_dwordx4 %3, %0 offset:1024" ::"s"(sbase), "s"(m0v), "v"(v0), "v"(v1) : "memory");
}
#define UNI(x) __builtin_amdgcn_readfirstlane(x)
template <int N> DI void wait_bar() { asm volatile("s_waitcnt vmcnt(%0) lgkmcnt(0)\n\ts_barrier" ::"i"(N) : "memory"); }
template <int N> DI void wait_vbar() { asm volatile("s_waitcnt vmcnt(%0)\n\ts_barrier" ::"i"(N) : "memory"); }
DI void bar_lds() { asm volatile("s_waitcnt lgkmcnt(0)\n\ts_barrier" ::: "memory"); }
typedef short v4i16_t __attribute__((ext_vector_type(4)));
DI bf16x4 lds_tr(lds_cptr p) {
  return __builtin_bit_cast(bf16x4, __builtin_amdgcn_ds_read_tr16_b64_v4i16((LAS v4i16_t*)p));
}
DI bf16x8 lds_tr8(lds_cptr p, int step) {
  const bf16x4 lo = lds_tr(p), hi = lds_tr(p + step);
  return __builtin_shufflevector(lo, hi, 0, 1, 2, 3, 4, 5, 6, 7);
}
DI bf16x8 lds_b128(lds_cptr p) { return *(const LAS bf16x8*)p; }
#define MFMA16(a, b, c) __builtin_amdgcn_mfma_f32_16x16x32_bf16((a), (b), (c), 0, 0, 0)
#define MFMA32(a, b, c) __builtin_amdgcn_mfma_f32_32x32x16_bf16((a), (b), (c), 0, 0, 0)
#define MFMA32K8(a, b, c) __builtin_amdgcn_mfma_f32_32x32x8bf16_1k((a), (b), (c), 0, 0, 0)
#define SBAR() __builtin_amdgcn_sched_barrier(0)
#define PIN(x) asm volatile("" : "+v"(x))
typedef __amdgpu_buffer_rsrc_t rsrc_t;
DI void st8_dev(rsrc_t r, unsigned off, u32x2 v) { __builtin_amdgcn_raw_buffer_store_b64(v, r, off, 0, 16); }
DI u32x4 ld16_dev(rsrc_t r, unsigned off) { return __builtin_amdgcn_raw_buffer_load_b128(r, off, 0, 16); }
DI u32x4 ld16_dev_s(rsrc_t r, unsigned off, unsigned soff) { return __builtin_amdgcn_raw_buffer_load_b128(r, off, soff, 16); }

DI void poll_flags(const char* ws, unsigned off, int n, unsigned tag, int lane, bool& dead) {
  gu32* f = (gu32*)(ws + off);
  unsigned spins = 0;
  while (!dead) {
    unsigned v = tag;
    if (lane < n) v = __hip_atomic_load(f + lane, RLX_AGENT);
    if (__all(v == tag)) break;
    if (++spins > SPIN_CAP) dead = true;
    __builtin_amdgcn_s_sleep(1);
  }
  asm volatile("" ::: "memory");
}
DI void publish(const char* ws, unsigned off, int nrep, unsigned rstride, unsigned tag, int wave, int lane) {
  wait_bar<0>();
  if (wave == 0 && lane < nrep) __hip_atomic_store((gu32*)(ws + off + lane * rstride), tag, RLX_AGENT);
}

DI float fmax3(float a, float b, float c) { return __builtin_fmaxf(__builtin_fmaxf(a, b), c); }
DI float rowmax16(const f32x16& s) {
  float a = fmax3(s[0], s[1], s[2]), b = fmax3(s[3], s[4], s[5]);
  a = fmax3(a, s[6], s[7]); b = fmax3(b, s[8], s[9]); a = fmax3(a, s[10], s[11]); b = fmax3(b, s[12], s[13]);
  a = fmax3(a, s[14], s[15]); a = __builtin_fmaxf(a, b);
  return __builtin_fmaxf(a, __shfl_xor(a, 32));
}
DI void pack_p(const f32x16& p, bf16x8& lo, bf16x8& hi) {
  u32x4 a, b;
#pragma unroll
  for (int i = 0; i < 4; ++i) { a[i] = pack_bf16(p[2 * i], p[2 * i + 1]); b[i] = pack_bf16(p[8 + 2 * i], p[9 + 2 * i]); }
  lo = __builtin_bit_cast(bf16x8, a); hi = __builtin_bit_cast(bf16x8, b);
}
DI float gelu_tanh(float u) {
  const float z = 0.7978845608028654f * (u + 0.044715f * u * u * u);
  const float e = __builtin_amdgcn_exp2f(-2.885390081777927f * z);
  return u * __builtin_amdgcn_rcpf(1.f + e);
}

DI void wslice_block(const u16* w, unsigned lds0, int lw, int lane, int blk) {
  const char* src = (const char*)w + lw * (160 * 1280);
  if (blk < 5) {
    const int r = lane >> 3, pos = lane & 7;
    const unsigned pe = (((pos >> 1) ^ ((r >> 1) & 1)) << 1 | (pos & 1)) * 16;
    const unsigned v0 = r * 1280 + pe, v1 = r * 1280 + (pe ^ 64) + 9216;
    glds4<false>(src + blk * (32 * 1280), lds0 + L_W + lw * 20480 + blk * 4096, v0, v1, v0 + 18432, v1 + 18432);
  } else {
    const unsigned t0 = (lane >> 1) * 1280 + 128 + (lane & 1) * 16;
    if (blk == 5) glds4<false>(src, lds0 + L_WT + lw * 5120, t0, t0 + 39936, t0 + 2 * 39936, t0 + 3 * 39936);
    else glds1<false>(src + 128 * 1280, lds0 + L_WT + lw * 5120 + 4096, t0);
  }
}

DI f32x4 sum4(lds_cptr lds, int t, int nt, int lane) {
  f32x4 s = *(const LAS f32x4*)(lds + t * 1024 + lane * 16);
#pragma unroll
  for (int k = 1; k < 4; ++k) s += *(const LAS f32x4*)(lds + (k * nt + t) * 1024 + lane * 16);
  return s;
}
DI void ln_finish(float s1, float s2, float& mu, float& var) { mu = s1 * (1.f / D_); var = fmaxf(s2 * (1.f / D_) - mu * mu, 0.f); }

constexpr int ATT_CH = 4;
constexpr int ATT_CHB = ATT_CH * KV_TILE;
constexpr int ATT_SLOT = 10752;
constexpr int ATT_NCH = S_ / 32 / ATT_CH;
constexpr int w2_slot(int st) { return (st % 9) < 4 ? L_RING + (st % 9) * 16384 : ((st % 9) - 4) * 16384; }
template <int N> struct IC { static constexpr int v = N; };
template <int I, int N, class F> DI void static_for(F&& f) { if constexpr (I < N) { f(IC<I>{}); static_for<I + 1, N>(f); } }

#define STAMP(i) do { } while (0)

__global__ __launch_bounds__(512, 2) void mega_kernel(const u16* __restrict__ x0, const u16* __restrict__ wq, const u16* __restrict__ wk,
                                                      const u16* __restrict__ wv, const u16* __restrict__ wo, const u16* __restrict__ w1,
                                                      const u16* __restrict__ b1, const u16* __restrict__ w2, const u16* __restrict__ b2,
                                                      u16* __restrict__ out, char* ws) {
  extern __shared__ __attribute__((aligned(16))) char smem[];
  const int tid = threadIdx.x;
  const unsigned lds0 = (unsigned)(uintptr_t)smem;
  const lds_cptr lds = (lds_cptr)smem;
  const lds_ptr ldw = (lds_ptr)smem;
#define FRESH() \
  int lane = tid & 63; asm volatile("" : "+v"(lane)); \
  int wave = UNI(tid >> 6); asm volatile("" : "+s"(wave)); \
  int bid = blockIdx.x; asm volatile("" : "+s"(bid)); \
  const int xcd = bid & 7, idx = bid >> 3, tb = xcd * 4 + (idx >> 3), j = idx & 7; \
  const int g = lane >> 4, c16 = lane & 15, kq = wave & 3, th = wave >> 2; \
  const int ea = wave & 3, ew0 = th ? 3 : 0, ewn = th ? 2 : 3; \
  const unsigned res_off = L_RES + (ea * 16 + c16) * 160 + 8 * g; \
  const int panel4 = (xcd & 1) * 4 + (idx & 3), ft = (xcd >> 1) * 7 + (idx >> 2); \
  const bool has4 = idx < 28 && ft < 27; \
  (void)xcd; (void)g; (void)c16; (void)kq; (void)th; (void)ea; (void)ew0; (void)ewn; (void)res_off; (void)tb; (void)j; (void)panel4; (void)has4
  const rsrc_t rws = __builtin_amdgcn_make_buffer_rsrc(ws, 0, WS_END, 0x00020000);
  unsigned tag = 0;
  bool dead = false;

#pragma unroll 1
  for (int layer = 0; layer < 2; ++layer) {
    const unsigned fl = WS_FLAGS + layer * FL_LAYER;
    const unsigned kvoff = WS_KV + layer * KV_LAYER;
    {
      FRESH();
      STAMP(0);
      if (wave >= 4) {
#pragma unroll 1
        for (int blk = 0; blk < 7; ++blk) wslice_block(wq + j * 80, lds0, wave - 4, lane, blk);
      } else {
        if (j < 2) {
          const char* xs = (const char*)(j == 0 ? wk : wv);
          if (wave < 3) {
            glds_lin<4, false>(xs + wave * 8192, lds0 + L_ATT + wave * 8192, lane * 16);
            glds_lin<4, false>(xs + wave * 8192 + 4096, lds0 + L_ATT + wave * 8192 + 4096, lane * 16);
          } else glds1<false>(xs + 24576, lds0 + L_ATT + 24576, lane * 16);
        }
        if (layer == 0) {
#pragma unroll 1
          for (int pc = wave; pc < 10; pc += 4) {
            const int n = pc * 64 + lane, row = n / 10, ch = n - row * 10;
            glds1<false>(x0 + (long)tb * 64 * D_ + j * 80, lds0 + L_RES + pc * 1024, row * 1280 + ch * 16);
          }
        }
      }
      rsrc_t ra = rws; unsigned abase, astr_t, astr_k;
      if (layer == 0) {
        ra = __builtin_amdgcn_make_buffer_rsrc((void*)x0, 0, XB, 0x00020000);
        abase = (tb * 64 + th * 32 + c16) * (D_ * 2) + kq * 64 + g * 16; astr_t = 16 * D_ * 2; astr_k = 256;
      } else {
        if (wave == 0) poll_flags(ws, WS_FLAGS + FL_X + tb * 128, 8, tag, lane, dead);
        asm volatile("s_barrier" ::: "memory");
        abase = WS_X + tb * 81920 + (kq * 4 + th * 2) * 1024 + lane * 16; astr_t = 1024; astr_k = 16384;
      }
      STAMP(1);
      u32x4 af[5][2];
#pragma unroll
      for (int s = 0; s < 5; ++s)
#pragma unroll
        for (int t = 0; t < 2; ++t) af[s][t] = ld16_dev_s(ra, abase, s * astr_k + t * astr_t);
      unsigned epoch = tag - 1u;
      if (layer == 0) epoch = __hip_atomic_load((gu32*)(ws + WS_EPOCH), RLX_AGENT);
      wait_bar<0>();
      tag = UNI(epoch) + 1u;
      bf16x8 ones;
#pragma unroll
      for (int i = 0; i < 8; ++i) ones[i] = (short)0x3f80;
      f32x4 acc[5][2], akv[2][2], aon[3], aonk;
#pragma unroll
      for (int i = 0; i < 5; ++i) { acc[i][0] = f32x4{0.f, 0.f, 0.f, 0.f}; acc[i][1] = acc[i][0]; }
#pragma unroll
      for (int i = 0; i < 2; ++i) { akv[i][0] = acc[0][0]; akv[i][1] = acc[0][0]; }
      aon[0] = aon[1] = aon[2] = aonk = acc[0][0];
      float st1[2] = {0.f, 0.f}, st2[2] = {0.f, 0.f};
      const int q = c16 >> 2, p = lane & 3;
      const int row = 32 * kq + 8 * g + q;
      const unsigned fsw = (((q >> 1) & 1) | ((g & 1) << 1)) * 32;
      const unsigned woff = L_W + row * 128 + 8 * p, toff = L_WT + row * 32 + 8 * p, xoff = L_ATT + row * 40 + 8 * p;
#pragma unroll
      for (int s = 0; s < 5; ++s) {
        const bf16x8 a0 = __builtin_bit_cast(bf16x8, af[s][0]), a1 = __builtin_bit_cast(bf16x8, af[s][1]);
        bf16x8 wf[5];
#pragma unroll
        for (int i = 0; i < 4; ++i) wf[i] = lds_tr8(lds + woff + s * 16384 + ((i * 32) ^ fsw), 512);
        wf[4] = lds_tr8(lds + toff + s * 4096, 128);
#pragma unroll
        for (int i = 0; i < 5; ++i) { acc[i][0] = MFMA16(wf[i], a0, acc[i][0]); acc[i][1] = MFMA16(wf[i], a1, acc[i][1]); }
        if (th == 0) { aon[0] = MFMA16(wf[0], ones, aon[0]); aon[1] = MFMA16(wf[1], ones, aon[1]); aon[2] = MFMA16(wf[2], ones, aon[2]); }
        else { aon[0] = MFMA16(wf[3], ones, aon[0]); aon[1] = MFMA16(wf[4], ones, aon[1]); }
        if (j < 2) {
          const bf16x8 x0f = lds_tr8(lds + xoff + s * 5120, 160), x1f = lds_tr8(lds + xoff + s * 5120 + 32, 160);
          const bf16x8 xs = th ? x1f : x0f;
          if (j == 0) {
            akv[0][0] = MFMA16(x0f, a0, akv[0][0]); akv[0][1] = MFMA16(x0f, a1, akv[0][1]);
            akv[1][0] = MFMA16(x1f, a0, akv[1][0]); akv[1][1] = MFMA16(x1f, a1, akv[1][1]);
            aonk = MFMA16(xs, ones, aonk);
          } else {
            akv[0][0] = MFMA16(a0, x0f, akv[0][0]); akv[0][1] = MFMA16(a1, x0f, akv[0][1]);
            akv[1][0] = MFMA16(a0, x1f, akv[1][0]); akv[1][1] = MFMA16(a1, x1f, akv[1][1]);
            aonk = MFMA16(ones, xs, aonk);
          }
        }
#pragma unroll
        for (int t = 0; t < 2; ++t)
#pragma unroll
          for (int e = 0; e < 4; ++e) {
            const float lo = bf_lo(af[s][t][e]), hi = bf_hi(af[s][t][e]);
            st1[t] += lo + hi;
            st2[t] = __builtin_fmaf(lo, lo, __builtin_fmaf(hi, hi, st2[t]));
          }
      }
      bar_lds();
      LAS float* stat = (LAS float*)(ldw + L_STG);
      LAS float* csum = (LAS float*)(ldw + L_STG + 2048);
#pragma unroll
      for (int t = 0; t < 2; ++t) {
        float a = st1[t], b = st2[t];
        a += __shfl_xor(a, 16); a += __shfl_xor(a, 32);
        b += __shfl_xor(b, 16); b += __shfl_xor(b, 32);
        if (g == 0) *(LAS f32x2*)(stat + (kq * 64 + th * 32 + t * 16 + c16) * 2) = f32x2{a, b};
      }
      if (j < 2) {
#pragma unroll
        for (int i = 0; i < 2; ++i)
#pragma unroll
          for (int t = 0; t < 2; ++t) *(LAS f32x4*)(ldw + ((kq * 7 + 5 + i) * 4 + th * 2 + t) * 1024 + lane * 16) = akv[i][t];
        if (j == 0) { if (c16 == 0) *(LAS f32x4*)(csum + (kq * 7 + 5 + th) * 16 + 4 * g) = aonk; }
        else { if (g == 0) csum[(kq * 7 + 5 + th) * 16 + c16] = aonk[0]; }
      }
#pragma unroll
      for (int i = 0; i < 5; ++i)
#pragma unroll
        for (int t = 0; t < 2; ++t) *(LAS f32x4*)(ldw + ((kq * 7 + i) * 4 + th * 2 + t) * 1024 + lane * 16) = acc[i][t];
      if (c16 == 0) {
#pragma unroll
        for (int i = 0; i < 3; ++i) if (i < ewn) *(LAS f32x4*)(csum + (kq * 7 + ew0 + i) * 16 + 4 * g) = aon[i];
      }
      bar_lds();
      float s1 = 0.f, s2 = 0.f;
#pragma unroll
      for (int k = 0; k < 4; ++k) { const f32x2 v = *(const LAS f32x2*)(stat + (k * 64 + ea * 16 + c16) * 2); s1 += v[0]; s2 += v[1]; }
      float mu, var; ln_finish(s1, s2, mu, var);
      const float rs = rsqrtf(var + LN_EPS);
      if (j < 2) {
        const int wt = 5 + th;
        const f32x4 s = sum4(lds, wt * 4 + ea, 28, lane);
        const unsigned tile = kvoff + (tb * 2 + (ea >> 1)) * KV_TILE;
        if (j == 0) {
          f32x4 cs = {0.f, 0.f, 0.f, 0.f};
#pragma unroll
          for (int k = 0; k < 4; ++k) cs += *(const LAS f32x4*)(csum + (k * 7 + wt) * 16 + 4 * g);
          u32x2 w; w[0] = pack_bf16((s[0] - mu * cs[0]) * rs, (s[1] - mu * cs[1]) * rs); w[1] = pack_bf16((s[2] - mu * cs[2]) * rs, (s[3] - mu * cs[3]) * rs);
          const int r32 = (ea & 1) * 16 + c16;
          if (th == 0) st8_dev(rws, tile + ((g >> 1) * 32 + r32) * 16 + (g & 1) * 8, w);
          else if (g == 0) st8_dev(rws, tile + KV_KT + r32 * 8, w);
        } else {
          float cs = 0.f;
#pragma unroll
          for (int k = 0; k < 4; ++k) cs += csum[(k * 7 + wt) * 16 + c16];
          float o[4];
#pragma unroll
          for (int e = 0; e < 4; ++e) o[e] = (s[e] - __shfl(mu, 4 * g + e) * cs) * __shfl(rs, 4 * g + e);
          const int d = th * 16 + c16;
          u32x2 w; w[0] = pack_bf16(o[0], o[1]); w[1] = pack_bf16(o[2], o[3]);
          if (d == DH_) { w[0] = 0x3f803f80u; w[1] = 0x3f803f80u; }
          if (d <= DH_) st8_dev(rws, tile + KV_V + (ea & 1) * KV_VS + ((g & 1) * 21 + d) * 16 + (g >> 1) * 8, w);
        }
        publish(ws, fl + FL_KV + (tb * 2 + j) * 4, 32, 256, tag, wave, lane);
      }
      const float r = rs * QSCALE;
#pragma unroll
      for (int i = 0; i < 3; ++i) {
        if (i < ewn) {
          const int wt = ew0 + i;
          const f32x4 s = sum4(lds, wt * 4 + ea, 28, lane);
          f32x4 cs = {0.f, 0.f, 0.f, 0.f};
#pragma unroll
          for (int k = 0; k < 4; ++k) cs += *(const LAS f32x4*)(csum + (k * 7 + wt) * 16 + 4 * g);
          u32x2 w; w[0] = pack_bf16((s[0] - mu * cs[0]) * r, (s[1] - mu * cs[1]) * r); w[1] = pack_bf16((s[2] - mu * cs[2]) * r, (s[3] - mu * cs[3]) * r);
          *(LAS u32x2*)(ldw + L_QT + (ea * 16 + c16) * 160 + wt * 32 + 8 * g) = w;
        }
      }
      bar_lds();
    }
    STAMP(2);
    bf16x8 w1r[16][2];
    {
      FRESH();
      const int r32 = lane & 31, hi = lane >> 5;
      const int hh = wave & 3, trow = th * 32 + r32;
      bf16x8 q16; bf16x4 q4;
      {
        const lds_cptr qp = lds + L_QT + trow * 160 + hh * 40;
        const u32x2 a = *(const LAS u32x2*)(qp + hi * 16), b = *(const LAS u32x2*)(qp + hi * 16 + 8), c = *(const LAS u32x2*)(qp + 32);
        u32x4 v = {a[0], a[1], b[0], b[1]};
        q16 = __builtin_bit_cast(bf16x8, v);
        u32x2 t = {hi ? 0u : c[0], hi ? 0u : c[1]};
        q4 = __builtin_bit_cast(bf16x4, t);
        PIN(q16); PIN(q4);
      }
      if (wave == 0) poll_flags(ws, fl + FL_KV + (bid & 31) * 256, 64, tag, lane, dead);
      bar_lds();
      STAMP(3);
      const unsigned koff = lane * 16;
      const char* KV = ws + kvoff + wave * 3072;
      auto issue_kv = [&](int ch) {
        const unsigned dst = lds0 + L_ATT + (ch % 3) * ATT_SLOT + wave * 3072;
        if (wave < 3) glds_lin<3, true>(KV + ch * ATT_CHB, dst, koff);
        else { glds1<true>(KV + ch * ATT_CHB, dst, koff); if (lane < 16) glds1<true>(KV + ch * ATT_CHB + 1024, dst + 1024, koff); }
      };
      const int ftc = has4 ? ft : 0;
      const char* w1s = (const char*)(w1 + ftc * 64 + th * 32) + (long)kq * 32 * (F_ * 2);
      const unsigned sv0 = (lane >> 2) * (F_ * 2) + ((lane & 3) ^ ((lane >> 4) & 2)) * 16, sv1 = sv0 + 16 * (F_ * 2) - 1024;
      const unsigned stg = L_STG + wave * 2048;
      const unsigned srd = stg + (8 * g + (c16 >> 2)) * 64 + (g & 1) * 32 + 8 * (lane & 3);
      const char* w1w = (const char*)(w1 + ftc * 64) + (long)((idx & 3) * 32 + (wave & 3) * 8) * (F_ * 2);
      const unsigned wv1 = (lane >> 3) * (F_ * 2) + (lane & 7) * 16;
      const char* w2w = (const char*)(w2 + tb * 64) + (long)(j * 216) * (S_ * 2);
      const unsigned wv2 = (lane >> 3) * (S_ * 2) + (lane & 7) * 16;
      if (wave < 4) { issue_kv(0); issue_kv(1); }
      const unsigned ktoff = KV_KT + r32 * 8, voff = KV_V + (hi * 21 + (r32 < 20 ? r32 : 20)) * 16;
      f32x16 o = {}, negm, sA, sB;
      bf16x8 k16, v0, v1; bf16x4 k4; u32x4 p0, p1;
      if (wave < 3) wait_bar<3>(); else if (wave == 3) wait_bar<2>(); else wait_bar<0>();
      const lds_cptr ring = lds + L_ATT;
      {
        k16 = lds_b128(ring + koff);
        k4 = *(const LAS bf16x4*)(ring + ktoff);
        f32x16 z = {};
        z = MFMA32(k16, q16, z); z = MFMA32K8(k4, q4, z);
        k16 = lds_b128(ring + KV_TILE + koff);
        k4 = *(const LAS bf16x4*)(ring + KV_TILE + ktoff);
        v1 = lds_b128(ring + voff);
        const float m = rowmax16(z);
#pragma unroll
        for (int i = 0; i < 16; ++i) { negm[i] = -m; sA[i] = z[i] - m; }
        p1 = u32x4{0u, 0u, 0u, 0u};
      }
#define EX(x) __builtin_amdgcn_exp2f(x)
#define EXP4(X, i) do { X[i] = EX(X[i]); X[i + 1] = EX(X[i + 1]); X[i + 2] = EX(X[i + 2]); X[i + 3] = EX(X[i + 3]); } while (0)
#define BF8(x) __builtin_bit_cast(bf16x8, x)
#define PK4(P, X, i) do { P[0] = pack_bf16(X[i], X[i + 1]); P[1] = pack_bf16(X[i + 2], X[i + 3]); P[2] = pack_bf16(X[i + 4], X[i + 5]); P[3] = pack_bf16(X[i + 6], X[i + 7]); } while (0)
#define STEP(C, N, kn, vc) do { \
    N = MFMA32(k16, q16, negm); SBAR(); \
    v0 = lds_b128((vc) + voff); EXP4(C, 0); PIN(C); SBAR(); \
    o = MFMA32(v1, BF8(p1), o); SBAR(); \
    k16 = lds_b128((kn) + koff); EXP4(C, 4); PIN(C); SBAR(); PK4(p0, C, 0); PIN(p0); SBAR(); \
    N = MFMA32K8(k4, q4, N); SBAR(); \
    v1 = lds_b128((vc) + voff + KV_VS); EXP4(C, 8); PIN(C); SBAR(); \
    o = MFMA32(v0, BF8(p0), o); SBAR(); \
    k4 = *(const LAS bf16x4*)((kn) + ktoff); EXP4(C, 12); PIN(C); SBAR(); PK4(p1, C, 8); PIN(p1); SBAR(); \
  } while (0)
      static_for<0, ATT_NCH>([&](auto CH) {
        constexpr int ch = decltype(CH)::v;
        const lds_cptr cb = ring + (ch % 3) * ATT_SLOT;
        const lds_cptr nb = (ch + 1 < ATT_NCH) ? ring + ((ch + 1) % 3) * ATT_SLOT : cb;
        STEP(sA, sB, cb + 2 * KV_TILE, cb);
        STEP(sB, sA, cb + 3 * KV_TILE, cb + KV_TILE);
        constexpr int pe = (ch == 0) ? 0 : ((ch - 1 < 6 ? 4 : (ch - 1 == 6 ? 1 : 0)) + (ch - 1 + 3 < 16 ? 1 : 0) + ((ch - 1 >= 8 && ch - 1 < 15) ? 1 : 0));
        if (wave < 4) wait_vbar<pe>(); else wait_vbar<0>();
        if constexpr (ch > 0) { w1r[ch - 1][0] = lds_tr8(lds + srd, 256); w1r[ch - 1][1] = lds_tr8(lds + (srd ^ 32), 256); }
        SBAR();
        STEP(sA, sB, nb, cb + 2 * KV_TILE);
        if constexpr (ch > 0) { PIN(w1r[ch - 1][0]); PIN(w1r[ch - 1][1]); }
        if (ch + 2 < ATT_NCH) { if (wave < 4) issue_kv(ch + 2); }
        glds2(w1s + (long)ch * 128 * (F_ * 2), lds0 + stg, sv0, sv1);
        if (wave < 4) {
          if (ch < 7) wslice_block(wo + j * 80, lds0, wave, lane, ch);
          if (ch + 3 < 16) glds1<false>(w1w + (long)(ch + 3) * 128 * (F_ * 2), lds0 + L_DUMP, wv1);
          if (ch >= 8 && ch < 15) { const int pc = (ch - 8) * 4 + wave; glds1<false>(w2w + (long)(pc < 27 ? pc : 26) * 8 * (S_ * 2), lds0 + L_DUMP, wv2); }
        }
        SBAR();
        STEP(sB, sA, nb + KV_TILE, cb + 3 * KV_TILE);
      });
      o = MFMA32(v1, BF8(p1), o);
      float l = hi ? o[8] : o[12];
      bool bad = !(l < 1.8446744e19f);
#pragma unroll
      for (int i = 0; i < 12; ++i) bad |= !(__builtin_fabsf(o[i]) < 3.0e38f);
      if (__builtin_expect(__any(bad), 0)) {
        float m = -INFINITY;
        o = f32x16{};
#pragma unroll 1
        for (int t = 0; t < S_ / 32; ++t) {
          const unsigned tp = kvoff + t * KV_TILE;
          const bf16x8 a16 = BF8(ld16_dev(rws, tp + koff));
          const u32x2 a4 = __builtin_amdgcn_raw_buffer_load_b64(rws, tp + ktoff, 0, 16);
          const bf16x8 w0 = BF8(ld16_dev(rws, tp + voff)), w1f = BF8(ld16_dev(rws, tp + voff + KV_VS));
          f32x16 z = {};
          z = MFMA32(a16, q16, z); z = MFMA32K8(__builtin_bit_cast(bf16x4, a4), q4, z);
          const float mn = __builtin_fmaxf(m, rowmax16(z));
          const float alpha = __builtin_amdgcn_exp2f(m - mn);
          m = mn;
#pragma unroll
          for (int i = 0; i < 16; ++i) { z[i] = __builtin_amdgcn_exp2f(z[i] - m); o[i] *= alpha; }
          bf16x8 a0, a1; pack_p(z, a0, a1);
          o = MFMA32(w0, a0, o); o = MFMA32(w1f, a1, o);
        }
        l = hi ? o[8] : o[12];
      }
      const float rl = 1.0f / l;
      const unsigned ob = WS_O + layer * XB + tb * 81920 + ((trow >> 4) * 64 + (trow & 15)) * 16;
#pragma unroll
      for (int gq = 0; gq < 3; ++gq) {
        const int d0 = 8 * gq + 4 * hi;
        if (d0 < DH_) {
          const int k = (j * 4 + hh) * DH_ + d0;
          u32x2 w; w[0] = pack_bf16(o[4 * gq] * rl, o[4 * gq + 1] * rl); w[1] = pack_bf16(o[4 * gq + 2] * rl, o[4 * gq + 3] * rl);
          st8_dev(rws, ob + (k >> 5) * 4096 + ((k & 31) >> 3) * 256 + (k & 7) * 2, w);
        }
      }
      STAMP(4);
      publish(ws, fl + FL_O + tb * 128 + j * 4, 1, 0, tag, wave, lane);
      w1r[15][0] = lds_tr8(lds + srd, 256); w1r[15][1] = lds_tr8(lds + (srd ^ 32), 256);
    }
    {
      FRESH();
      if (wave == 0) poll_flags(ws, fl + FL_O + tb * 128, 8, tag, lane, dead);
      asm volatile("s_barrier" ::: "memory");
      STAMP(5);
      const unsigned abase = WS_O + layer * XB + tb * 81920 + (kq * 4 + th * 2) * 1024 + lane * 16;
      u32x4 af[5][2];
#pragma unroll
      for (int s = 0; s < 5; ++s)
#pragma unroll
        for (int t = 0; t < 2; ++t) af[s][t] = ld16_dev_s(rws, abase + t * 1024, s * 16384);
      f32x4 acc[5][2];
#pragma unroll
      for (int i = 0; i < 5; ++i) { acc[i][0] = f32x4{0.f, 0.f, 0.f, 0.f}; acc[i][1] = acc[i][0]; }
      const int q = c16 >> 2, p = lane & 3;
      const int row = 32 * kq + 8 * g + q;
      const unsigned fsw = (((q >> 1) & 1) | ((g & 1) << 1)) * 32;
      const unsigned woff = L_W + row * 128 + 8 * p, toff = L_WT + row * 32 + 8 * p;
#pragma unroll
      for (int s = 0; s < 5; ++s) {
        const bf16x8 a0 = __builtin_bit_cast(bf16x8, af[s][0]), a1 = __builtin_bit_cast(bf16x8, af[s][1]);
#pragma unroll
        for (int i = 0; i < 5; ++i) {
          const bf16x8 wf = i < 4 ? lds_tr8(lds + woff + s * 16384 + ((i * 32) ^ fsw), 512) : lds_tr8(lds + toff + s * 4096, 128);
          acc[i][0] = MFMA16(wf, a0, acc[i][0]); acc[i][1] = MFMA16(wf, a1, acc[i][1]);
        }
        SBAR();
      }
      bar_lds();
#pragma unroll
      for (int i = 0; i < 5; ++i)
#pragma unroll
        for (int t = 0; t < 2; ++t) *(LAS f32x4*)(ldw + ((kq * 5 + i) * 4 + th * 2 + t) * 1024 + lane * 16) = acc[i][t];
      bar_lds();
      if (wave >= 4) {
        const int r = lane >> 3, pos = lane & 7;
        const unsigned pe = (((pos >> 1) ^ ((r >> 1) & 1)) << 1 | (pos & 1)) * 16;
        const unsigned v0 = r * (S_ * 2) + pe + 4096, v1 = (r + 8) * (S_ * 2) + (pe ^ 64) + 3072, vstep = 16 * (S_ * 2) - 2048;
        const char* wsrc = (const char*)(w2 + tb * 64) - 4096 + (long)kq * 32 * (S_ * 2);
#pragma unroll 1
        for (int st = 0; st < 4; ++st) glds4<false>(wsrc + (long)st * 128 * (S_ * 2), lds0 + L_RING + st * 16384 + kq * 4096, v0, v1, v0 + vstep, v1 + vstep);
        asm volatile("s_barrier" ::: "memory");
      } else {
        float s1 = 0.f, s2 = 0.f;
#pragma unroll
        for (int wt = 0; wt < 5; ++wt) {
          const f32x4 s = sum4(lds, wt * 4 + ea, 20, lane);
          const u32x2 r = *(const LAS u32x2*)(lds + res_off + wt * 32);
          u32x2 w;
          w[0] = pack_bf16(bf_lo(r[0]) + s[0], bf_hi(r[0]) + s[1]);
          w[1] = pack_bf16(bf_lo(r[1]) + s[2], bf_hi(r[1]) + s[3]);
          *(LAS u32x2*)(ldw + res_off + wt * 32) = w;
          const float a = bf_lo(w[0]), b = bf_hi(w[0]), c = bf_lo(w[1]), d = bf_hi(w[1]);
          s1 += (a + b) + (c + d);
          s2 += (a * a + b * b) + (c * c + d * d);
        }
        s1 += __shfl_xor(s1, 16); s1 += __shfl_xor(s1, 32);
        s2 += __shfl_xor(s2, 16); s2 += __shfl_xor(s2, 32);
        gu64* gr = (gu64*)(ws + WS_STAT + layer * ST_LAYER + (tb * 4 + ea) * 2048);
        if (lane < 32) __hip_atomic_store(gr + j * 32 + lane, ((unsigned long long)tag << 32) | __float_as_uint(g ? s2 : s1), RLX_AGENT);
        float tot;
        {
          unsigned spins = 0;
          for (;;) {
            bool ok = true; tot = 0.f;
#pragma unroll
            for (int i = 0; i < 4; ++i) {
              const unsigned long long v = __hip_atomic_load(gr + i * 64 + lane, RLX_AGENT);
              tot += __uint_as_float((unsigned)v); ok &= (unsigned)(v >> 32) == tag;
            }
            if (__all(ok) || dead) break;
            if (++spins > SPIN_CAP) dead = true;
            __builtin_amdgcn_s_sleep(1);
          }
        }
        STAMP(6);
        tot += __shfl_xor(tot, 32);
        float mu, var; ln_finish(__shfl(tot, c16), __shfl(tot, 16 + c16), mu, var);
        const float r1 = rsqrtf(var + LN_EPS);
        const float rr = r1 * rsqrtf(var * r1 * r1 + LN_EPS);
        float mu4[4], rr4[4];
#pragma unroll
        for (int e = 0; e < 4; ++e) { mu4[e] = __shfl(mu, 4 * g + e); rr4[e] = __shfl(rr, 4 * g + e); }
        const unsigned hb = WS_H + layer * XB + j * 327680 + ((tb * 2 + (ea >> 1)) * 5) * 1024 + (c16 + 16 * ((ea & 1) * 2 + (g >> 1))) * 16 + (g & 1) * 8;
#pragma unroll
        for (int wt = 0; wt < 5; ++wt) {
          const u32x2 v = __builtin_bit_cast(u32x2, lds_tr(lds + L_RES + (ea * 16 + 4 * g + q) * 160 + wt * 32 + 8 * p));
          u32x2 w;
          w[0] = pack_bf16((bf_lo(v[0]) - mu4[0]) * rr4[0], (bf_hi(v[0]) - mu4[1]) * rr4[1]);
          w[1] = pack_bf16((bf_lo(v[1]) - mu4[2]) * rr4[2], (bf_hi(v[1]) - mu4[3]) * rr4[3]);
          st8_dev(rws, hb + wt * 1024, w);
        }
        publish(ws, fl + FL_H + j * 512 + tb * 4, 4, 128, tag, wave, lane);
      }
      STAMP(7);
    }
    {
      FRESH();
      if (has4) {
        const int f = ft * 64 + ea * 16 + 4 * g;
        const u32x2 bias = *(const u32x2*)(b1 + f);
        if (wave == 0) poll_flags(ws, fl + FL_H + panel4 * 512 + ((idx >> 2) & 3) * 128, 32, tag, lane, dead);
        asm volatile("s_barrier" ::: "memory");
        STAMP(8);
        const unsigned l16 = lane * 16;
        const int po = (th ? 12 + 2 * kq : 3 * kq) * 1024;
        const char* hsrc = ws + WS_H + layer * XB + panel4 * 327680 + po;
        auto issue = [&](int st) {
          if (th == 0) glds_lin<3, true>(hsrc + st * 20480, lds0 + (st & 3) * 20480 + po, l16);
          else glds_lin<2, true>(hsrc + st * 20480, lds0 + (st & 3) * 20480 + po, l16);
        };
        issue(0); issue(1); issue(2);
        f32x4 acc[2][5];
#pragma unroll
        for (int i = 0; i < 2; ++i)
#pragma unroll
          for (int a = 0; a < 5; ++a) acc[i][a] = f32x4{0.f, 0.f, 0.f, 0.f};
        const lds_cptr ab = lds + kq * 5120 + l16;
        static_for<0, 16>([&](auto ST) {
          constexpr int st = decltype(ST)::v;
          constexpr int ahead = (15 - st) < 2 ? (15 - st) : 2;
          if (th == 0) wait_bar<3 * ahead>(); else wait_bar<2 * ahead>();
          bf16x8 af[5];
#pragma unroll
          for (int a = 0; a < 5; ++a) af[a] = lds_b128(ab + (st & 3) * 20480 + a * 1024);
          if (st + 3 < 16) { if (th == 0) issue(st + 3); }
          SBAR();
#pragma unroll
          for (int a = 0; a < 5; ++a) { acc[0][a] = MFMA16(w1r[st][0], af[a], acc[0][a]); acc[1][a] = MFMA16(w1r[st][1], af[a], acc[1][a]); }
          SBAR();
          if (st + 3 < 16) { if (th == 1) issue(st + 3); }
        });
        bar_lds();
#pragma unroll
        for (int i = 0; i < 2; ++i)
#pragma unroll
          for (int a = 0; a < 5; ++a) *(LAS f32x4*)(ldw + ((kq * 4 + 2 * th + i) * 5 + a) * 1024 + l16) = acc[i][a];
        bar_lds();
        STAMP(9);
        const unsigned ub = WS_U + layer * UB + ((panel4 * U_KS + (f >> 5)) * 5) * 1024 + (c16 + 16 * ((f & 31) >> 3)) * 16 + (f & 7) * 2;
#pragma unroll
        for (int i = 0; i < 3; ++i) {
          if (i < ewn) {
            const int at = ew0 + i;
            const f32x4 s = sum4(lds, ea * 5 + at, 20, lane);
            u32x2 w;
            w[0] = pack_bf16(gelu_tanh(s[0] + bf_lo(bias[0])), gelu_tanh(s[1] + bf_hi(bias[0])));
            w[1] = pack_bf16(gelu_tanh(s[2] + bf_lo(bias[1])), gelu_tanh(s[3] + bf_hi(bias[1])));
            st8_dev(rws, ub + at * 1024, w);
          }
        }
        publish(ws, fl + FL_U + panel4 * 512 + ft * 4, 4, 128, tag, wave, lane);
      }
    }
    STAMP(10);
    {
      FRESH();
      const int tok = tb * 64 + ea * 16 + c16;
      const float bias = __uint_as_float((unsigned)b2[tok] << 16);
      if (wave >= 4) {
        const int r = lane >> 3, pos = lane & 7;
        const unsigned pe = (((pos >> 1) ^ ((r >> 1) & 1)) << 1 | (pos & 1)) * 16;
        const unsigned v0 = r * (S_ * 2) + pe + 4096, v1 = (r + 8) * (S_ * 2) + (pe ^ 64) + 3072, vstep = 16 * (S_ * 2) - 2048;
        const char* wsrc = (const char*)(w2 + tb * 64) - 4096 + (long)kq * 32 * (S_ * 2);
#pragma unroll 1
        for (int st = 4; st < 9; ++st) glds4<false>(wsrc + (long)st * 128 * (S_ * 2), lds0 + (st - 4) * 16384 + kq * 4096, v0, v1, v0 + vstep, v1 + vstep);
        asm volatile("s_barrier" ::: "memory");
        static_for<0, 14>([&](auto ST) {
          constexpr int st = decltype(ST)::v;
          constexpr int last = (st + 7 > 13) ? 13 : (st + 7 < 8 ? 8 : st + 7);
          wait_vbar<4 * (last - st)>();
          if (st >= 1 && st + 8 < 14) {
            const long o = (st + 8 == 13 && kq >= 2) ? (long)(2 * 13 - 1) * 64 * (S_ * 2) : (long)(st + 8) * 128 * (S_ * 2);
            glds4<false>(wsrc + o, lds0 + w2_slot(st + 8) + kq * 4096, v0, v1, v0 + vstep, v1 + vstep);
          }
        });
        asm volatile("s_barrier" ::: "memory");
      } else {
        if (wave == 0) poll_flags(ws, fl + FL_U + j * 512 + (tb & 3) * 128, 27, tag, lane, dead);
        asm volatile("s_barrier" ::: "memory");
        STAMP(11);
        const unsigned abase = WS_U + layer * UB + j * (U_KS * 5 * 1024) + kq * 5120 + lane * 16;
        const int q = c16 >> 2, p = lane & 3;
        const unsigned fsw = (((q >> 1) & 1) | ((g & 1) << 1)) * 32;
        const lds_cptr wrow = lds + (32 * kq + 8 * g + q) * 128 + 8 * p;
        u32x4 af[3][5];
#pragma unroll
        for (int st = 0; st < 3; ++st)
#pragma unroll
          for (int a = 0; a < 5; ++a) af[st][a] = ld16_dev_s(rws, abase + (a & 3) * 1024, st * 20480 + (a >> 2) * 4096);
        f32x4 acc[4][5];
#pragma unroll
        for (int i = 0; i < 4; ++i)
#pragma unroll
          for (int a = 0; a < 5; ++a) acc[i][a] = f32x4{0.f, 0.f, 0.f, 0.f};
        static_for<0, 14>([&](auto ST) {
          constexpr int st = decltype(ST)::v;
          bar_lds();
          if (st < 13 || kq < 2) {
            bf16x8 wf[4];
#pragma unroll
            for (int i = 0; i < 4; ++i) wf[i] = lds_tr8(wrow + w2_slot(st) + ((i * 32) ^ fsw), 512);
            SBAR();
#pragma unroll
            for (int a = 0; a < 5; ++a) {
              const bf16x8 x = __builtin_bit_cast(bf16x8, af[st % 3][a]);
#pragma unroll
              for (int i = 0; i < 4; ++i) acc[i][a] = MFMA16(x, wf[i], acc[i][a]);
              SBAR();
              if (st + 3 < 14) af[st % 3][a] = ld16_dev_s(rws, abase + (a & 3) * 1024, (st + 3) * 20480 + (a >> 2) * 4096);
              SBAR();
            }
          }
        });
        bar_lds();
#pragma unroll
        for (int i = 0; i < 4; ++i)
#pragma unroll
          for (int a = 0; a < 5; ++a) *(LAS f32x4*)(ldw + ((kq * 4 + i) * 5 + a) * 1024 + lane * 16) = acc[i][a];
      }
      bar_lds();
      STAMP(12);
#pragma unroll
      for (int i = 0; i < 3; ++i) {
        if (i < ewn) {
          const int at = ew0 + i;
          const f32x4 s = sum4(lds, ea * 5 + at, 20, lane);
          const u32x2 r = *(const LAS u32x2*)(lds + res_off + at * 32);
          u32x2 w;
          w[0] = pack_bf16(bf_lo(r[0]) + (s[0] + bias), bf_hi(r[0]) + (s[1] + bias));
          w[1] = pack_bf16(bf_lo(r[1]) + (s[2] + bias), bf_hi(r[1]) + (s[3] + bias));
          const int d = j * 80 + at * 16 + 4 * g;
          if (layer == 0) {
            *(LAS u32x2*)(ldw + res_off + at * 32) = w;
            st8_dev(rws, WS_X + tb * 81920 + (((d >> 5) * 4 + ea) * 64 + c16 + 16 * ((d & 31) >> 3)) * 16 + (d & 7) * 2, w);
          } else {
            *(u32x2*)(out + (long)tok * D_ + d) = w;
          }
        }
      }
      if (layer == 0) publish(ws, WS_FLAGS + FL_X + tb * 128 + j * 4, 1, 0, tag, wave, lane);
      STAMP(13);
    }
  }
  if (blockIdx.x == 0 && tid == 0) __hip_atomic_store((gu32*)(ws + WS_EPOCH), tag, RLX_AGENT);
}

extern "C" void kernel_launch(void* const* d_in, const int* in_sizes, int n_in, void* d_out, int out_size, void* d_ws,
                              size_t ws_size, hipStream_t stream) {
  (void)hipFuncSetAttribute((const void*)mega_kernel, hipFuncAttributeMaxDynamicSharedMemorySize, LDS_BYTES);
  mega_kernel<<<256, 512, LDS_BYTES, stream>>>((const u16*)d_in[0], (const u16*)d_in[1], (const u16*)d_in[2], (const u16*)d_in[3],
                                               (const u16*)d_in[4], (const u16*)d_in[6], (const u16*)d_in[7], (const u16*)d_in[8],
                                               (const u16*)d_in[9], (u16*)d_out, (char*)d_ws);
}
```

```cpp
#include <hip/hip_runtime.h>
#include <hip/hip_bf16.h>
#include <stdint.h>

typedef __attribute__((ext_vector_type(8))) short bf16x8;
typedef __attribute__((ext_vector_type(4))) short bf16x4;
typedef __attribute__((ext_vector_type(16))) float f32x16;
typedef __attribute__((ext_vector_type(4))) float f32x4;
typedef __attribute__((ext_vector_type(2))) float f32x2;
typedef __attribute__((ext_vector_type(2))) __bf16 bf2_t;
typedef __attribute__((ext_vector_type(4))) unsigned u32x4;
typedef __attribute__((ext_vector_type(2))) unsigned u32x2;
typedef unsigned short u16;
#define LAS __attribute__((address_space(3)))
#define GAS __attribute__((address_space(1)))
typedef LAS const char* lds_cptr;
typedef LAS char* lds_ptr;
typedef GAS unsigned gu32;
typedef GAS unsigned long long gu64;
#define DI __device__ __forceinline__
#define RLX_AGENT __ATOMIC_RELAXED, __HIP_MEMORY_SCOPE_AGENT

constexpr int S_ = 2048, D_ = 640, DH_ = 20, F_ = 1728;
constexpr float LN_EPS = 1e-5f;
constexpr float QSCALE = 0.22360679774997896f * 1.4426950408889634f;

constexpr int KV_TILE = 2624, KV_KT = 1024, KV_V = 1280, KV_VS = 672;

constexpr unsigned WS_EPOCH = 0;
constexpr unsigned WS_FLAGS = 4096, FL_LAYER = 65536;
constexpr unsigned FL_KV = 0;
constexpr unsigned FL_O = 32768;
constexpr unsigned FL_X = 36864;
constexpr unsigned FL_H = 40960;
constexpr unsigned FL_U = 45056;
constexpr unsigned WS_DIAG = 262144;
constexpr unsigned WS_STAT = 524288, ST_LAYER = 524288;
constexpr unsigned WS_KV = WS_STAT + 2 * ST_LAYER, KV_LAYER = 196608;
constexpr unsigned XB = S_ * D_ * 2;
constexpr unsigned WS_O = WS_KV + 2 * KV_LAYER;
constexpr unsigned WS_H = WS_O + 2 * XB;
constexpr int U_KS = 56;
constexpr unsigned UB = 8 * U_KS * 5 * 1024;
constexpr unsigned WS_U = WS_H + 2 * XB;
constexpr unsigned WS_X = WS_U + 2 * UB;
constexpr unsigned WS_END = WS_X + XB;

constexpr int L_W = 0;
constexpr int L_WT = 81920;
constexpr int L_RING = 81920;
constexpr int L_ATT = 102400;
constexpr int L_QT = 81920;
constexpr int L_KVP = 128064;
constexpr int L_STG = 134656;
constexpr int L_RES = 151040;
constexpr int L_DUMP = 161280;
constexpr int LDS_BYTES = 163840;
constexpr unsigned SPIN_CAP = 1u << 21;

DI unsigned pack_bf16(float lo, float hi) {
  f32x2 v = {lo, hi};
  bf2_t b = __builtin_convertvector(v, bf2_t);
  return __builtin_bit_cast(unsigned, b);
}
DI float bf_lo(unsigned u) { return __uint_as_float(u << 16); }
DI float bf_hi(unsigned u) { return __uint_as_float(u & 0xffff0000u); }

#define GLDS_HEAD "s_mov_b32 m0, %1\n\ts_nop 4\n\t"
template <bool DEV> DI void glds1(const void* sbase, unsigned m0v, unsigned v0) {
  if (DEV) asm volatile(GLDS_HEAD "global_load_lds_dwordx4 %2, %0 sc1" ::"s"(sbase), "s"(m0v), "v"(v0) : "memory");
  else asm volatile(GLDS_HEAD "global_load_lds_dwordx4 %2, %0" ::"s"(sbase), "s"(m0v), "v"(v0) : "memory");
}
template <bool DEV> DI void glds4(const void* sbase, unsigned m0v, unsigned v0, unsigned v1, unsigned v2, unsigned v3) {
  if (DEV)
    asm volatile(GLDS_HEAD "global_load_lds_dwordx4 %2, %0 sc1\n\tglobal_load_lds_dwordx4 %3, %0 offset:1024 sc1\n\t"
                           "global_load_lds_dwordx4 %4, %0 offset:2048 sc1\n\tglobal_load_lds_dwordx4 %5, %0 offset:3072 sc1"
                 ::"s"(sbase), "s"(m0v), "v"(v0), "v"(v1), "v"(v2), "v"(v3) : "memory");
  else
    asm volatile(GLDS_HEAD "global_load_lds_dwordx4 %2, %0\n\tglobal_load_lds_dwordx4 %3, %0 offset:1024\n\t"
                           "global_load_lds_dwordx4 %4, %0 offset:2048\n\tglobal_load_lds_dwordx4 %5, %0 offset:3072"
                 ::"s"(sbase), "s"(m0v), "v"(v0), "v"(v1), "v"(v2), "v"(v3) : "memory");
}
template <int N, bool DEV> DI void glds_lin(const void* sbase, unsigned m0v, unsigned v) {
  if (N == 1) glds1<DEV>(sbase, m0v, v);
  else if (N == 4) glds4<DEV>(sbase, m0v, v, v, v, v);
  else if (N == 2) {
    if (DEV) asm volatile(GLDS_HEAD "global_load_lds_dwordx4 %2, %0 sc1\n\tglobal_load_lds_dwordx4 %2, %0 offset:1024 sc1" ::"s"(sbase), "s"(m0v), "v"(v) : "memory");
    else asm volatile(GLDS_HEAD "global_load_lds_dwordx4 %2, %0\n\tglobal_load_lds_dwordx4 %2, %0 offset:1024" ::"s"(sbase), "s"(m0v), "v"(v) : "memory");
  } else {
    if (DEV) asm volatile(GLDS_HEAD "global_load_lds_dwordx4 %2, %0 sc1\n\tglobal_load_lds_dwordx4 %2, %0 offset:1024 sc1\n\tglobal_load_lds_dwordx4 %2, %0 offset:2048 sc1" ::"s"(sbase), "s"(m0v), "v"(v) : "memory");
    else asm volatile(GLDS_HEAD "global_load_lds_dwordx4 %2, %0\n\tglobal_load_lds_dwordx4 %2, %0 offset:1024\n\tglobal_load_lds_dwordx4 %2, %0 offset:2048" ::"s"(sbase), "s"(m0v), "v"(v) : "memory");
  }
}
DI void glds2(const void* sbase, unsigned m0v, unsigned v0, unsigned v1) {
  asm volatile(GLDS_HEAD "global_load_lds_dwordx4 %2, %0\n\tglobal_load_lds_dwordx4 %3, %0 offset:1024" ::"s"(sbase), "s"(m0v), "v"(v0), "v"(v1) : "memory");
}
#define UNI(x) __builtin_amdgcn_readfirstlane(x)
template <int N> DI void wait_bar() { asm volatile("s_waitcnt vmcnt(%0) lgkmcnt(0)\n\ts_barrier" ::"i"(N) : "memory"); }
template <int N> DI void wait_vbar() { asm volatile("s_waitcnt vmcnt(%0)\n\ts_barrier" ::"i"(N) : "memory"); }
DI void bar_lds() { asm volatile("s_waitcnt lgkmcnt(0)\n\ts_barrier" ::: "memory"); }
typedef short v4i16_t __attribute__((ext_vector_type(4)));
DI bf16x4 lds_tr(lds_cptr p) {
  return __builtin_bit_cast(bf16x4, __builtin_amdgcn_ds_read_tr16_b64_v4i16((LAS v4i16_t*)p));
}
DI bf16x8 lds_tr8(lds_cptr p, int step) {
  const bf16x4 lo = lds_tr(p), hi = lds_tr(p + step);
  return __builtin_shufflevector(lo, hi, 0, 1, 2, 3, 4, 5, 6, 7);
}
DI bf16x8 lds_b128(lds_cptr p) { return *(const LAS bf16x8*)p; }
#define MFMA16(a, b, c) __builtin_amdgcn_mfma_f32_16x16x32_bf16((a), (b), (c), 0, 0, 0)
#define MFMA32(a, b, c) __builtin_amdgcn_mfma_f32_32x32x16_bf16((a), (b), (c), 0, 0, 0)
#define MFMA32K8(a, b, c) __builtin_amdgcn_mfma_f32_32x32x8bf16_1k((a), (b), (c), 0, 0, 0)
#define SBAR() __builtin_amdgcn_sched_barrier(0)
#define PIN(x) asm volatile("" : "+v"(x))
typedef __amdgpu_buffer_rsrc_t rsrc_t;
DI void st8_dev(rsrc_t r, unsigned off, u32x2 v) { __builtin_amdgcn_raw_buffer_store_b64(v, r, off, 0, 16); }
DI u32x4 ld16_dev(rsrc_t r, unsigned off) { return __builtin_amdgcn_raw_buffer_load_b128(r, off, 0, 16); }
DI u32x4 ld16_dev_s(rsrc_t r, unsigned off, unsigned soff) { return __builtin_amdgcn_raw_buffer_load_b128(r, off, soff, 16); }

DI void poll_flags(const char* ws, unsigned off, int n, unsigned tag, int lane, bool& dead) {
  gu32* f = (gu32*)(ws + off);
  unsigned spins = 0;
  while (!dead) {
    unsigned v = tag;
    if (lane < n) v = __hip_atomic_load(f + lane, RLX_AGENT);
    if (__all(v == tag)) break;
    if (++spins > SPIN_CAP) dead = true;
    __builtin_amdgcn_s_sleep(1);
  }
  asm volatile("" ::: "memory");
}
DI void publish(const char* ws, unsigned off, int nrep, unsigned rstride, unsigned tag, int wave, int lane) {
  wait_bar<0>();
  if (wave == 0 && lane < nrep) __hip_atomic_store((gu32*)(ws + off + lane * rstride), tag, RLX_AGENT);
}

DI float fmax3(float a, float b, float c) { return __builtin_fmaxf(__builtin_fmaxf(a, b), c); }
DI float rowmax16(const f32x16& s) {
  float a = fmax3(s[0], s[1], s[2]), b = fmax3(s[3], s[4], s[5]);
  a = fmax3(a, s[6], s[7]); b = fmax3(b, s[8], s[9]); a = fmax3(a, s[10], s[11]); b = fmax3(b, s[12], s[13]);
  a = fmax3(a, s[14], s[15]); a = __builtin_fmaxf(a, b);
  return __builtin_fmaxf(a, __shfl_xor(a, 32));
}
DI void pack_p(const f32x16& p, bf16x8& lo, bf16x8& hi) {
  u32x4 a, b;
#pragma unroll
  for (int i = 0; i < 4; ++i) { a[i] = pack_bf16(p[2 * i], p[2 * i + 1]); b[i] = pack_bf16(p[8 + 2 * i], p[9 + 2 * i]); }
  lo = __builtin_bit_cast(bf16x8, a); hi = __builtin_bit_cast(bf16x8, b);
}
DI float gelu_tanh(float u) {
  const float z = 0.7978845608028654f * (u + 0.044715f * u * u * u);
  const float e = __builtin_amdgcn_exp2f(-2.885390081777927f * z);
  return u * __builtin_amdgcn_rcpf(1.f + e);
}

DI void wslice_block(const u16* w, unsigned lds0, int lw, int lane, int blk) {
  const char* src = (const char*)w + lw * (160 * 1280);
  if (blk < 5) {
    const int r = lane >> 3, pos = lane & 7;
    const unsigned pe = (((pos >> 1) ^ ((r >> 1) & 1)) << 1 | (pos & 1)) * 16;
    const unsigned v0 = r * 1280 + pe, v1 = r * 1280 + (pe ^ 64) + 9216;
    glds4<false>(src + blk * (32 * 1280), lds0 + L_W + lw * 20480 + blk * 4096, v0, v1, v0 + 18432, v1 + 18432);
  } else {
    const unsigned t0 = (lane >> 1) * 1280 + 128 + (lane & 1) * 16;
    if (blk == 5) glds4<false>(src, lds0 + L_WT + lw * 5120, t0, t0 + 39936, t0 + 2 * 39936, t0 + 3 * 39936);
    else glds1<false>(src + 128 * 1280, lds0 + L_WT + lw * 5120 + 4096, t0);
  }
}

DI float xsum16(float x) {
  auto r = __builtin_amdgcn_permlane16_swap(__float_as_uint(x), __float_as_uint(x), false, false);
  return __uint_as_float(r[0]) + __uint_as_float(r[1]);
}
DI float xsum32(float x) {
  auto r = __builtin_amdgcn_permlane32_swap(__float_as_uint(x), __float_as_uint(x), false, false);
  return __uint_as_float(r[0]) + __uint_as_float(r[1]);
}
DI f32x4 ldf4(lds_cptr p) { return *(const LAS f32x4*)p; }

DI f32x4 sum4(lds_cptr lds, int t, int nt, int lane) {
  f32x4 s = *(const LAS f32x4*)(lds + t * 1024 + lane * 16);
#pragma unroll
  for (int k = 1; k < 4; ++k) s += *(const LAS f32x4*)(lds + (k * nt + t) * 1024 + lane * 16);
  return s;
}
DI void ln_finish(float s1, float s2, float& mu, float& var) { mu = s1 * (1.f / D_); var = fmaxf(s2 * (1.f / D_) - mu * mu, 0.f); }

constexpr int ATT_CH = 4;
constexpr int ATT_CHB = ATT_CH * KV_TILE;
constexpr int ATT_SLOT = 10752;
constexpr int ATT_NCH = S_ / 32 / ATT_CH;
constexpr int w2_slot(int st) { return (st % 9) < 4 ? L_RING + (st % 9) * 16384 : ((st % 9) - 4) * 16384; }
template <int N> struct IC { static constexpr int v = N; };
template <int I, int N, class F> DI void static_for(F&& f) { if constexpr (I < N) { f(IC<I>{}); static_for<I + 1, N>(f); } }

#define STAMP(i) do { } while (0)

__global__ __launch_bounds__(512, 2) void mega_kernel(const u16* __restrict__ x0, const u16* __restrict__ wq, const u16* __restrict__ wk,
                                                      const u16* __restrict__ wv, const u16* __restrict__ wo, const u16* __restrict__ w1,
                                                      const u16* __restrict__ b1, const u16* __restrict__ w2, const u16* __restrict__ b2,
                                                      u16* __restrict__ out, char* ws) {
  extern __shared__ __attribute__((aligned(16))) char smem[];
  const int tid = threadIdx.x;
  const unsigned lds0 = (unsigned)(uintptr_t)smem;
  const lds_cptr lds = (lds_cptr)smem;
  const lds_ptr ldw = (lds_ptr)smem;
#define FRESH() \
  int lane = tid & 63; asm volatile("" : "+v"(lane)); \
  int wave = UNI(tid >> 6); asm volatile("" : "+s"(wave)); \
  int bid = blockIdx.x; asm volatile("" : "+s"(bid)); \
  const int xcd = bid & 7, idx = bid >> 3, tb = xcd * 4 + (idx >> 3), j = idx & 7; \
  const int g = lane >> 4, c16 = lane & 15, kq = wave & 3, th = wave >> 2; \
  const int ea = wave & 3, ew0 = th ? 3 : 0, ewn = th ? 2 : 3; \
  const unsigned res_off = L_RES + (ea * 16 + c16) * 160 + 8 * g; \
  const int panel4 = (xcd & 1) * 4 + (idx & 3), ft = (xcd >> 1) * 7 + (idx >> 2); \
  const bool has4 = idx < 28 && ft < 27; \
  (void)xcd; (void)g; (void)c16; (void)kq; (void)th; (void)ea; (void)ew0; (void)ewn; (void)res_off; (void)tb; (void)j; (void)panel4; (void)has4
  const rsrc_t rws = __builtin_amdgcn_make_buffer_rsrc(ws, 0, WS_END, 0x00020000);
  unsigned tag = 0;
  bool dead = false;

#pragma unroll 1
  for (int layer = 0; layer < 2; ++layer) {
    const unsigned fl = WS_FLAGS + layer * FL_LAYER;
    const unsigned kvoff = WS_KV + layer * KV_LAYER;
    {
      FRESH();
      STAMP(0);
      if (wave >= 4) {
        if (layer > 0) {
#pragma unroll 1
          for (int blk = 0; blk < 7; ++blk) wslice_block(wq + j * 80, lds0, wave - 4, lane, blk);
        }
      } else {
        {
          const char* xs = (const char*)((j & 1) ? wv : wk);
          if (wave < 3) {
            glds_lin<4, false>(xs + wave * 8192, lds0 + L_ATT + wave * 8192, lane * 16);
            glds_lin<4, false>(xs + wave * 8192 + 4096, lds0 + L_ATT + wave * 8192 + 4096, lane * 16);
          } else glds1<false>(xs + 24576, lds0 + L_ATT + 24576, lane * 16);
        }
        if (layer == 0) {
#pragma unroll 1
          for (int pc = wave; pc < 10; pc += 4) {
            const int n = pc * 64 + lane, row = n / 10, ch = n - row * 10;
            glds1<false>(x0 + (long)tb * 64 * D_ + j * 80, lds0 + L_RES + pc * 1024, row * 1280 + ch * 16);
          }
        }
      }
      rsrc_t ra = rws; unsigned abase, astr_t, astr_k;
      if (layer == 0) {
        ra = __builtin_amdgcn_make_buffer_rsrc((void*)x0, 0, XB, 0x00020000);
        abase = (tb * 64 + th * 32 + c16) * (D_ * 2) + kq * 64 + g * 16; astr_t = 16 * D_ * 2; astr_k = 256;
      } else {
        if (wave == 0) poll_flags(ws, WS_FLAGS + FL_X + tb * 128, 8, tag, lane, dead);
        wait_vbar<0>();
        abase = WS_X + tb * 81920 + (kq * 4 + th * 2) * 1024 + lane * 16; astr_t = 1024; astr_k = 16384;
      }
      STAMP(1);
      u32x4 af[5][2];
#pragma unroll
      for (int s = 0; s < 5; ++s)
#pragma unroll
        for (int t = 0; t < 2; ++t) af[s][t] = ld16_dev_s(ra, abase, s * astr_k + t * astr_t);
      if (layer == 0) {
        const unsigned epoch = __hip_atomic_load((gu32*)(ws + WS_EPOCH), RLX_AGENT);
        wait_vbar<0>();
        tag = UNI(epoch) + 1u;
        if (wave >= 4) {
#pragma unroll 1
          for (int blk = 0; blk < 7; ++blk) wslice_block(wq + j * 80, lds0, wave - 4, lane, blk);
        }
      }
      bf16x8 ones;
#pragma unroll
      for (int i = 0; i < 8; ++i) ones[i] = (short)0x3f80;
      const f32x4 z4 = {0.f, 0.f, 0.f, 0.f};
      const int q = c16 >> 2, p = lane & 3;
      const int row = 32 * kq + 8 * g + q;
      const unsigned fsw = (((q >> 1) & 1) | ((g & 1) << 1)) * 32;
      const unsigned woff = L_W + row * 128 + 8 * p, toff = L_WT + row * 32 + 8 * p, xoff = L_ATT + row * 40 + 8 * p;
      LAS float* stat = (LAS float*)(ldw + L_DUMP);
      LAS float* csum = (LAS float*)(ldw + L_KVP + 20480);
      const int tk = j >> 1;
      const bool isv = j & 1;
      {
        f32x4 a1s[2] = {z4, z4}, a2s[2] = {z4, z4}, akv[2] = {z4, z4}, aok[2] = {z4, z4};
#pragma unroll
        for (int s = 0; s < 5; ++s) {
          const bf16x8 a0 = __builtin_bit_cast(bf16x8, af[s][0]), a1 = __builtin_bit_cast(bf16x8, af[s][1]);
          if (th == (tk >> 1)) {
            const bf16x8 a = (tk & 1) ? a1 : a0;
            const bf16x8 x0f = lds_tr8(lds + xoff + s * 5120, 160), x1f = lds_tr8(lds + xoff + s * 5120 + 32, 160);
            if (!isv) {
              akv[0] = MFMA16(x0f, a, akv[0]); akv[1] = MFMA16(x1f, a, akv[1]);
              aok[0] = MFMA16(x0f, ones, aok[0]); aok[1] = MFMA16(x1f, ones, aok[1]);
            } else {
              akv[0] = MFMA16(a, x0f, akv[0]); akv[1] = MFMA16(a, x1f, akv[1]);
              aok[0] = MFMA16(ones, x0f, aok[0]); aok[1] = MFMA16(ones, x1f, aok[1]);
            }
          }
          a1s[0] = MFMA16(ones, a0, a1s[0]); a1s[1] = MFMA16(ones, a1, a1s[1]);
          a2s[0] = MFMA16(a0, a0, a2s[0]); a2s[1] = MFMA16(a1, a1, a2s[1]);
        }
        if (g == q) {
#pragma unroll
          for (int t = 0; t < 2; ++t) {
            const float d = (p & 2) ? ((p & 1) ? a2s[t][3] : a2s[t][2]) : ((p & 1) ? a2s[t][1] : a2s[t][0]);
            *(LAS f32x2*)(stat + (kq * 64 + th * 32 + t * 16 + c16) * 2) = f32x2{a1s[t][0], d};
          }
        }
        if (th == (tk >> 1)) {
          const bool l2 = isv ? (c16 < 4) : (g == 0);
          const int i2 = isv ? g * 4 + c16 : c16;
          const int o = L_KVP + kq * 2560;
          *(LAS f32x4*)(ldw + o + lane * 16) = akv[0];
          *(LAS f32x4*)(ldw + o + 1280 + lane * 16) = aok[0];
          if (l2) { *(LAS f32x4*)(ldw + o + 1024 + i2 * 16) = akv[1]; *(LAS f32x4*)(ldw + o + 2304 + i2 * 16) = aok[1]; }
        }
      }
      wait_bar<0>();
      float mu, rs;
      {
        f32x2 sv[4];
#pragma unroll
        for (int k = 0; k < 4; ++k) sv[k] = *(const LAS f32x2*)(stat + (k * 64 + ea * 16 + c16) * 2);
        float var; ln_finish((sv[0][0] + sv[1][0]) + (sv[2][0] + sv[3][0]), (sv[0][1] + sv[1][1]) + (sv[2][1] + sv[3][1]), mu, var);
        rs = rsqrtf(var + LN_EPS);
      }
      if (ea == tk) {
        const int li = th == 0 ? lane : 64 + (isv ? g * 4 + (c16 & 3) : c16);
        f32x4 s = z4, cs = z4;
#pragma unroll
        for (int k = 0; k < 4; ++k) { s += ldf4(lds + L_KVP + k * 2560 + li * 16); cs += ldf4(lds + L_KVP + k * 2560 + 1280 + li * 16); }
        const unsigned tile = kvoff + (tb * 2 + (ea >> 1)) * KV_TILE;
        if (!isv) {
          u32x2 w; w[0] = pack_bf16((s[0] - mu * cs[0]) * rs, (s[1] - mu * cs[1]) * rs); w[1] = pack_bf16((s[2] - mu * cs[2]) * rs, (s[3] - mu * cs[3]) * rs);
          const int r32 = (ea & 1) * 16 + c16;
          if (th == 0) st8_dev(rws, tile + ((g >> 1) * 32 + r32) * 16 + (g & 1) * 8, w);
          else if (g == 0) st8_dev(rws, tile + KV_KT + r32 * 8, w);
        } else {
          float o[4];
#pragma unroll
          for (int e = 0; e < 4; ++e) o[e] = (s[e] - __shfl(mu, 4 * g + e) * cs[e]) * __shfl(rs, 4 * g + e);
          const int d = th * 16 + c16;
          u32x2 w; w[0] = pack_bf16(o[0], o[1]); w[1] = pack_bf16(o[2], o[3]);
          if (d == DH_) { w[0] = 0x3f803f80u; w[1] = 0x3f803f80u; }
          if (d <= DH_) st8_dev(rws, tile + KV_V + (ea & 1) * KV_VS + ((g & 1) * 21 + d) * 16 + (g >> 1) * 8, w);
        }
      }
      f32x4 acc[5][2], aon[3] = {z4, z4, z4};
#pragma unroll
      for (int i = 0; i < 5; ++i) { acc[i][0] = z4; acc[i][1] = z4; }
#pragma unroll
      for (int s = 0; s < 5; ++s) {
        const bf16x8 a0 = __builtin_bit_cast(bf16x8, af[s][0]), a1 = __builtin_bit_cast(bf16x8, af[s][1]);
        bf16x8 wf[5];
#pragma unroll
        for (int i = 0; i < 4; ++i) wf[i] = lds_tr8(lds + woff + s * 16384 + ((i * 32) ^ fsw), 512);
        wf[4] = lds_tr8(lds + toff + s * 4096, 128);
#pragma unroll
        for (int i = 0; i < 5; ++i) { acc[i][0] = MFMA16(wf[i], a0, acc[i][0]); acc[i][1] = MFMA16(wf[i], a1, acc[i][1]); }
        if (th == 0) { aon[0] = MFMA16(wf[0], ones, aon[0]); aon[1] = MFMA16(wf[1], ones, aon[1]); aon[2] = MFMA16(wf[2], ones, aon[2]); }
        else { aon[0] = MFMA16(wf[3], ones, aon[0]); aon[1] = MFMA16(wf[4], ones, aon[1]); }
        if (s == 1) publish(ws, fl + FL_KV + (tb * 8 + j) * 4, 32, 1024, tag, wave, lane);
      }
      bar_lds();
#pragma unroll
      for (int i = 0; i < 5; ++i)
#pragma unroll
        for (int t = 0; t < 2; ++t) *(LAS f32x4*)(ldw + ((kq * 5 + i) * 4 + th * 2 + t) * 1024 + lane * 16) = acc[i][t];
      if (c16 == 0) {
#pragma unroll
        for (int i = 0; i < 3; ++i) if (i < ewn) *(LAS f32x4*)(csum + (kq * 7 + ew0 + i) * 16 + 4 * g) = aon[i];
      }
      bar_lds();
      {
        const float r = rs * QSCALE;
        f32x4 s[3], cs[3];
#pragma unroll
        for (int i = 0; i < 3; ++i) {
          const int wt = min(ew0 + i, 4);
          s[i] = sum4(lds, wt * 4 + ea, 20, lane);
          cs[i] = z4;
#pragma unroll
          for (int k = 0; k < 4; ++k) cs[i] += *(const LAS f32x4*)(csum + (k * 7 + wt) * 16 + 4 * g);
        }
#pragma unroll
        for (int i = 0; i < 3; ++i) {
          if (i < ewn) {
            u32x2 w;
            w[0] = pack_bf16((s[i][0] - mu * cs[i][0]) * r, (s[i][1] - mu * cs[i][1]) * r);
            w[1] = pack_bf16((s[i][2] - mu * cs[i][2]) * r, (s[i][3] - mu * cs[i][3]) * r);
            *(LAS u32x2*)(ldw + L_QT + (ea * 16 + c16) * 160 + (ew0 + i) * 32 + 8 * g) = w;
          }
        }
      }
      bar_lds();
    }
    STAMP(2);
    bf16x8 w1r[16][2];
    {
      FRESH();
      const int r32 = lane & 31, hi = lane >> 5;
      const int hh = wave & 3, trow = th * 32 + r32;
      bf16x8 q16; bf16x4 q4;
      {
        const lds_cptr qp = lds + L_QT + trow * 160 + hh * 40;
        const u32x2 a = *(const LAS u32x2*)(qp + hi * 16), b = *(const LAS u32x2*)(qp + hi * 16 + 8), c = *(const LAS u32x2*)(qp + 32);
        u32x4 v = {a[0], a[1], b[0], b[1]};
        q16 = __builtin_bit_cast(bf16x8, v);
        u32x2 t = {hi ? 0u : c[0], hi ? 0u : c[1]};
        q4 = __builtin_bit_cast(bf16x4, t);
        PIN(q16); PIN(q4);
      }
      if (wave == 0) {
        unsigned spins = 0;
        while (!dead) {
          const u32x4 v = ld16_dev(rws, fl + FL_KV + (bid & 31) * 1024 + lane * 16);
          if (__all(v[0] == tag && v[1] == tag && v[2] == tag && v[3] == tag)) break;
          if (++spins > SPIN_CAP) dead = true;
          __builtin_amdgcn_s_sleep(1);
          asm volatile("" ::: "memory");
        }
      }
      bar_lds();
      STAMP(3);
      const unsigned koff = lane * 16;
      const char* KV = ws + kvoff + wave * 3072;
      auto issue_kv = [&](int ch) {
        const unsigned dst = lds0 + L_ATT + (ch % 3) * ATT_SLOT + wave * 3072;
        if (wave < 3) glds_lin<3, true>(KV + ch * ATT_CHB, dst, koff);
        else { glds1<true>(KV + ch * ATT_CHB, dst, koff); if (lane < 16) glds1<true>(KV + ch * ATT_CHB + 1024, dst + 1024, koff); }
      };
      const int ftc = has4 ? ft : 0;
      const char* w1s = (const char*)(w1 + ftc * 64 + th * 32) + (long)kq * 32 * (F_ * 2);
      const unsigned sv0 = (lane >> 2) * (F_ * 2) + ((lane & 3) ^ ((lane >> 4) & 2)) * 16, sv1 = sv0 + 16 * (F_ * 2) - 1024;
      const unsigned stg = L_STG + wave * 2048;
      const unsigned srd = stg + (8 * g + (c16 >> 2)) * 64 + (g & 1) * 32 + 8 * (lane & 3);
      const char* w1w = (const char*)(w1 + ftc * 64) + (long)((idx & 3) * 32 + (wave & 3) * 8) * (F_ * 2);
      const unsigned wv1 = (lane >> 3) * (F_ * 2) + (lane & 7) * 16;
      const char* w2w = (const char*)(w2 + tb * 64) + (long)(j * 216) * (S_ * 2);
      const unsigned wv2 = (lane >> 3) * (S_ * 2) + (lane & 7) * 16;
      if (wave < 4) { issue_kv(0); issue_kv(1); }
      const unsigned ktoff = KV_KT + r32 * 8, voff = KV_V + (hi * 21 + (r32 < 20 ? r32 : 20)) * 16;
      f32x16 o = {}, negm, sA, sB;
      bf16x8 k16, v0, v1; bf16x4 k4; u32x4 p0, p1;
      if (wave < 3) wait_bar<3>(); else if (wave == 3) wait_bar<2>(); else wait_bar<0>();
      const lds_cptr ring = lds + L_ATT;
      {
        k16 = lds_b128(ring + koff);
        k4 = *(const LAS bf16x4*)(ring + ktoff);
        f32x16 z = {};
        z = MFMA32(k16, q16, z); z = MFMA32K8(k4, q4, z);
        k16 = lds_b128(ring + KV_TILE + koff);
        k4 = *(const LAS bf16x4*)(ring + KV_TILE + ktoff);
        v1 = lds_b128(ring + voff);
        const float m = rowmax16(z);
#pragma unroll
        for (int i = 0; i < 16; ++i) { negm[i] = -m; sA[i] = z[i] - m; }
        p1 = u32x4{0u, 0u, 0u, 0u};
      }
#define EX(x) __builtin_amdgcn_exp2f(x)
#define EXP4(X, i) do { X[i] = EX(X[i]); X[i + 1] = EX(X[i + 1]); X[i + 2] = EX(X[i + 2]); X[i + 3] = EX(X[i + 3]); } while (0)
#define BF8(x) __builtin_bit_cast(bf16x8, x)
#define PK4(P, X, i) do { P[0] = pack_bf16(X[i], X[i + 1]); P[1] = pack_bf16(X[i + 2], X[i + 3]); P[2] = pack_bf16(X[i + 4], X[i + 5]); P[3] = pack_bf16(X[i + 6], X[i + 7]); } while (0)
#define STEP(C, N, kn, vc) do { \
    N = MFMA32(k16, q16, negm); SBAR(); \
    v0 = lds_b128((vc) + voff); EXP4(C, 0); PIN(C); SBAR(); \
    o = MFMA32(v1, BF8(p1), o); SBAR(); \
    k16 = lds_b128((kn) + koff); EXP4(C, 4); PIN(C); SBAR(); PK4(p0, C, 0); PIN(p0); SBAR(); \
    N = MFMA32K8(k4, q4, N); SBAR(); \
    v1 = lds_b128((vc) + voff + KV_VS); EXP4(C, 8); PIN(C); SBAR(); \
    o = MFMA32(v0, BF8(p0), o); SBAR(); \
    k4 = *(const LAS bf16x4*)((kn) + ktoff); EXP4(C, 12); PIN(C); SBAR(); PK4(p1, C, 8); PIN(p1); SBAR(); \
  } while (0)
      static_for<0, ATT_NCH>([&](auto CH) {
        constexpr int ch = decltype(CH)::v;
        const lds_cptr cb = ring + (ch % 3) * ATT_SLOT;
        const lds_cptr nb = (ch + 1 < ATT_NCH) ? ring + ((ch + 1) % 3) * ATT_SLOT : cb;
        STEP(sA, sB, cb + 2 * KV_TILE, cb);
        STEP(sB, sA, cb + 3 * KV_TILE, cb + KV_TILE);
        constexpr int pe = (ch == 0) ? 0 : ((ch - 1 < 6 ? 4 : (ch - 1 == 6 ? 1 : 0)) + (ch - 1 + 3 < 16 ? 1 : 0) + ((ch - 1 >= 8 && ch - 1 < 15) ? 1 : 0));
        if (wave < 4) wait_vbar<pe>(); else wait_vbar<0>();
        if constexpr (ch > 0) { w1r[ch - 1][0] = lds_tr8(lds + srd, 256); w1r[ch - 1][1] = lds_tr8(lds + (srd ^ 32), 256); }
        SBAR();
        STEP(sA, sB, nb, cb + 2 * KV_TILE);
        if constexpr (ch > 0) { PIN(w1r[ch - 1][0]); PIN(w1r[ch - 1][1]); }
        if (ch + 2 < ATT_NCH) { if (wave < 4) issue_kv(ch + 2); }
        glds2(w1s + (long)ch * 128 * (F_ * 2), lds0 + stg, sv0, sv1);
        if (wave < 4) {
          if (ch < 7) wslice_block(wo + j * 80, lds0, wave, lane, ch);
          if (ch + 3 < 16) glds1<false>(w1w + (long)(ch + 3) * 128 * (F_ * 2), lds0 + L_DUMP, wv1);
          if (ch >= 8 && ch < 15) { const int pc = (ch - 8) * 4 + wave; glds1<false>(w2w + (long)(pc < 27 ? pc : 26) * 8 * (S_ * 2), lds0 + L_DUMP, wv2); }
        }
        SBAR();
        STEP(sB, sA, nb + KV_TILE, cb + 3 * KV_TILE);
      });
      o = MFMA32(v1, BF8(p1), o);
      float l = hi ? o[8] : o[12];
      bool bad = !(l < 1.8446744e19f);
#pragma unroll
      for (int i = 0; i < 12; ++i) bad |= !(__builtin_fabsf(o[i]) < 3.0e38f);
      if (__builtin_expect(__any(bad), 0)) {
        float m = -INFINITY;
        o = f32x16{};
#pragma unroll 1
        for (int t = 0; t < S_ / 32; ++t) {
          const unsigned tp = kvoff + t * KV_TILE;
          const bf16x8 a16 = BF8(ld16_dev(rws, tp + koff));
          const u32x2 a4 = __builtin_amdgcn_raw_buffer_load_b64(rws, tp + ktoff, 0, 16);
          const bf16x8 w0 = BF8(ld16_dev(rws, tp + voff)), w1f = BF8(ld16_dev(rws, tp + voff + KV_VS));
          f32x16 z = {};
          z = MFMA32(a16, q16, z); z = MFMA32K8(__builtin_bit_cast(bf16x4, a4), q4, z);
          const float mn = __builtin_fmaxf(m, rowmax16(z));
          const float alpha = __builtin_amdgcn_exp2f(m - mn);
          m = mn;
#pragma unroll
          for (int i = 0; i < 16; ++i) { z[i] = __builtin_amdgcn_exp2f(z[i] - m); o[i] *= alpha; }
          bf16x8 a0, a1; pack_p(z, a0, a1);
          o = MFMA32(w0, a0, o); o = MFMA32(w1f, a1, o);
        }
        l = hi ? o[8] : o[12];
      }
      const float rl = 1.0f / l;
      const unsigned ob = WS_O + layer * XB + tb * 81920 + ((trow >> 4) * 64 + (trow & 15)) * 16;
#pragma unroll
      for (int gq = 0; gq < 3; ++gq) {
        const int d0 = 8 * gq + 4 * hi;
        if (d0 < DH_) {
          const int k = (j * 4 + hh) * DH_ + d0;
          u32x2 w; w[0] = pack_bf16(o[4 * gq] * rl, o[4 * gq + 1] * rl); w[1] = pack_bf16(o[4 * gq + 2] * rl, o[4 * gq + 3] * rl);
          st8_dev(rws, ob + (k >> 5) * 4096 + ((k & 31) >> 3) * 256 + (k & 7) * 2, w);
        }
      }
      STAMP(4);
      publish(ws, fl + FL_O + tb * 128 + j * 4, 1, 0, tag, wave, lane);
      w1r[15][0] = lds_tr8(lds + srd, 256); w1r[15][1] = lds_tr8(lds + (srd ^ 32), 256);
    }
    {
      FRESH();
      if (wave == 0) poll_flags(ws, fl + FL_O + tb * 128, 8, tag, lane, dead);
      asm volatile("s_barrier" ::: "memory");
      STAMP(5);
      const unsigned abase = WS_O + layer * XB + tb * 81920 + (kq * 4 + th * 2) * 1024 + lane * 16;
      u32x4 af[5][2];
#pragma unroll
      for (int s = 0; s < 5; ++s)
#pragma unroll
        for (int t = 0; t < 2; ++t) af[s][t] = ld16_dev_s(rws, abase + t * 1024, s * 16384);
      f32x4 acc[5][2];
#pragma unroll
      for (int i = 0; i < 5; ++i) { acc[i][0] = f32x4{0.f, 0.f, 0.f, 0.f}; acc[i][1] = acc[i][0]; }
      const int q = c16 >> 2, p = lane & 3;
      const int row = 32 * kq + 8 * g + q;
      const unsigned fsw = (((q >> 1) & 1) | ((g & 1) << 1)) * 32;
      const unsigned woff = L_W + row * 128 + 8 * p, toff = L_WT + row * 32 + 8 * p;
      bf16x8 wf[2][5];
#pragma unroll
      for (int i = 0; i < 5; ++i) wf[0][i] = i < 4 ? lds_tr8(lds + woff + ((i * 32) ^ fsw), 512) : lds_tr8(lds + toff, 128);
#pragma unroll
      for (int s = 0; s < 5; ++s) {
        const bf16x8 a0 = __builtin_bit_cast(bf16x8, af[s][0]), a1 = __builtin_bit_cast(bf16x8, af[s][1]);
#pragma unroll
        for (int i = 0; i < 5; ++i) {
          if (s < 4) wf[(s + 1) & 1][i] = i < 4 ? lds_tr8(lds + woff + (s + 1) * 16384 + ((i * 32) ^ fsw), 512) : lds_tr8(lds + toff + (s + 1) * 4096, 128);
          acc[i][0] = MFMA16(wf[s & 1][i], a0, acc[i][0]); acc[i][1] = MFMA16(wf[s & 1][i], a1, acc[i][1]);
          SBAR();
        }
      }
      bar_lds();
#pragma unroll
      for (int i = 0; i < 5; ++i)
#pragma unroll
        for (int t = 0; t < 2; ++t) *(LAS f32x4*)(ldw + ((kq * 5 + i) * 4 + th * 2 + t) * 1024 + lane * 16) = acc[i][t];
      bar_lds();
      gu64* gr = (gu64*)(ws + WS_STAT + layer * ST_LAYER + (tb * 4 + ea) * 4096);
      {
        f32x4 s[3]; u32x2 r[3];
#pragma unroll
        for (int i = 0; i < 3; ++i) {
          const int wt = min(ew0 + i, 4);
          s[i] = sum4(lds, wt * 4 + ea, 20, lane);
          r[i] = *(const LAS u32x2*)(lds + res_off + wt * 32);
        }
        float s1 = 0.f, s2 = 0.f;
#pragma unroll
        for (int i = 0; i < 3; ++i) {
          if (i < ewn) {
            u32x2 w;
            w[0] = pack_bf16(bf_lo(r[i][0]) + s[i][0], bf_hi(r[i][0]) + s[i][1]);
            w[1] = pack_bf16(bf_lo(r[i][1]) + s[i][2], bf_hi(r[i][1]) + s[i][3]);
            *(LAS u32x2*)(ldw + res_off + (ew0 + i) * 32) = w;
            const float a = bf_lo(w[0]), b = bf_hi(w[0]), c = bf_lo(w[1]), d = bf_hi(w[1]);
            s1 += (a + b) + (c + d);
            s2 += (a * a + b * b) + (c * c + d * d);
          }
        }
        s1 = xsum32(xsum16(s1)); s2 = xsum32(xsum16(s2));
        if (lane < 32) __hip_atomic_store(gr + (2 * j + th) * 32 + lane, ((unsigned long long)tag << 32) | __float_as_uint(g ? s2 : s1), RLX_AGENT);
      }
      bar_lds();
      if (wave >= 4) {
        const int r = lane >> 3, pos = lane & 7;
        const unsigned pe = (((pos >> 1) ^ ((r >> 1) & 1)) << 1 | (pos & 1)) * 16;
        const unsigned v0 = r * (S_ * 2) + pe + 4096, v1 = (r + 8) * (S_ * 2) + (pe ^ 64) + 3072, vstep = 16 * (S_ * 2) - 2048;
        const char* wsrc = (const char*)(w2 + tb * 64) - 4096 + (long)kq * 32 * (S_ * 2);
#pragma unroll 1
        for (int st = 0; st < 4; ++st) glds4<false>(wsrc + (long)st * 128 * (S_ * 2), lds0 + L_RING + st * 16384 + kq * 4096, v0, v1, v0 + vstep, v1 + vstep);
        asm volatile("s_barrier" ::: "memory");
      } else {
        u32x2 xv[5];
#pragma unroll
        for (int wt = 0; wt < 5; ++wt) xv[wt] = __builtin_bit_cast(u32x2, lds_tr(lds + L_RES + (ea * 16 + 4 * g + q) * 160 + wt * 32 + 8 * p));
        float tot;
        {
          unsigned spins = 0;
          for (;;) {
            bool ok = true; tot = 0.f;
#pragma unroll
            for (int i = 0; i < 8; ++i) {
              const unsigned long long v = __hip_atomic_load(gr + i * 64 + lane, RLX_AGENT);
              tot += __uint_as_float((unsigned)v); ok &= (unsigned)(v >> 32) == tag;
            }
            if (__all(ok) || dead) break;
            if (++spins > SPIN_CAP) dead = true;
            __builtin_amdgcn_s_sleep(1);
          }
        }
        STAMP(6);
        tot = xsum32(tot);
        float mu4[4], rr4[4];
#pragma unroll
        for (int e = 0; e < 4; ++e) { mu4[e] = __shfl(tot, 4 * g + e); rr4[e] = __shfl(tot, 16 + 4 * g + e); }
#pragma unroll
        for (int e = 0; e < 4; ++e) {
          float var; ln_finish(mu4[e], rr4[e], mu4[e], var);
          const float r1 = rsqrtf(var + LN_EPS);
          rr4[e] = r1 * rsqrtf(var * r1 * r1 + LN_EPS);
        }
        const unsigned hb = WS_H + layer * XB + j * 327680 + ((tb * 2 + (ea >> 1)) * 5) * 1024 + (c16 + 16 * ((ea & 1) * 2 + (g >> 1))) * 16 + (g & 1) * 8;
#pragma unroll
        for (int wt = 0; wt < 5; ++wt) {
          const u32x2 v = xv[wt];
          u32x2 w;
          w[0] = pack_bf16((bf_lo(v[0]) - mu4[0]) * rr4[0], (bf_hi(v[0]) - mu4[1]) * rr4[1]);
          w[1] = pack_bf16((bf_lo(v[1]) - mu4[2]) * rr4[2], (bf_hi(v[1]) - mu4[3]) * rr4[3]);
          st8_dev(rws, hb + wt * 1024, w);
        }
        publish(ws, fl + FL_H + j * 512 + tb * 4, 4, 128, tag, wave, lane);
      }
      STAMP(7);
    }
    {
      FRESH();
      if (has4) {
        const int f = ft * 64 + ea * 16 + 4 * g;
        const u32x2 bias = *(const u32x2*)(b1 + f);
        if (wave == 0) poll_flags(ws, fl + FL_H + panel4 * 512 + ((idx >> 2) & 3) * 128, 32, tag, lane, dead);
        asm volatile("s_barrier" ::: "memory");
        STAMP(8);
        const unsigned l16 = lane * 16;
        const int po = (th ? 12 + 2 * kq : 3 * kq) * 1024;
        const char* hsrc = ws + WS_H + layer * XB + panel4 * 327680 + po;
        auto issue = [&](int st) {
          if (th == 0) glds_lin<3, true>(hsrc + st * 20480, lds0 + (st & 3) * 20480 + po, l16);
          else glds_lin<2, true>(hsrc + st * 20480, lds0 + (st & 3) * 20480 + po, l16);
        };
        issue(0); issue(1); issue(2);
        f32x4 acc[2][5];
#pragma unroll
        for (int i = 0; i < 2; ++i)
#pragma unroll
          for (int a = 0; a < 5; ++a) acc[i][a] = f32x4{0.f, 0.f, 0.f, 0.f};
        const lds_cptr ab = lds + kq * 5120 + l16;
        static_for<0, 16>([&](auto ST) {
          constexpr int st = decltype(ST)::v;
          constexpr int ahead = (15 - st) < 2 ? (15 - st) : 2;
          if (th == 0) wait_bar<3 * ahead>(); else wait_bar<2 * ahead>();
          bf16x8 af[5];
#pragma unroll
          for (int a = 0; a < 5; ++a) af[a] = lds_b128(ab + (st & 3) * 20480 + a * 1024);
          if (st + 3 < 16) { if (th == 0) issue(st + 3); }
          SBAR();
#pragma unroll
          for (int a = 0; a < 5; ++a) { acc[0][a] = MFMA16(w1r[st][0], af[a], acc[0][a]); acc[1][a] = MFMA16(w1r[st][1], af[a], acc[1][a]); }
          SBAR();
          if (st + 3 < 16) { if (th == 1) issue(st + 3); }
        });
        bar_lds();
#pragma unroll
        for (int i = 0; i < 2; ++i)
#pragma unroll
          for (int a = 0; a < 5; ++a) *(LAS f32x4*)(ldw + ((kq * 4 + 2 * th + i) * 5 + a) * 1024 + l16) = acc[i][a];
        bar_lds();
        STAMP(9);
        const unsigned ub = WS_U + layer * UB + ((panel4 * U_KS + (f >> 5)) * 5) * 1024 + (c16 + 16 * ((f & 31) >> 3)) * 16 + (f & 7) * 2;
        f32x4 sv[3];
#pragma unroll
        for (int i = 0; i < 3; ++i) sv[i] = sum4(lds, ea * 5 + min(ew0 + i, 4), 20, lane);
#pragma unroll
        for (int i = 0; i < 3; ++i) {
          if (i < ewn) {
            const f32x4 s = sv[i];
            u32x2 w;
            w[0] = pack_bf16(gelu_tanh(s[0] + bf_lo(bias[0])), gelu_tanh(s[1] + bf_hi(bias[0])));
            w[1] = pack_bf16(gelu_tanh(s[2] + bf_lo(bias[1])), gelu_tanh(s[3] + bf_hi(bias[1])));
            st8_dev(rws, ub + (ew0 + i) * 1024, w);
          }
        }
        publish(ws, fl + FL_U + panel4 * 512 + ft * 4, 4, 128, tag, wave, lane);
      }
    }
    STAMP(10);
    {
      FRESH();
      const int tok = tb * 64 + ea * 16 + c16;
      const float bias = __uint_as_float((unsigned)b2[tok] << 16);
      if (wave >= 4) {
        const int r = lane >> 3, pos = lane & 7;
        const unsigned pe = (((pos >> 1) ^ ((r >> 1) & 1)) << 1 | (pos & 1)) * 16;
        const unsigned v0 = r * (S_ * 2) + pe + 4096, v1 = (r + 8) * (S_ * 2) + (pe ^ 64) + 3072, vstep = 16 * (S_ * 2) - 2048;
        const char* wsrc = (const char*)(w2 + tb * 64) - 4096 + (long)kq * 32 * (S_ * 2);
#pragma unroll 1
        for (int st = 4; st < 9; ++st) glds4<false>(wsrc + (long)st * 128 * (S_ * 2), lds0 + (st - 4) * 16384 + kq * 4096, v0, v1, v0 + vstep, v1 + vstep);
        asm volatile("s_barrier" ::: "memory");
        static_for<0, 14>([&](auto ST) {
          constexpr int st = decltype(ST)::v;
          constexpr int last = (st + 7 > 13) ? 13 : (st + 7 < 8 ? 8 : st + 7);
          wait_vbar<4 * (last - st)>();
          if (st >= 1 && st + 8 < 14) {
            const long o = (st + 8 == 13 && kq >= 2) ? (long)(2 * 13 - 1) * 64 * (S_ * 2) : (long)(st + 8) * 128 * (S_ * 2);
            glds4<false>(wsrc + o, lds0 + w2_slot(st + 8) + kq * 4096, v0, v1, v0 + vstep, v1 + vstep);
          }
        });
        asm volatile("s_barrier" ::: "memory");
      } else {
        if (wave == 0) poll_flags(ws, fl + FL_U + j * 512 + (tb & 3) * 128, 27, tag, lane, dead);
        asm volatile("s_barrier" ::: "memory");
        STAMP(11);
        const unsigned abase = WS_U + layer * UB + j * (U_KS * 5 * 1024) + kq * 5120 + lane * 16;
        const int q = c16 >> 2, p = lane & 3;
        const unsigned fsw = (((q >> 1) & 1) | ((g & 1) << 1)) * 32;
        const lds_cptr wrow = lds + (32 * kq + 8 * g + q) * 128 + 8 * p;
        u32x4 af[3][5];
#pragma unroll
        for (int st = 0; st < 3; ++st)
#pragma unroll
          for (int a = 0; a < 5; ++a) af[st][a] = ld16_dev_s(rws, abase + (a & 3) * 1024, st * 20480 + (a >> 2) * 4096);
        f32x4 acc[4][5];
#pragma unroll
        for (int i = 0; i < 4; ++i)
#pragma unroll
          for (int a = 0; a < 5; ++a) acc[i][a] = f32x4{0.f, 0.f, 0.f, 0.f};
        static_for<0, 14>([&](auto ST) {
          constexpr int st = decltype(ST)::v;
          bar_lds();
          if (st < 13 || kq < 2) {
            bf16x8 wf[4];
#pragma unroll
            for (int i = 0; i < 4; ++i) wf[i] = lds_tr8(wrow + w2_slot(st) + ((i * 32) ^ fsw), 512);
            SBAR();
#pragma unroll
            for (int a = 0; a < 5; ++a) {
              const bf16x8 x = __builtin_bit_cast(bf16x8, af[st % 3][a]);
#pragma unroll
              for (int i = 0; i < 4; ++i) acc[i][a] = MFMA16(x, wf[i], acc[i][a]);
              SBAR();
              if (st + 3 < 14) af[st % 3][a] = ld16_dev_s(rws, abase + (a & 3) * 1024, (st + 3) * 20480 + (a >> 2) * 4096);
              SBAR();
            }
          }
        });
        bar_lds();
#pragma unroll
        for (int i = 0; i < 4; ++i)
#pragma unroll
          for (int a = 0; a < 5; ++a) *(LAS f32x4*)(ldw + ((kq * 4 + i) * 5 + a) * 1024 + lane * 16) = acc[i][a];
      }
      bar_lds();
      STAMP(12);
      f32x4 sv[3]; u32x2 rv[3];
#pragma unroll
      for (int i = 0; i < 3; ++i) { const int at = min(ew0 + i, 4); sv[i] = sum4(lds, ea * 5 + at, 20, lane); rv[i] = *(const LAS u32x2*)(lds + res_off + at * 32); }
#pragma unroll
      for (int i = 0; i < 3; ++i) {
        if (i < ewn) {
          const int at = ew0 + i;
          const f32x4 s = sv[i];
          const u32x2 r = rv[i];
          u32x2 w;
          w[0] = pack_bf16(bf_lo(r[0]) + (s[0] + bias), bf_hi(r[0]) + (s[1] + bias));
          w[1] = pack_bf16(bf_lo(r[1]) + (s[2] + bias), bf_hi(r[1]) + (s[3] + bias));
          const int d = j * 80 + at * 16 + 4 * g;
          if (layer == 0) {
            *(LAS u32x2*)(ldw + res_off + at * 32) = w;
            st8_dev(rws, WS_X + tb * 81920 + (((d >> 5) * 4 + ea) * 64 + c16 + 16 * ((d & 31) >> 3)) * 16 + (d & 7) * 2, w);
          } else {
            *(u32x2*)(out + (long)tok * D_ + d) = w;
          }
        }
      }
      if (layer == 0) publish(ws, WS_FLAGS + FL_X + tb * 128 + j * 4, 1, 0, tag, wave, lane);
      STAMP(13);
    }
  }
  if (blockIdx.x == 0 && tid == 0) __hip_atomic_store((gu32*)(ws + WS_EPOCH), tag, RLX_AGENT);
}

extern "C" void kernel_launch(void* const* d_in, const int* in_sizes, int n_in, void* d_out, int out_size, void* d_ws,
                              size_t ws_size, hipStream_t stream) {
  (void)hipFuncSetAttribute((const void*)mega_kernel, hipFuncAttributeMaxDynamicSharedMemorySize, LDS_BYTES);
  mega_kernel<<<256, 512, LDS_BYTES, stream>>>((const u16*)d_in[0], (const u16*)d_in[1], (const u16*)d_in[2], (const u16*)d_in[3],
                                               (const u16*)d_in[4], (const u16*)d_in[6], (const u16*)d_in[7], (const u16*)d_in[8],
                                               (const u16*)d_in[9], (u16*)d_out, (char*)d_ws);
}
```

```cpp
#include <hip/hip_runtime.h>
#include <hip/hip_bf16.h>
#include <stdint.h>

typedef __attribute__((ext_vector_type(8))) short bf16x8;
typedef __attribute__((ext_vector_type(4))) short bf16x4;
typedef __attribute__((ext_vector_type(16))) float f32x16;
typedef __attribute__((ext_vector_type(4))) float f32x4;
typedef __attribute__((ext_vector_type(2))) float f32x2;
typedef __attribute__((ext_vector_type(2))) __bf16 bf2_t;
typedef __attribute__((ext_vector_type(4))) unsigned u32x4;
typedef __attribute__((ext_vector_type(2))) unsigned u32x2;
typedef unsigned short u16;
#define LAS __attribute__((address_space(3)))
#define GAS __attribute__((address_space(1)))
typedef LAS const char* lds_cptr;
typedef LAS char* lds_ptr;
typedef GAS unsigned gu32;
typedef GAS unsigned long long gu64;
#define DI __device__ __forceinline__
#define RLX_AGENT __ATOMIC_RELAXED, __HIP_MEMORY_SCOPE_AGENT

constexpr int S_ = 2048, D_ = 640, DH_ = 20, F_ = 1728;
constexpr float LN_EPS = 1e-5f;
constexpr float QSCALE = 0.22360679774997896f * 1.4426950408889634f;

constexpr int KV_TILE = 2624, KV_KT = 1024, KV_V = 1280, KV_VS = 672;

constexpr unsigned WS_EPOCH = 0;
constexpr unsigned WS_FLAGS = 4096, FL_LAYER = 65536;
constexpr unsigned FL_KV = 0;
constexpr unsigned FL_O = 32768;
constexpr unsigned FL_X = 36864;
constexpr unsigned FL_H = 40960;
constexpr unsigned FL_U = 45056;
constexpr unsigned WS_DIAG = 262144;
constexpr unsigned WS_STAT = 524288, ST_LAYER = 524288;
constexpr unsigned WS_KV = WS_STAT + 2 * ST_LAYER, KV_LAYER = 196608;
constexpr unsigned XB = S_ * D_ * 2;
constexpr unsigned WS_O = WS_KV + 2 * KV_LAYER;
constexpr unsigned WS_H = WS_O + 2 * XB;
constexpr int U_KS = 56;
constexpr unsigned UB = 8 * U_KS * 5 * 1024;
constexpr unsigned WS_U = WS_H + 2 * XB;
constexpr unsigned WS_X = WS_U + 2 * UB;
constexpr unsigned WS_END = WS_X + XB;

constexpr int L_W = 0;
constexpr int L_WT = 81920;
constexpr int L_RING = 81920;
constexpr int L_ATT = 102400;
constexpr int L_QT = 81920;
constexpr int L_KVP = 128064;
constexpr int L_STG = 134656;
constexpr int L_RES = 151040;
constexpr int L_DUMP = 161280;
constexpr int LDS_BYTES = 163840;
constexpr unsigned SPIN_CAP = 1u << 21;

DI unsigned pack_bf16(float lo, float hi) {
  f32x2 v = {lo, hi};
  bf2_t b = __builtin_convertvector(v, bf2_t);
  return __builtin_bit_cast(unsigned, b);
}
DI float bf_lo(unsigned u) { return __uint_as_float(u << 16); }
DI float bf_hi(unsigned u) { return __uint_as_float(u & 0xffff0000u); }
DI f32x4 rnd4(f32x4 v) {
  const unsigned a = pack_bf16(v[0], v[1]), b = pack_bf16(v[2], v[3]);
  return f32x4{bf_lo(a), bf_hi(a), bf_lo(b), bf_hi(b)};
}

#define GLDS_HEAD "s_mov_b32 m0, %1\n\ts_nop 4\n\t"
template <bool DEV> DI void glds1(const void* sbase, unsigned m0v, unsigned v0) {
  if (DEV) asm volatile(GLDS_HEAD "global_load_lds_dwordx4 %2, %0 sc1" ::"s"(sbase), "s"(m0v), "v"(v0) : "memory");
  else asm volatile(GLDS_HEAD "global_load_lds_dwordx4 %2, %0" ::"s"(sbase), "s"(m0v), "v"(v0) : "memory");
}
template <bool DEV> DI void glds4(const void* sbase, unsigned m0v, unsigned v0, unsigned v1, unsigned v2, unsigned v3) {
  if (DEV)
    asm volatile(GLDS_HEAD "global_load_lds_dwordx4 %2, %0 sc1\n\tglobal_load_lds_dwordx4 %3, %0 offset:1024 sc1\n\t"
                           "global_load_lds_dwordx4 %4, %0 offset:2048 sc1\n\tglobal_load_lds_dwordx4 %5, %0 offset:3072 sc1"
                 ::"s"(sbase), "s"(m0v), "v"(v0), "v"(v1), "v"(v2), "v"(v3) : "memory");
  else
    asm volatile(GLDS_HEAD "global_load_lds_dwordx4 %2, %0\n\tglobal_load_lds_dwordx4 %3, %0 offset:1024\n\t"
                           "global_load_lds_dwordx4 %4, %0 offset:2048\n\tglobal_load_lds_dwordx4 %5, %0 offset:3072"
                 ::"s"(sbase), "s"(m0v), "v"(v0), "v"(v1), "v"(v2), "v"(v3) : "memory");
}
template <int N, bool DEV> DI void glds_lin(const void* sbase, unsigned m0v, unsigned v) {
  if (N == 1) glds1<DEV>(sbase, m0v, v);
  else if (N == 4) glds4<DEV>(sbase, m0v, v, v, v, v);
  else if (N == 2) {
    if (DEV) asm volatile(GLDS_HEAD "global_load_lds_dwordx4 %2, %0 sc1\n\tglobal_load_lds_dwordx4 %2, %0 offset:1024 sc1" ::"s"(sbase), "s"(m0v), "v"(v) : "memory");
    else asm volatile(GLDS_HEAD "global_load_lds_dwordx4 %2, %0\n\tglobal_load_lds_dwordx4 %2, %0 offset:1024" ::"s"(sbase), "s"(m0v), "v"(v) : "memory");
  } else {
    if (DEV) asm volatile(GLDS_HEAD "global_load_lds_dwordx4 %2, %0 sc1\n\tglobal_load_lds_dwordx4 %2, %0 offset:1024 sc1\n\tglobal_load_lds_dwordx4 %2, %0 offset:2048 sc1" ::"s"(sbase), "s"(m0v), "v"(v) : "memory");
    else asm volatile(GLDS_HEAD "global_load_lds_dwordx4 %2, %0\n\tglobal_load_lds_dwordx4 %2, %0 offset:1024\n\tglobal_load_lds_dwordx4 %2, %0 offset:2048" ::"s"(sbase), "s"(m0v), "v"(v) : "memory");
  }
}
DI void glds2(const void* sbase, unsigned m0v, unsigned v0, unsigned v1) {
  asm volatile(GLDS_HEAD "global_load_lds_dwordx4 %2, %0\n\tglobal_load_lds_dwordx4 %3, %0 offset:1024" ::"s"(sbase), "s"(m0v), "v"(v0), "v"(v1) : "memory");
}
#define UNI(x) __builtin_amdgcn_readfirstlane(x)
template <int N> DI void wait_bar() { asm volatile("s_waitcnt vmcnt(%0) lgkmcnt(0)\n\ts_barrier" ::"i"(N) : "memory"); }
template <int N> DI void wait_vbar() { asm volatile("s_waitcnt vmcnt(%0)\n\ts_barrier" ::"i"(N) : "memory"); }
DI void bar_lds() { asm volatile("s_waitcnt lgkmcnt(0)\n\ts_barrier" ::: "memory"); }
typedef short v4i16_t __attribute__((ext_vector_type(4)));
DI bf16x4 lds_tr(lds_cptr p) {
  return __builtin_bit_cast(bf16x4, __builtin_amdgcn_ds_read_tr16_b64_v4i16((LAS v4i16_t*)p));
}
DI bf16x8 lds_tr8(lds_cptr p, int step) {
  const bf16x4 lo = lds_tr(p), hi = lds_tr(p + step);
  return __builtin_shufflevector(lo, hi, 0, 1, 2, 3, 4, 5, 6, 7);
}
DI bf16x8 lds_b128(lds_cptr p) { return *(const LAS bf16x8*)p; }
#define MFMA16(a, b, c) __builtin_amdgcn_mfma_f32_16x16x32_bf16((a), (b), (c), 0, 0, 0)
#define MFMA32(a, b, c) __builtin_amdgcn_mfma_f32_32x32x16_bf16((a), (b), (c), 0, 0, 0)
#define MFMA32K8(a, b, c) __builtin_amdgcn_mfma_f32_32x32x8bf16_1k((a), (b), (c), 0, 0, 0)
#define SBAR() __builtin_amdgcn_sched_barrier(0)
#define PIN(x) asm volatile("" : "+v"(x))
typedef __amdgpu_buffer_rsrc_t rsrc_t;
DI void st8_dev(rsrc_t r, unsigned off, u32x2 v) { __builtin_amdgcn_raw_buffer_store_b64(v, r, off, 0, 16); }
DI u32x4 ld16_dev(rsrc_t r, unsigned off) { return __builtin_amdgcn_raw_buffer_load_b128(r, off, 0, 16); }
DI u32x4 ld16_dev_s(rsrc_t r, unsigned off, unsigned soff) { return __builtin_amdgcn_raw_buffer_load_b128(r, off, soff, 16); }

DI void poll_flags(const char* ws, unsigned off, int n, unsigned tag, int lane, bool& dead) {
  gu32* f = (gu32*)(ws + off);
  unsigned spins = 0;
  while (!dead) {
    unsigned v = tag;
    if (lane < n) v = __hip_atomic_load(f + lane, RLX_AGENT);
    if (__all(v == tag)) break;
    if (++spins > SPIN_CAP) dead = true;
    __builtin_amdgcn_s_sleep(1);
  }
  asm volatile("" ::: "memory");
}
DI void publish(const char* ws, unsigned off, int nrep, unsigned rstride, unsigned tag, int wave, int lane) {
  wait_bar<0>();
  if (wave == 0 && lane < nrep) __hip_atomic_store((gu32*)(ws + off + lane * rstride), tag, RLX_AGENT);
}

DI float fmax3(float a, float b, float c) { return __builtin_fmaxf(__builtin_fmaxf(a, b), c); }
DI float rowmax16(const f32x16& s) {
  float a = fmax3(s[0], s[1], s[2]), b = fmax3(s[3], s[4], s[5]);
  a = fmax3(a, s[6], s[7]); b = fmax3(b, s[8], s[9]); a = fmax3(a, s[10], s[11]); b = fmax3(b, s[12], s[13]);
  a = fmax3(a, s[14], s[15]); a = __builtin_fmaxf(a, b);
  return __builtin_fmaxf(a, __shfl_xor(a, 32));
}
DI void pack_p(const f32x16& p, bf16x8& lo, bf16x8& hi) {
  u32x4 a, b;
#pragma unroll
  for (int i = 0; i < 4; ++i) { a[i] = pack_bf16(p[2 * i], p[2 * i + 1]); b[i] = pack_bf16(p[8 + 2 * i], p[9 + 2 * i]); }
  lo = __builtin_bit_cast(bf16x8, a); hi = __builtin_bit_cast(bf16x8, b);
}
DI float gelu_tanh(float u) {
  const float z = 0.7978845608028654f * (u + 0.044715f * u * u * u);
  const float e = __builtin_amdgcn_exp2f(-2.885390081777927f * z);
  return u * __builtin_amdgcn_rcpf(1.f + e);
}

DI void wslice_block(const u16* w, unsigned lds0, int lw, int lane, int blk) {
  const char* src = (const char*)w + lw * (160 * 1280);
  if (blk < 5) {
    const int r = lane >> 3, pos = lane & 7;
    const unsigned pe = (((pos >> 1) ^ ((r >> 1) & 1)) << 1 | (pos & 1)) * 16;
    const unsigned v0 = r * 1280 + pe, v1 = r * 1280 + (pe ^ 64) + 9216;
    glds4<false>(src + blk * (32 * 1280), lds0 + L_W + lw * 20480 + blk * 4096, v0, v1, v0 + 18432, v1 + 18432);
  } else {
    const unsigned t0 = (lane >> 1) * 1280 + 128 + (lane & 1) * 16;
    if (blk == 5) glds4<false>(src, lds0 + L_WT + lw * 5120, t0, t0 + 39936, t0 + 2 * 39936, t0 + 3 * 39936);
    else glds1<false>(src + 128 * 1280, lds0 + L_WT + lw * 5120 + 4096, t0);
  }
}

DI float xsum16(float x) {
  auto r = __builtin_amdgcn_permlane16_swap(__float_as_uint(x), __float_as_uint(x), false, false);
  return __uint_as_float(r[0]) + __uint_as_float(r[1]);
}
DI float xsum32(float x) {
  auto r = __builtin_amdgcn_permlane32_swap(__float_as_uint(x), __float_as_uint(x), false, false);
  return __uint_as_float(r[0]) + __uint_as_float(r[1]);
}
DI f32x4 ldf4(lds_cptr p) { return *(const LAS f32x4*)p; }

DI f32x4 sum4(lds_cptr lds, int t, int nt, int lane) {
  f32x4 s = *(const LAS f32x4*)(lds + t * 1024 + lane * 16);
#pragma unroll
  for (int k = 1; k < 4; ++k) s += *(const LAS f32x4*)(lds + (k * nt + t) * 1024 + lane * 16);
  return s;
}
DI void ln_finish(float s1, float s2, float& mu, float& var) { mu = s1 * (1.f / D_); var = fmaxf(s2 * (1.f / D_) - mu * mu, 0.f); }

constexpr int ATT_CH = 4;
constexpr int ATT_CHB = ATT_CH * KV_TILE;
constexpr int ATT_SLOT = 10752;
constexpr int ATT_NCH = S_ / 32 / ATT_CH;
constexpr int w2_slot(int st) { return (st % 9) < 4 ? L_RING + (st % 9) * 16384 : ((st % 9) - 4) * 16384; }
template <int N> struct IC { static constexpr int v = N; };
template <int I, int N, class F> DI void static_for(F&& f) { if constexpr (I < N) { f(IC<I>{}); static_for<I + 1, N>(f); } }

#define STAMP(i) do { } while (0)

__global__ __launch_bounds__(512, 2) void mega_kernel(const u16* __restrict__ x0, const u16* __restrict__ wq, const u16* __restrict__ wk,
                                                      const u16* __restrict__ wv, const u16* __restrict__ wo, const u16* __restrict__ w1,
                                                      const u16* __restrict__ b1, const u16* __restrict__ w2, const u16* __restrict__ b2,
                                                      u16* __restrict__ out, char* ws) {
  extern __shared__ __attribute__((aligned(16))) char smem[];
  const int tid = threadIdx.x;
  const unsigned lds0 = (unsigned)(uintptr_t)smem;
  const lds_cptr lds = (lds_cptr)smem;
  const lds_ptr ldw = (lds_ptr)smem;
#define FRESH() \
  int lane = tid & 63; asm volatile("" : "+v"(lane)); \
  int wave = UNI(tid >> 6); asm volatile("" : "+s"(wave)); \
  int bid = blockIdx.x; asm volatile("" : "+s"(bid)); \
  const int xcd = bid & 7, idx = bid >> 3, tb = xcd * 4 + (idx >> 3), j = idx & 7; \
  const int g = lane >> 4, c16 = lane & 15, kq = wave & 3, th = wave >> 2; \
  const int ea = wave & 3, ew0 = th ? 3 : 0, ewn = th ? 2 : 3; \
  const unsigned res_off = L_RES + (ea * 16 + c16) * 160 + 8 * g; \
  const int panel4 = (xcd & 1) * 4 + (idx & 3), ft = (xcd >> 1) * 7 + (idx >> 2); \
  const bool has4 = idx < 28 && ft < 27; \
  (void)xcd; (void)g; (void)c16; (void)kq; (void)th; (void)ea; (void)ew0; (void)ewn; (void)res_off; (void)tb; (void)j; (void)panel4; (void)has4
  const rsrc_t rws = __builtin_amdgcn_make_buffer_rsrc(ws, 0, WS_END, 0x00020000);
  unsigned tag = 0;
  bool dead = false;

#pragma unroll 1
  for (int layer = 0; layer < 2; ++layer) {
    const unsigned fl = WS_FLAGS + layer * FL_LAYER;
    const unsigned kvoff = WS_KV + layer * KV_LAYER;
    {
      FRESH();
      STAMP(0);
      if (wave >= 4) {
        if (layer > 0) {
#pragma unroll 1
          for (int blk = 0; blk < 7; ++blk) wslice_block(wq + j * 80, lds0, wave - 4, lane, blk);
        }
      } else {
        {
          const char* xs = (const char*)((j & 1) ? wv : wk);
          if (wave < 3) {
            glds_lin<4, false>(xs + wave * 8192, lds0 + L_ATT + wave * 8192, lane * 16);
            glds_lin<4, false>(xs + wave * 8192 + 4096, lds0 + L_ATT + wave * 8192 + 4096, lane * 16);
          } else glds1<false>(xs + 24576, lds0 + L_ATT + 24576, lane * 16);
        }
        if (layer == 0) {
#pragma unroll 1
          for (int pc = wave; pc < 10; pc += 4) {
            const int n = pc * 64 + lane, row = n / 10, ch = n - row * 10;
            glds1<false>(x0 + (long)tb * 64 * D_ + j * 80, lds0 + L_RES + pc * 1024, row * 1280 + ch * 16);
          }
        }
      }
      rsrc_t ra = rws; unsigned abase, astr_t, astr_k;
      if (layer == 0) {
        ra = __builtin_amdgcn_make_buffer_rsrc((void*)x0, 0, XB, 0x00020000);
        abase = (tb * 64 + th * 32 + c16) * (D_ * 2) + kq * 64 + g * 16; astr_t = 16 * D_ * 2; astr_k = 256;
      } else {
        if (wave == 0) poll_flags(ws, WS_FLAGS + FL_X + tb * 128, 8, tag, lane, dead);
        wait_vbar<0>();
        abase = WS_X + tb * 81920 + (kq * 4 + th * 2) * 1024 + lane * 16; astr_t = 1024; astr_k = 16384;
      }
      STAMP(1);
      u32x4 af[5][2];
#pragma unroll
      for (int s = 0; s < 5; ++s)
#pragma unroll
        for (int t = 0; t < 2; ++t) af[s][t] = ld16_dev_s(ra, abase, s * astr_k + t * astr_t);
      if (layer == 0) {
        const unsigned epoch = __hip_atomic_load((gu32*)(ws + WS_EPOCH), RLX_AGENT);
        wait_vbar<0>();
        tag = UNI(epoch) + 1u;
        if (wave >= 4) {
#pragma unroll 1
          for (int blk = 0; blk < 7; ++blk) wslice_block(wq + j * 80, lds0, wave - 4, lane, blk);
        }
      }
      bf16x8 ones;
#pragma unroll
      for (int i = 0; i < 8; ++i) ones[i] = (short)0x3f80;
      const f32x4 z4 = {0.f, 0.f, 0.f, 0.f};
      const int q = c16 >> 2, p = lane & 3;
      const int row = 32 * kq + 8 * g + q;
      const unsigned fsw = (((q >> 1) & 1) | ((g & 1) << 1)) * 32;
      const unsigned woff = L_W + row * 128 + 8 * p, toff = L_WT + row * 32 + 8 * p, xoff = L_ATT + row * 40 + 8 * p;
      LAS float* stat = (LAS float*)(ldw + L_DUMP);
      LAS float* csum = (LAS float*)(ldw + L_KVP + 20480);
      const int tk = j >> 1;
      const bool isv = j & 1;
      {
        f32x4 a1s[2] = {z4, z4}, a2s[2] = {z4, z4}, akv[2] = {z4, z4}, aok[2] = {z4, z4};
#pragma unroll
        for (int s = 0; s < 5; ++s) {
          const bf16x8 a0 = __builtin_bit_cast(bf16x8, af[s][0]), a1 = __builtin_bit_cast(bf16x8, af[s][1]);
          if (th == (tk >> 1)) {
            const bf16x8 a = (tk & 1) ? a1 : a0;
            const bf16x8 x0f = lds_tr8(lds + xoff + s * 5120, 160), x1f = lds_tr8(lds + xoff + s * 5120 + 32, 160);
            if (!isv) {
              akv[0] = MFMA16(x0f, a, akv[0]); akv[1] = MFMA16(x1f, a, akv[1]);
              aok[0] = MFMA16(x0f, ones, aok[0]); aok[1] = MFMA16(x1f, ones, aok[1]);
            } else {
              akv[0] = MFMA16(a, x0f, akv[0]); akv[1] = MFMA16(a, x1f, akv[1]);
              aok[0] = MFMA16(ones, x0f, aok[0]); aok[1] = MFMA16(ones, x1f, aok[1]);
            }
          }
          a1s[0] = MFMA16(ones, a0, a1s[0]); a1s[1] = MFMA16(ones, a1, a1s[1]);
          a2s[0] = MFMA16(a0, a0, a2s[0]); a2s[1] = MFMA16(a1, a1, a2s[1]);
        }
        if (g == q) {
#pragma unroll
          for (int t = 0; t < 2; ++t) {
            const float d = (p & 2) ? ((p & 1) ? a2s[t][3] : a2s[t][2]) : ((p & 1) ? a2s[t][1] : a2s[t][0]);
            *(LAS f32x2*)(stat + (kq * 64 + th * 32 + t * 16 + c16) * 2) = f32x2{a1s[t][0], d};
          }
        }
        if (th == (tk >> 1)) {
          const bool l2 = isv ? (c16 < 4) : (g == 0);
          const int i2 = isv ? g * 4 + c16 : c16;
          const int o = L_KVP + kq * 2560;
          *(LAS f32x4*)(ldw + o + lane * 16) = akv[0];
          *(LAS f32x4*)(ldw + o + 1280 + lane * 16) = aok[0];
          if (l2) { *(LAS f32x4*)(ldw + o + 1024 + i2 * 16) = akv[1]; *(LAS f32x4*)(ldw + o + 2304 + i2 * 16) = aok[1]; }
        }
      }
      wait_bar<0>();
      float mu, rs;
      {
        f32x2 sv[4];
#pragma unroll
        for (int k = 0; k < 4; ++k) sv[k] = *(const LAS f32x2*)(stat + (k * 64 + ea * 16 + c16) * 2);
        float var; ln_finish((sv[0][0] + sv[1][0]) + (sv[2][0] + sv[3][0]), (sv[0][1] + sv[1][1]) + (sv[2][1] + sv[3][1]), mu, var);
        rs = rsqrtf(var + LN_EPS);
      }
      if (ea == tk) {
        const int li = th == 0 ? lane : 64 + (isv ? g * 4 + (c16 & 3) : c16);
        f32x4 s = z4, cs = z4;
#pragma unroll
        for (int k = 0; k < 4; ++k) { s += ldf4(lds + L_KVP + k * 2560 + li * 16); cs += ldf4(lds + L_KVP + k * 2560 + 1280 + li * 16); }
        const unsigned tile = kvoff + (tb * 2 + (ea >> 1)) * KV_TILE;
        if (!isv) {
          u32x2 w; w[0] = pack_bf16((s[0] - mu * cs[0]) * rs, (s[1] - mu * cs[1]) * rs); w[1] = pack_bf16((s[2] - mu * cs[2]) * rs, (s[3] - mu * cs[3]) * rs);
          const int r32 = (ea & 1) * 16 + c16;
          if (th == 0) st8_dev(rws, tile + ((g >> 1) * 32 + r32) * 16 + (g & 1) * 8, w);
          else if (g == 0) st8_dev(rws, tile + KV_KT + r32 * 8, w);
        } else {
          float o[4];
#pragma unroll
          for (int e = 0; e < 4; ++e) o[e] = (s[e] - __shfl(mu, 4 * g + e) * cs[e]) * __shfl(rs, 4 * g + e);
          const int d = th * 16 + c16;
          u32x2 w; w[0] = pack_bf16(o[0], o[1]); w[1] = pack_bf16(o[2], o[3]);
          if (d == DH_) { w[0] = 0x3f803f80u; w[1] = 0x3f803f80u; }
          if (d <= DH_) st8_dev(rws, tile + KV_V + (ea & 1) * KV_VS + ((g & 1) * 21 + d) * 16 + (g >> 1) * 8, w);
        }
      }
      f32x4 acc[5][2], aon[3] = {z4, z4, z4};
#pragma unroll
      for (int i = 0; i < 5; ++i) { acc[i][0] = z4; acc[i][1] = z4; }
#pragma unroll
      for (int s = 0; s < 5; ++s) {
        const bf16x8 a0 = __builtin_bit_cast(bf16x8, af[s][0]), a1 = __builtin_bit_cast(bf16x8, af[s][1]);
        bf16x8 wf[5];
#pragma unroll
        for (int i = 0; i < 4; ++i) wf[i] = lds_tr8(lds + woff + s * 16384 + ((i * 32) ^ fsw), 512);
        wf[4] = lds_tr8(lds + toff + s * 4096, 128);
#pragma unroll
        for (int i = 0; i < 5; ++i) { acc[i][0] = MFMA16(wf[i], a0, acc[i][0]); acc[i][1] = MFMA16(wf[i], a1, acc[i][1]); }
        if (th == 0) { aon[0] = MFMA16(wf[0], ones, aon[0]); aon[1] = MFMA16(wf[1], ones, aon[1]); aon[2] = MFMA16(wf[2], ones, aon[2]); }
        else { aon[0] = MFMA16(wf[3], ones, aon[0]); aon[1] = MFMA16(wf[4], ones, aon[1]); }
        if (s == 1) publish(ws, fl + FL_KV + (tb * 8 + j) * 4, 32, 1024, tag, wave, lane);
      }
      bar_lds();
#pragma unroll
      for (int i = 0; i < 5; ++i)
#pragma unroll
        for (int t = 0; t < 2; ++t) *(LAS f32x4*)(ldw + ((kq * 5 + i) * 4 + th * 2 + t) * 1024 + lane * 16) = acc[i][t];
      if (c16 == 0) {
#pragma unroll
        for (int i = 0; i < 3; ++i) if (i < ewn) *(LAS f32x4*)(csum + (kq * 7 + ew0 + i) * 16 + 4 * g) = aon[i];
      }
      bar_lds();
      {
        const float r = rs * QSCALE;
        f32x4 s[3], cs[3];
#pragma unroll
        for (int i = 0; i < 3; ++i) {
          const int wt = min(ew0 + i, 4);
          s[i] = sum4(lds, wt * 4 + ea, 20, lane);
          cs[i] = z4;
#pragma unroll
          for (int k = 0; k < 4; ++k) cs[i] += *(const LAS f32x4*)(csum + (k * 7 + wt) * 16 + 4 * g);
        }
#pragma unroll
        for (int i = 0; i < 3; ++i) {
          if (i < ewn) {
            u32x2 w;
            w[0] = pack_bf16((s[i][0] - mu * cs[i][0]) * r, (s[i][1] - mu * cs[i][1]) * r);
            w[1] = pack_bf16((s[i][2] - mu * cs[i][2]) * r, (s[i][3] - mu * cs[i][3]) * r);
            *(LAS u32x2*)(ldw + L_QT + (ea * 16 + c16) * 160 + (ew0 + i) * 32 + 8 * g) = w;
          }
        }
      }
      bar_lds();
    }
    STAMP(2);
    bf16x8 w1r[16][2];
    {
      FRESH();
      const int r32 = lane & 31, hi = lane >> 5;
      const int hh = wave & 3, trow = th * 32 + r32;
      bf16x8 q16; bf16x4 q4;
      {
        const lds_cptr qp = lds + L_QT + trow * 160 + hh * 40;
        const u32x2 a = *(const LAS u32x2*)(qp + hi * 16), b = *(const LAS u32x2*)(qp + hi * 16 + 8), c = *(const LAS u32x2*)(qp + 32);
        u32x4 v = {a[0], a[1], b[0], b[1]};
        q16 = __builtin_bit_cast(bf16x8, v);
        u32x2 t = {hi ? 0u : c[0], hi ? 0u : c[1]};
        q4 = __builtin_bit_cast(bf16x4, t);
        PIN(q16); PIN(q4);
      }
      if (wave == 0) {
        unsigned spins = 0;
        while (!dead) {
          const u32x4 v = ld16_dev(rws, fl + FL_KV + (bid & 31) * 1024 + lane * 16);
          if (__all(v[0] == tag && v[1] == tag && v[2] == tag && v[3] == tag)) break;
          if (++spins > SPIN_CAP) dead = true;
          __builtin_amdgcn_s_sleep(1);
          asm volatile("" ::: "memory");
        }
      }
      bar_lds();
      STAMP(3);
      const unsigned koff = lane * 16;
      const char* KV = ws + kvoff + wave * 3072;
      auto issue_kv = [&](int ch) {
        const unsigned dst = lds0 + L_ATT + (ch % 3) * ATT_SLOT + wave * 3072;
        if (wave < 3) glds_lin<3, true>(KV + ch * ATT_CHB, dst, koff);
        else { glds1<true>(KV + ch * ATT_CHB, dst, koff); if (lane < 16) glds1<true>(KV + ch * ATT_CHB + 1024, dst + 1024, koff); }
      };
      const int ftc = has4 ? ft : 0;
      const char* w1s = (const char*)(w1 + ftc * 64 + th * 32) + (long)kq * 32 * (F_ * 2);
      const unsigned sv0 = (lane >> 2) * (F_ * 2) + ((lane & 3) ^ ((lane >> 4) & 2)) * 16, sv1 = sv0 + 16 * (F_ * 2) - 1024;
      const unsigned stg = L_STG + wave * 2048;
      const unsigned srd = stg + (8 * g + (c16 >> 2)) * 64 + (g & 1) * 32 + 8 * (lane & 3);
      const char* w1w = (const char*)(w1 + ftc * 64) + (long)((idx & 3) * 32 + (wave & 3) * 8) * (F_ * 2);
      const unsigned wv1 = (lane >> 3) * (F_ * 2) + (lane & 7) * 16;
      const char* w2w = (const char*)(w2 + tb * 64) + (long)(j * 216) * (S_ * 2);
      const unsigned wv2 = (lane >> 3) * (S_ * 2) + (lane & 7) * 16;
      if (wave < 4) { issue_kv(0); issue_kv(1); }
      const unsigned ktoff = KV_KT + r32 * 8, voff = KV_V + (hi * 21 + (r32 < 20 ? r32 : 20)) * 16;
      f32x16 o = {}, negm, sA, sB;
      bf16x8 k16, v0, v1; bf16x4 k4; u32x4 p0, p1;
      if (wave < 3) wait_bar<3>(); else if (wave == 3) wait_bar<2>(); else wait_bar<0>();
      const lds_cptr ring = lds + L_ATT;
      {
        k16 = lds_b128(ring + koff);
        k4 = *(const LAS bf16x4*)(ring + ktoff);
        f32x16 z = {};
        z = MFMA32(k16, q16, z); z = MFMA32K8(k4, q4, z);
        k16 = lds_b128(ring + KV_TILE + koff);
        k4 = *(const LAS bf16x4*)(ring + KV_TILE + ktoff);
        v1 = lds_b128(ring + voff);
        const float m = rowmax16(z);
#pragma unroll
        for (int i = 0; i < 16; ++i) { negm[i] = -m; sA[i] = z[i] - m; }
        p1 = u32x4{0u, 0u, 0u, 0u};
      }
#define EX(x) __builtin_amdgcn_exp2f(x)
#define EXP4(X, i) do { X[i] = EX(X[i]); X[i + 1] = EX(X[i + 1]); X[i + 2] = EX(X[i + 2]); X[i + 3] = EX(X[i + 3]); } while (0)
#define BF8(x) __builtin_bit_cast(bf16x8, x)
#define PK4(P, X, i) do { P[0] = pack_bf16(X[i], X[i + 1]); P[1] = pack_bf16(X[i + 2], X[i + 3]); P[2] = pack_bf16(X[i + 4], X[i + 5]); P[3] = pack_bf16(X[i + 6], X[i + 7]); } while (0)
#define STEP(C, N, kn, vc) do { \
    N = MFMA32(k16, q16, negm); SBAR(); \
    v0 = lds_b128((vc) + voff); EXP4(C, 0); PIN(C); SBAR(); \
    o = MFMA32(v1, BF8(p1), o); SBAR(); \
    k16 = lds_b128((kn) + koff); EXP4(C, 4); PIN(C); SBAR(); PK4(p0, C, 0); PIN(p0); SBAR(); \
    N = MFMA32K8(k4, q4, N); SBAR(); \
    v1 = lds_b128((vc) + voff + KV_VS); EXP4(C, 8); PIN(C); SBAR(); \
    o = MFMA32(v0, BF8(p0), o); SBAR(); \
    k4 = *(const LAS bf16x4*)((kn) + ktoff); EXP4(C, 12); PIN(C); SBAR(); PK4(p1, C, 8); PIN(p1); SBAR(); \
  } while (0)
      static_for<0, ATT_NCH>([&](auto CH) {
        constexpr int ch = decltype(CH)::v;
        const lds_cptr cb = ring + (ch % 3) * ATT_SLOT;
        const lds_cptr nb = (ch + 1 < ATT_NCH) ? ring + ((ch + 1) % 3) * ATT_SLOT : cb;
        STEP(sA, sB, cb + 2 * KV_TILE, cb);
        STEP(sB, sA, cb + 3 * KV_TILE, cb + KV_TILE);
        constexpr int pe = (ch == 0) ? 0 : ((ch - 1 < 6 ? 4 : (ch - 1 == 6 ? 1 : 0)) + (ch - 1 + 3 < 16 ? 1 : 0) + ((ch - 1 >= 8 && ch - 1 < 15) ? 1 : 0));
        if (wave < 4) wait_vbar<pe>(); else wait_vbar<0>();
        if constexpr (ch > 0) { w1r[ch - 1][0] = lds_tr8(lds + srd, 256); w1r[ch - 1][1] = lds_tr8(lds + (srd ^ 32), 256); }
        SBAR();
        STEP(sA, sB, nb, cb + 2 * KV_TILE);
        if constexpr (ch > 0) { PIN(w1r[ch - 1][0]); PIN(w1r[ch - 1][1]); }
        if (ch + 2 < ATT_NCH) { if (wave < 4) issue_kv(ch + 2); }
        glds2(w1s + (long)ch * 128 * (F_ * 2), lds0 + stg, sv0, sv1);
        if (wave < 4) {
          if (ch < 7) wslice_block(wo + j * 80, lds0, wave, lane, ch);
          if (ch + 3 < 16) glds1<false>(w1w + (long)(ch + 3) * 128 * (F_ * 2), lds0 + L_DUMP, wv1);
          if (ch >= 8 && ch < 15) { const int pc = (ch - 8) * 4 + wave; glds1<false>(w2w + (long)(pc < 27 ? pc : 26) * 8 * (S_ * 2), lds0 + L_DUMP, wv2); }
        }
        SBAR();
        STEP(sB, sA, nb + KV_TILE, cb + 3 * KV_TILE);
      });
      o = MFMA32(v1, BF8(p1), o);
      float l = hi ? o[8] : o[12];
      bool bad = !(l < 1.8446744e19f);
#pragma unroll
      for (int i = 0; i < 12; ++i) bad |= !(__builtin_fabsf(o[i]) < 3.0e38f);
      if (__builtin_expect(__any(bad), 0)) {
        float m = -INFINITY;
        o = f32x16{};
#pragma unroll 1
        for (int t = 0; t < S_ / 32; ++t) {
          const unsigned tp = kvoff + t * KV_TILE;
          const bf16x8 a16 = BF8(ld16_dev(rws, tp + koff));
          const u32x2 a4 = __builtin_amdgcn_raw_buffer_load_b64(rws, tp + ktoff, 0, 16);
          const bf16x8 w0 = BF8(ld16_dev(rws, tp + voff)), w1f = BF8(ld16_dev(rws, tp + voff + KV_VS));
          f32x16 z = {};
          z = MFMA32(a16, q16, z); z = MFMA32K8(__builtin_bit_cast(bf16x4, a4), q4, z);
          const float mn = __builtin_fmaxf(m, rowmax16(z));
          const float alpha = __builtin_amdgcn_exp2f(m - mn);
          m = mn;
#pragma unroll
          for (int i = 0; i < 16; ++i) { z[i] = __builtin_amdgcn_exp2f(z[i] - m); o[i] *= alpha; }
          bf16x8 a0, a1; pack_p(z, a0, a1);
          o = MFMA32(w0, a0, o); o = MFMA32(w1f, a1, o);
        }
        l = hi ? o[8] : o[12];
      }
      const float rl = 1.0f / l;
      const unsigned ob = WS_O + layer * XB + tb * 81920 + ((trow >> 4) * 64 + (trow & 15)) * 16;
#pragma unroll
      for (int gq = 0; gq < 3; ++gq) {
        const int d0 = 8 * gq + 4 * hi;
        if (d0 < DH_) {
          const int k = (j * 4 + hh) * DH_ + d0;
          u32x2 w; w[0] = pack_bf16(o[4 * gq] * rl, o[4 * gq + 1] * rl); w[1] = pack_bf16(o[4 * gq + 2] * rl, o[4 * gq + 3] * rl);
          st8_dev(rws, ob + (k >> 5) * 4096 + ((k & 31) >> 3) * 256 + (k & 7) * 2, w);
        }
      }
      STAMP(4);
      publish(ws, fl + FL_O + tb * 128 + j * 4, 1, 0, tag, wave, lane);
      w1r[15][0] = lds_tr8(lds + srd, 256); w1r[15][1] = lds_tr8(lds + (srd ^ 32), 256);
    }
    {
      FRESH();
      if (wave == 0) poll_flags(ws, fl + FL_O + tb * 128, 8, tag, lane, dead);
      asm volatile("s_barrier" ::: "memory");
      STAMP(5);
      const unsigned abase = WS_O + layer * XB + tb * 81920 + (kq * 4 + th * 2) * 1024 + lane * 16;
      u32x4 af[5][2];
#pragma unroll
      for (int s = 0; s < 5; ++s)
#pragma unroll
        for (int t = 0; t < 2; ++t) af[s][t] = ld16_dev_s(rws, abase + t * 1024, s * 16384);
      f32x4 acc[5][2];
#pragma unroll
      for (int i = 0; i < 5; ++i) { acc[i][0] = f32x4{0.f, 0.f, 0.f, 0.f}; acc[i][1] = acc[i][0]; }
      const int q = c16 >> 2, p = lane & 3;
      const int row = 32 * kq + 8 * g + q;
      const unsigned fsw = (((q >> 1) & 1) | ((g & 1) << 1)) * 32;
      const unsigned woff = L_W + row * 128 + 8 * p, toff = L_WT + row * 32 + 8 * p;
      bf16x8 wf[2][5];
#pragma unroll
      for (int i = 0; i < 5; ++i) wf[0][i] = i < 4 ? lds_tr8(lds + woff + ((i * 32) ^ fsw), 512) : lds_tr8(lds + toff, 128);
#pragma unroll
      for (int s = 0; s < 5; ++s) {
        const bf16x8 a0 = __builtin_bit_cast(bf16x8, af[s][0]), a1 = __builtin_bit_cast(bf16x8, af[s][1]);
#pragma unroll
        for (int i = 0; i < 5; ++i) {
          if (s < 4) wf[(s + 1) & 1][i] = i < 4 ? lds_tr8(lds + woff + (s + 1) * 16384 + ((i * 32) ^ fsw), 512) : lds_tr8(lds + toff + (s + 1) * 4096, 128);
          acc[i][0] = MFMA16(wf[s & 1][i], a0, acc[i][0]); acc[i][1] = MFMA16(wf[s & 1][i], a1, acc[i][1]);
          SBAR();
        }
      }
      bar_lds();
#pragma unroll
      for (int i = 0; i < 5; ++i)
#pragma unroll
        for (int t = 0; t < 2; ++t) *(LAS f32x4*)(ldw + ((kq * 5 + i) * 4 + th * 2 + t) * 1024 + lane * 16) = acc[i][t];
      bar_lds();
      gu64* gr = (gu64*)(ws + WS_STAT + layer * ST_LAYER + (tb * 4 + ea) * 4096);
      {
        f32x4 s[3]; u32x2 r[3];
#pragma unroll
        for (int i = 0; i < 3; ++i) {
          const int wt = min(ew0 + i, 4);
          s[i] = sum4(lds, wt * 4 + ea, 20, lane);
          r[i] = *(const LAS u32x2*)(lds + res_off + wt * 32);
        }
        float s1 = 0.f, s2 = 0.f;
#pragma unroll
        for (int i = 0; i < 3; ++i) {
          if (i < ewn) {
            const f32x4 y = rnd4(s[i]);
            u32x2 w;
            w[0] = pack_bf16(bf_lo(r[i][0]) + y[0], bf_hi(r[i][0]) + y[1]);
            w[1] = pack_bf16(bf_lo(r[i][1]) + y[2], bf_hi(r[i][1]) + y[3]);
            *(LAS u32x2*)(ldw + res_off + (ew0 + i) * 32) = w;
            const float a = bf_lo(w[0]), b = bf_hi(w[0]), c = bf_lo(w[1]), d = bf_hi(w[1]);
            s1 += (a + b) + (c + d);
            s2 += (a * a + b * b) + (c * c + d * d);
          }
        }
        s1 = xsum32(xsum16(s1)); s2 = xsum32(xsum16(s2));
        if (lane < 32) __hip_atomic_store(gr + (2 * j + th) * 32 + lane, ((unsigned long long)tag << 32) | __float_as_uint(g ? s2 : s1), RLX_AGENT);
      }
      bar_lds();
      if (wave >= 4) {
        const int r = lane >> 3, pos = lane & 7;
        const unsigned pe = (((pos >> 1) ^ ((r >> 1) & 1)) << 1 | (pos & 1)) * 16;
        const unsigned v0 = r * (S_ * 2) + pe + 4096, v1 = (r + 8) * (S_ * 2) + (pe ^ 64) + 3072, vstep = 16 * (S_ * 2) - 2048;
        const char* wsrc = (const char*)(w2 + tb * 64) - 4096 + (long)kq * 32 * (S_ * 2);
#pragma unroll 1
        for (int st = 0; st < 4; ++st) glds4<false>(wsrc + (long)st * 128 * (S_ * 2), lds0 + L_RING + st * 16384 + kq * 4096, v0, v1, v0 + vstep, v1 + vstep);
        asm volatile("s_barrier" ::: "memory");
      } else {
        u32x2 xv[5];
#pragma unroll
        for (int wt = 0; wt < 5; ++wt) xv[wt] = __builtin_bit_cast(u32x2, lds_tr(lds + L_RES + (ea * 16 + 4 * g + q) * 160 + wt * 32 + 8 * p));
        float tot;
        {
          unsigned spins = 0;
          for (;;) {
            bool ok = true; tot = 0.f;
#pragma unroll
            for (int i = 0; i < 8; ++i) {
              const unsigned long long v = __hip_atomic_load(gr + i * 64 + lane, RLX_AGENT);
              tot += __uint_as_float((unsigned)v); ok &= (unsigned)(v >> 32) == tag;
            }
            if (__all(ok) || dead) break;
            if (++spins > SPIN_CAP) dead = true;
            __builtin_amdgcn_s_sleep(1);
          }
        }
        STAMP(6);
        tot = xsum32(tot);
        float mu4[4], rr4[4];
#pragma unroll
        for (int e = 0; e < 4; ++e) { mu4[e] = __shfl(tot, 4 * g + e); rr4[e] = __shfl(tot, 16 + 4 * g + e); }
#pragma unroll
        for (int e = 0; e < 4; ++e) {
          float var; ln_finish(mu4[e], rr4[e], mu4[e], var);
          const float r1 = rsqrtf(var + LN_EPS);
          rr4[e] = r1 * rsqrtf(var * r1 * r1 + LN_EPS);
        }
        const unsigned hb = WS_H + layer * XB + j * 327680 + ((tb * 2 + (ea >> 1)) * 5) * 1024 + (c16 + 16 * ((ea & 1) * 2 + (g >> 1))) * 16 + (g & 1) * 8;
#pragma unroll
        for (int wt = 0; wt < 5; ++wt) {
          const u32x2 v = xv[wt];
          u32x2 w;
          w[0] = pack_bf16((bf_lo(v[0]) - mu4[0]) * rr4[0], (bf_hi(v[0]) - mu4[1]) * rr4[1]);
          w[1] = pack_bf16((bf_lo(v[1]) - mu4[2]) * rr4[2], (bf_hi(v[1]) - mu4[3]) * rr4[3]);
          st8_dev(rws, hb + wt * 1024, w);
        }
        publish(ws, fl + FL_H + j * 512 + tb * 4, 4, 128, tag, wave, lane);
      }
      STAMP(7);
    }
    {
      FRESH();
      if (has4) {
        const int f = ft * 64 + ea * 16 + 4 * g;
        const u32x2 bias = *(const u32x2*)(b1 + f);
        if (wave == 0) poll_flags(ws, fl + FL_H + panel4 * 512 + ((idx >> 2) & 3) * 128, 32, tag, lane, dead);
        asm volatile("s_barrier" ::: "memory");
        STAMP(8);
        const unsigned l16 = lane * 16;
        const int po = (th ? 12 + 2 * kq : 3 * kq) * 1024;
        const char* hsrc = ws + WS_H + layer * XB + panel4 * 327680 + po;
        auto issue = [&](int st) {
          if (th == 0) glds_lin<3, true>(hsrc + st * 20480, lds0 + (st & 3) * 20480 + po, l16);
          else glds_lin<2, true>(hsrc + st * 20480, lds0 + (st & 3) * 20480 + po, l16);
        };
        issue(0); issue(1); issue(2);
        f32x4 acc[2][5];
#pragma unroll
        for (int i = 0; i < 2; ++i)
#pragma unroll
          for (int a = 0; a < 5; ++a) acc[i][a] = f32x4{0.f, 0.f, 0.f, 0.f};
        const lds_cptr ab = lds + kq * 5120 + l16;
        static_for<0, 16>([&](auto ST) {
          constexpr int st = decltype(ST)::v;
          constexpr int ahead = (15 - st) < 2 ? (15 - st) : 2;
          if (th == 0) wait_bar<3 * ahead>(); else wait_bar<2 * ahead>();
          bf16x8 af[5];
#pragma unroll
          for (int a = 0; a < 5; ++a) af[a] = lds_b128(ab + (st & 3) * 20480 + a * 1024);
          SBAR();
#pragma unroll
          for (int a = 0; a < 5; ++a) {
            if (st + 3 < 16) {
              if ((a & 1) == th) glds1<true>(hsrc + (st + 3) * 20480 + (a >> 1) * 1024, lds0 + ((st + 3) & 3) * 20480 + po + (a >> 1) * 1024, l16);
            }
            SBAR();
            acc[0][a] = MFMA16(w1r[st][0], af[a], acc[0][a]); acc[1][a] = MFMA16(w1r[st][1], af[a], acc[1][a]);
            SBAR();
          }
        });
        bar_lds();
#pragma unroll
        for (int i = 0; i < 2; ++i)
#pragma unroll
          for (int a = 0; a < 5; ++a) *(LAS f32x4*)(ldw + ((kq * 4 + 2 * th + i) * 5 + a) * 1024 + l16) = acc[i][a];
        bar_lds();
        STAMP(9);
        const unsigned ub = WS_U + layer * UB + ((panel4 * U_KS + (f >> 5)) * 5) * 1024 + (c16 + 16 * ((f & 31) >> 3)) * 16 + (f & 7) * 2;
        f32x4 sv[3];
#pragma unroll
        for (int i = 0; i < 3; ++i) sv[i] = sum4(lds, ea * 5 + min(ew0 + i, 4), 20, lane);
#pragma unroll
        for (int i = 0; i < 3; ++i) {
          if (i < ewn) {
            const f32x4 s = rnd4(rnd4(sv[i]) + f32x4{bf_lo(bias[0]), bf_hi(bias[0]), bf_lo(bias[1]), bf_hi(bias[1])});
            u32x2 w;
            w[0] = pack_bf16(gelu_tanh(s[0]), gelu_tanh(s[1]));
            w[1] = pack_bf16(gelu_tanh(s[2]), gelu_tanh(s[3]));
            st8_dev(rws, ub + (ew0 + i) * 1024, w);
          }
        }
        publish(ws, fl + FL_U + panel4 * 512 + ft * 4, 4, 128, tag, wave, lane);
      }
    }
    STAMP(10);
    {
      FRESH();
      const int tok = tb * 64 + ea * 16 + c16;
      const float bias = __uint_as_float((unsigned)b2[tok] << 16);
      if (wave >= 4) {
        const int r = lane >> 3, pos = lane & 7;
        const unsigned pe = (((pos >> 1) ^ ((r >> 1) & 1)) << 1 | (pos & 1)) * 16;
        const unsigned v0 = r * (S_ * 2) + pe + 4096, v1 = (r + 8) * (S_ * 2) + (pe ^ 64) + 3072, vstep = 16 * (S_ * 2) - 2048;
        const char* wsrc = (const char*)(w2 + tb * 64) - 4096 + (long)kq * 32 * (S_ * 2);
#pragma unroll 1
        for (int st = 4; st < 9; ++st) glds4<false>(wsrc + (long)st * 128 * (S_ * 2), lds0 + (st - 4) * 16384 + kq * 4096, v0, v1, v0 + vstep, v1 + vstep);
        asm volatile("s_barrier" ::: "memory");
        static_for<0, 14>([&](auto ST) {
          constexpr int st = decltype(ST)::v;
          constexpr int last = (st + 7 > 13) ? 13 : (st + 7 < 8 ? 8 : st + 7);
          wait_vbar<4 * (last - st)>();
          if (st >= 1 && st + 8 < 14) {
            const long o = (st + 8 == 13 && kq >= 2) ? (long)(2 * 13 - 1) * 64 * (S_ * 2) : (long)(st + 8) * 128 * (S_ * 2);
            glds4<false>(wsrc + o, lds0 + w2_slot(st + 8) + kq * 4096, v0, v1, v0 + vstep, v1 + vstep);
          }
        });
        asm volatile("s_barrier" ::: "memory");
      } else {
        if (wave == 0) poll_flags(ws, fl + FL_U + j * 512 + (tb & 3) * 128, 27, tag, lane, dead);
        asm volatile("s_barrier" ::: "memory");
        STAMP(11);
        const unsigned abase = WS_U + layer * UB + j * (U_KS * 5 * 1024) + kq * 5120 + lane * 16;
        const int q = c16 >> 2, p = lane & 3;
        const unsigned fsw = (((q >> 1) & 1) | ((g & 1) << 1)) * 32;
        const lds_cptr wrow = lds + (32 * kq + 8 * g + q) * 128 + 8 * p;
        u32x4 af[3][5];
#pragma unroll
        for (int st = 0; st < 3; ++st)
#pragma unroll
          for (int a = 0; a < 5; ++a) af[st][a] = ld16_dev_s(rws, abase + (a & 3) * 1024, st * 20480 + (a >> 2) * 4096);
        f32x4 acc[4][5];
#pragma unroll
        for (int i = 0; i < 4; ++i)
#pragma unroll
          for (int a = 0; a < 5; ++a) acc[i][a] = f32x4{0.f, 0.f, 0.f, 0.f};
        static_for<0, 14>([&](auto ST) {
          constexpr int st = decltype(ST)::v;
          bar_lds();
          if (st < 13 || kq < 2) {
            bf16x8 wf[4];
#pragma unroll
            for (int i = 0; i < 4; ++i) wf[i] = lds_tr8(wrow + w2_slot(st) + ((i * 32) ^ fsw), 512);
            SBAR();
#pragma unroll
            for (int a = 0; a < 5; ++a) {
              const bf16x8 x = __builtin_bit_cast(bf16x8, af[st % 3][a]);
#pragma unroll
              for (int i = 0; i < 4; ++i) acc[i][a] = MFMA16(x, wf[i], acc[i][a]);
              SBAR();
              if (st + 3 < 14) af[st % 3][a] = ld16_dev_s(rws, abase + (a & 3) * 1024, (st + 3) * 20480 + (a >> 2) * 4096);
              SBAR();
            }
          }
        });
        bar_lds();
#pragma unroll
        for (int i = 0; i < 4; ++i)
#pragma unroll
          for (int a = 0; a < 5; ++a) *(LAS f32x4*)(ldw + ((kq * 4 + i) * 5 + a) * 1024 + lane * 16) = acc[i][a];
      }
      bar_lds();
      STAMP(12);
      f32x4 sv[3]; u32x2 rv[3];
#pragma unroll
      for (int i = 0; i < 3; ++i) { const int at = min(ew0 + i, 4); sv[i] = sum4(lds, ea * 5 + at, 20, lane); rv[i] = *(const LAS u32x2*)(lds + res_off + at * 32); }
#pragma unroll
      for (int i = 0; i < 3; ++i) {
        if (i < ewn) {
          const int at = ew0 + i;
          const f32x4 s = rnd4(rnd4(sv[i]) + bias);
          const u32x2 r = rv[i];
          u32x2 w;
          w[0] = pack_bf16(bf_lo(r[0]) + s[0], bf_hi(r[0]) + s[1]);
          w[1] = pack_bf16(bf_lo(r[1]) + s[2], bf_hi(r[1]) + s[3]);
          const int d = j * 80 + at * 16 + 4 * g;
          if (layer == 0) {
            *(LAS u32x2*)(ldw + res_off + at * 32) = w;
            st8_dev(rws, WS_X + tb * 81920 + (((d >> 5) * 4 + ea) * 64 + c16 + 16 * ((d & 31) >> 3)) * 16 + (d & 7) * 2, w);
          } else {
            *(u32x2*)(out + (long)tok * D_ + d) = w;
          }
        }
      }
      if (layer == 0) publish(ws, WS_FLAGS + FL_X + tb * 128 + j * 4, 1, 0, tag, wave, lane);
      STAMP(13);
    }
  }
  if (blockIdx.x == 0 && tid == 0) __hip_atomic_store((gu32*)(ws + WS_EPOCH), tag, RLX_AGENT);
}

extern "C" void kernel_launch(void* const* d_in, const int* in_sizes, int n_in, void* d_out, int out_size, void* d_ws,
                              size_t ws_size, hipStream_t stream) {
  (void)hipFuncSetAttribute((const void*)mega_kernel, hipFuncAttributeMaxDynamicSharedMemorySize, LDS_BYTES);
  mega_kernel<<<256, 512, LDS_BYTES, stream>>>((const u16*)d_in[0], (const u16*)d_in[1], (const u16*)d_in[2], (const u16*)d_in[3],
                                               (const u16*)d_in[4], (const u16*)d_in[6], (const u16*)d_in[7], (const u16*)d_in[8],
                                               (const u16*)d_in[9], (u16*)d_out, (char*)d_ws);
}
```

```cpp
#include <hip/hip_runtime.h>
#include <hip/hip_bf16.h>
#include <stdint.h>

typedef __attribute__((ext_vector_type(8))) short bf16x8;
typedef __attribute__((ext_vector_type(4))) short bf16x4;
typedef __attribute__((ext_vector_type(16))) float f32x16;
typedef __attribute__((ext_vector_type(4))) float f32x4;
typedef __attribute__((ext_vector_type(2))) float f32x2;
typedef __attribute__((ext_vector_type(2))) __bf16 bf2_t;
typedef __attribute__((ext_vector_type(4))) unsigned u32x4;
typedef __attribute__((ext_vector_type(2))) unsigned u32x2;
typedef unsigned short u16;
#define LAS __attribute__((address_space(3)))
#define GAS __attribute__((address_space(1)))
typedef LAS const char* lds_cptr;
typedef LAS char* lds_ptr;
typedef GAS unsigned gu32;
typedef GAS unsigned long long gu64;
#define DI __device__ __forceinline__
#define RLX_AGENT __ATOMIC_RELAXED, __HIP_MEMORY_SCOPE_AGENT

constexpr int S_ = 2048, D_ = 640, DH_ = 20, F_ = 1728;
constexpr float LN_EPS = 1e-5f;
constexpr float QSCALE = 0.22360679774997896f * 1.4426950408889634f;

constexpr int KV_TILE = 2624, KV_KT = 1024, KV_V = 1280, KV_VS = 672;

constexpr unsigned WS_EPOCH = 0;
constexpr unsigned WS_FLAGS = 4096, FL_LAYER = 65536;
constexpr unsigned FL_KV = 0;
constexpr unsigned FL_O = 32768;
constexpr unsigned FL_X = 36864;
constexpr unsigned FL_H = 40960;
constexpr unsigned FL_U = 45056;
constexpr unsigned WS_DIAG = 262144;
constexpr unsigned WS_STAT = 524288, ST_LAYER = 524288;
constexpr unsigned WS_KV = WS_STAT + 2 * ST_LAYER, KV_LAYER = 196608;
constexpr unsigned XB = S_ * D_ * 2;
constexpr unsigned WS_O = WS_KV + 2 * KV_LAYER;
constexpr unsigned WS_H = WS_O + 2 * XB;
constexpr int U_KS = 56;
constexpr unsigned UB = 8 * U_KS * 5 * 1024;
constexpr unsigned WS_U = WS_H + 2 * XB;
constexpr unsigned WS_X = WS_U + 2 * UB;
constexpr unsigned WS_END = WS_X + XB;

constexpr int L_W = 0;
constexpr int L_WT = 81920;
constexpr int L_RING = 81920;
constexpr int L_ATT = 102400;
constexpr int L_QT = 81920;
constexpr int L_KVP = 128064;
constexpr int L_STG = 134656;
constexpr int L_RES = 151040;
constexpr int L_DUMP = 161280;
constexpr int LDS_BYTES = 163840;
constexpr unsigned SPIN_CAP = 1u << 21;

DI unsigned pack_bf16(float lo, float hi) {
  f32x2 v = {lo, hi};
  bf2_t b = __builtin_convertvector(v, bf2_t);
  return __builtin_bit_cast(unsigned, b);
}
DI float bf_lo(unsigned u) { return __uint_as_float(u << 16); }
DI float bf_hi(unsigned u) { return __uint_as_float(u & 0xffff0000u); }
DI float rnd1(float v) { return bf_lo(pack_bf16(v, 0.f)); }
DI float ln_rsd(float var) { return __builtin_amdgcn_rcpf(rnd1(__builtin_amdgcn_sqrtf(rnd1(rnd1(var) + LN_EPS)))); }

#define GLDS_HEAD "s_mov_b32 m0, %1\n\ts_nop 4\n\t"
template <bool DEV> DI void glds1(const void* sbase, unsigned m0v, unsigned v0) {
  if (DEV) asm volatile(GLDS_HEAD "global_load_lds_dwordx4 %2, %0 sc1" ::"s"(sbase), "s"(m0v), "v"(v0) : "memory");
  else asm volatile(GLDS_HEAD "global_load_lds_dwordx4 %2, %0" ::"s"(sbase), "s"(m0v), "v"(v0) : "memory");
}
template <bool DEV> DI void glds4(const void* sbase, unsigned m0v, unsigned v0, unsigned v1, unsigned v2, unsigned v3) {
  if (DEV)
    asm volatile(GLDS_HEAD "global_load_lds_dwordx4 %2, %0 sc1\n\tglobal_load_lds_dwordx4 %3, %0 offset:1024 sc1\n\t"
                           "global_load_lds_dwordx4 %4, %0 offset:2048 sc1\n\tglobal_load_lds_dwordx4 %5, %0 offset:3072 sc1"
                 ::"s"(sbase), "s"(m0v), "v"(v0), "v"(v1), "v"(v2), "v"(v3) : "memory");
  else
    asm volatile(GLDS_HEAD "global_load_lds_dwordx4 %2, %0\n\tglobal_load_lds_dwordx4 %3, %0 offset:1024\n\t"
                           "global_load_lds_dwordx4 %4, %0 offset:2048\n\tglobal_load_lds_dwordx4 %5, %0 offset:3072"
                 ::"s"(sbase), "s"(m0v), "v"(v0), "v"(v1), "v"(v2), "v"(v3) : "memory");
}
template <int N, bool DEV> DI void glds_lin(const void* sbase, unsigned m0v, unsigned v) {
  if (N == 1) glds1<DEV>(sbase, m0v, v);
  else if (N == 4) glds4<DEV>(sbase, m0v, v, v, v, v);
  else if (N == 2) {
    if (DEV) asm volatile(GLDS_HEAD "global_load_lds_dwordx4 %2, %0 sc1\n\tglobal_load_lds_dwordx4 %2, %0 offset:1024 sc1" ::"s"(sbase), "s"(m0v), "v"(v) : "memory");
    else asm volatile(GLDS_HEAD "global_load_lds_dwordx4 %2, %0\n\tglobal_load_lds_dwordx4 %2, %0 offset:1024" ::"s"(sbase), "s"(m0v), "v"(v) : "memory");
  } else {
    if (DEV) asm volatile(GLDS_HEAD "global_load_lds_dwordx4 %2, %0 sc1\n\tglobal_load_lds_dwordx4 %2, %0 offset:1024 sc1\n\tglobal_load_lds_dwordx4 %2, %0 offset:2048 sc1" ::"s"(sbase), "s"(m0v), "v"(v) : "memory");
    else asm volatile(GLDS_HEAD "global_load_lds_dwordx4 %2, %0\n\tglobal_load_lds_dwordx4 %2, %0 offset:1024\n\tglobal_load_lds_dwordx4 %2, %0 offset:2048" ::"s"(sbase), "s"(m0v), "v"(v) : "memory");
  }
}
DI void glds2(const void* sbase, unsigned m0v, unsigned v0, unsigned v1) {
  asm volatile(GLDS_HEAD "global_load_lds_dwordx4 %2, %0\n\tglobal_load_lds_dwordx4 %3, %0 offset:1024" ::"s"(sbase), "s"(m0v), "v"(v0), "v"(v1) : "memory");
}
#define UNI(x) __builtin_amdgcn_readfirstlane(x)
template <int N> DI void wait_bar() { asm volatile("s_waitcnt vmcnt(%0) lgkmcnt(0)\n\ts_barrier" ::"i"(N) : "memory"); }
template <int N> DI void wait_vbar() { asm volatile("s_waitcnt vmcnt(%0)\n\ts_barrier" ::"i"(N) : "memory"); }
DI void bar_lds() { asm volatile("s_waitcnt lgkmcnt(0)\n\ts_barrier" ::: "memory"); }
typedef short v4i16_t __attribute__((ext_vector_type(4)));
DI bf16x4 lds_tr(lds_cptr p) {
  return __builtin_bit_cast(bf16x4, __builtin_amdgcn_ds_read_tr16_b64_v4i16((LAS v4i16_t*)p));
}
DI bf16x8 lds_tr8(lds_cptr p, int step) {
  const bf16x4 lo = lds_tr(p), hi = lds_tr(p + step);
  return __builtin_shufflevector(lo, hi, 0, 1, 2, 3, 4, 5, 6, 7);
}
DI bf16x8 lds_b128(lds_cptr p) { return *(const LAS bf16x8*)p; }
#define MFMA16(a, b, c) __builtin_amdgcn_mfma_f32_16x16x32_bf16((a), (b), (c), 0, 0, 0)
#define MFMA32(a, b, c) __builtin_amdgcn_mfma_f32_32x32x16_bf16((a), (b), (c), 0, 0, 0)
#define MFMA32K8(a, b, c) __builtin_amdgcn_mfma_f32_32x32x8bf16_1k((a), (b), (c), 0, 0, 0)
#define SBAR() __builtin_amdgcn_sched_barrier(0)
#define PIN(x) asm volatile("" : "+v"(x))
typedef __amdgpu_buffer_rsrc_t rsrc_t;
DI void st8_dev(rsrc_t r, unsigned off, u32x2 v) { __builtin_amdgcn_raw_buffer_store_b64(v, r, off, 0, 16); }
DI u32x4 ld16_dev(rsrc_t r, unsigned off) { return __builtin_amdgcn_raw_buffer_load_b128(r, off, 0, 16); }
DI u32x4 ld16_dev_s(rsrc_t r, unsigned off, unsigned soff) { return __builtin_amdgcn_raw_buffer_load_b128(r, off, soff, 16); }

DI void poll_flags(const char* ws, unsigned off, int n, unsigned tag, int lane, bool& dead) {
  gu32* f = (gu32*)(ws + off);
  unsigned spins = 0;
  while (!dead) {
    unsigned v = tag;
    if (lane < n) v = __hip_atomic_load(f + lane, RLX_AGENT);
    if (__all(v == tag)) break;
    if (++spins > SPIN_CAP) dead = true;
    __builtin_amdgcn_s_sleep(1);
  }
  asm volatile("" ::: "memory");
}
DI void publish(const char* ws, unsigned off, int nrep, unsigned rstride, unsigned tag, int wave, int lane) {
  wait_bar<0>();
  if (wave == 0 && lane < nrep) __hip_atomic_store((gu32*)(ws + off + lane * rstride), tag, RLX_AGENT);
}

DI float fmax3(float a, float b, float c) { return __builtin_fmaxf(__builtin_fmaxf(a, b), c); }
DI float rowmax16(const f32x16& s) {
  float a = fmax3(s[0], s[1], s[2]), b = fmax3(s[3], s[4], s[5]);
  a = fmax3(a, s[6], s[7]); b = fmax3(b, s[8], s[9]); a = fmax3(a, s[10], s[11]); b = fmax3(b, s[12], s[13]);
  a = fmax3(a, s[14], s[15]); a = __builtin_fmaxf(a, b);
  return __builtin_fmaxf(a, __shfl_xor(a, 32));
}
DI void pack_p(const f32x16& p, bf16x8& lo, bf16x8& hi) {
  u32x4 a, b;
#pragma unroll
  for (int i = 0; i < 4; ++i) { a[i] = pack_bf16(p[2 * i], p[2 * i + 1]); b[i] = pack_bf16(p[8 + 2 * i], p[9 + 2 * i]); }
  lo = __builtin_bit_cast(bf16x8, a); hi = __builtin_bit_cast(bf16x8, b);
}
DI float gelu_tanh(float u) {
  const float z = 0.7978845608028654f * (u + 0.044715f * u * u * u);
  const float e = __builtin_amdgcn_exp2f(-2.885390081777927f * z);
  return u * __builtin_amdgcn_rcpf(1.f + e);
}

DI void wslice_block(const u16* w, unsigned lds0, int lw, int lane, int blk) {
  const char* src = (const char*)w + lw * (160 * 1280);
  if (blk < 5) {
    const int r = lane >> 3, pos = lane & 7;
    const unsigned pe = (((pos >> 1) ^ ((r >> 1) & 1)) << 1 | (pos & 1)) * 16;
    const unsigned v0 = r * 1280 + pe, v1 = r * 1280 + (pe ^ 64) + 9216;
    glds4<false>(src + blk * (32 * 1280), lds0 + L_W + lw * 20480 + blk * 4096, v0, v1, v0 + 18432, v1 + 18432);
  } else {
    const unsigned t0 = (lane >> 1) * 1280 + 128 + (lane & 1) * 16;
    if (blk == 5) glds4<false>(src, lds0 + L_WT + lw * 5120, t0, t0 + 39936, t0 + 2 * 39936, t0 + 3 * 39936);
    else glds1<false>(src + 128 * 1280, lds0 + L_WT + lw * 5120 + 4096, t0);
  }
}

DI void touch(const void* sbase, unsigned voff, unsigned& sink) {
  asm volatile("s_nop 4\n\tglobal_load_dword %0, %1, %2" : "+v"(sink) : "v"(voff), "s"(sbase) : "memory");
}
DI float xsum16(float x) {
  auto r = __builtin_amdgcn_permlane16_swap(__float_as_uint(x), __float_as_uint(x), false, false);
  return __uint_as_float(r[0]) + __uint_as_float(r[1]);
}
DI float xsum32(float x) {
  auto r = __builtin_amdgcn_permlane32_swap(__float_as_uint(x), __float_as_uint(x), false, false);
  return __uint_as_float(r[0]) + __uint_as_float(r[1]);
}
DI f32x4 ldf4(lds_cptr p) { return *(const LAS f32x4*)p; }

DI f32x4 sum4(lds_cptr lds, int t, int nt, int lane) {
  f32x4 s = *(const LAS f32x4*)(lds + t * 1024 + lane * 16);
#pragma unroll
  for (int k = 1; k < 4; ++k) s += *(const LAS f32x4*)(lds + (k * nt + t) * 1024 + lane * 16);
  return s;
}
DI void ln_finish(float s1, float s2, float& mu, float& var) { mu = s1 * (1.f / D_); var = fmaxf(s2 * (1.f / D_) - mu * mu, 0.f); }

constexpr int ATT_CH = 4;
constexpr int ATT_CHB = ATT_CH * KV_TILE;
constexpr int ATT_SLOT = 10752;
constexpr int ATT_NCH = S_ / 32 / ATT_CH;
constexpr int w2_slot(int st) { return (st % 9) < 4 ? L_RING + (st % 9) * 16384 : ((st % 9) - 4) * 16384; }
template <int N> struct IC { static constexpr int v = N; };
template <int I, int N, class F> DI void static_for(F&& f) { if constexpr (I < N) { f(IC<I>{}); static_for<I + 1, N>(f); } }

#define STAMP(i) do { } while (0)

__global__ __launch_bounds__(512, 2) void mega_kernel(const u16* __restrict__ x0, const u16* __restrict__ wq, const u16* __restrict__ wk,
                                                      const u16* __restrict__ wv, const u16* __restrict__ wo, const u16* __restrict__ w1,
                                                      const u16* __restrict__ b1, const u16* __restrict__ w2, const u16* __restrict__ b2,
                                                      u16* __restrict__ out, char* ws) {
  extern __shared__ __attribute__((aligned(16))) char smem[];
  const int tid = threadIdx.x;
  const unsigned lds0 = (unsigned)(uintptr_t)smem;
  const lds_cptr lds = (lds_cptr)smem;
  const lds_ptr ldw = (lds_ptr)smem;
#define FRESH() \
  int lane = tid & 63; asm volatile("" : "+v"(lane)); \
  int wave = UNI(tid >> 6); asm volatile("" : "+s"(wave)); \
  int bid = blockIdx.x; asm volatile("" : "+s"(bid)); \
  const int xcd = bid & 7, idx = bid >> 3, tb = xcd * 4 + (idx >> 3), j = idx & 7; \
  const int g = lane >> 4, c16 = lane & 15, kq = wave & 3, th = wave >> 2; \
  const int ea = wave & 3, ew0 = th ? 3 : 0, ewn = th ? 2 : 3; \
  const unsigned res_off = L_RES + (ea * 16 + c16) * 160 + 8 * g; \
  const int panel4 = (xcd & 1) * 4 + (idx & 3), ft = (xcd >> 1) * 7 + (idx >> 2); \
  const bool has4 = idx < 28 && ft < 27; \
  (void)xcd; (void)g; (void)c16; (void)kq; (void)th; (void)ea; (void)ew0; (void)ewn; (void)res_off; (void)tb; (void)j; (void)panel4; (void)has4
  const rsrc_t rws = __builtin_amdgcn_make_buffer_rsrc(ws, 0, WS_END, 0x00020000);
  unsigned tag = 0;
  bool dead = false;

#pragma unroll 1
  for (int layer = 0; layer < 2; ++layer) {
    const unsigned fl = WS_FLAGS + layer * FL_LAYER;
    const unsigned kvoff = WS_KV + layer * KV_LAYER;
    unsigned sink = 0;
    {
      FRESH();
      STAMP(0);
      if (wave >= 4) {
        if (layer > 0) {
#pragma unroll 1
          for (int blk = 0; blk < 7; ++blk) wslice_block(wq + j * 80, lds0, wave - 4, lane, blk);
        }
      } else {
        {
          const char* xs = (const char*)((j & 1) ? wv : wk);
          if (wave < 3) {
            glds_lin<4, false>(xs + wave * 8192, lds0 + L_ATT + wave * 8192, lane * 16);
            glds_lin<4, false>(xs + wave * 8192 + 4096, lds0 + L_ATT + wave * 8192 + 4096, lane * 16);
          } else glds1<false>(xs + 24576, lds0 + L_ATT + 24576, lane * 16);
        }
        if (layer == 0) {
#pragma unroll 1
          for (int pc = wave; pc < 10; pc += 4) {
            const int n = pc * 64 + lane, row = n / 10, ch = n - row * 10;
            glds1<false>(x0 + (long)tb * 64 * D_ + j * 80, lds0 + L_RES + pc * 1024, row * 1280 + ch * 16);
          }
        }
      }
      rsrc_t ra = rws; unsigned abase, astr_t, astr_k;
      if (layer == 0) {
        ra = __builtin_amdgcn_make_buffer_rsrc((void*)x0, 0, XB, 0x00020000);
        abase = (tb * 64 + th * 32 + c16) * (D_ * 2) + kq * 64 + g * 16; astr_t = 16 * D_ * 2; astr_k = 256;
      } else {
        if (wave == 0) poll_flags(ws, WS_FLAGS + FL_X + tb * 128, 8, tag, lane, dead);
        wait_vbar<0>();
        abase = WS_X + tb * 81920 + (kq * 4 + th * 2) * 1024 + lane * 16; astr_t = 1024; astr_k = 16384;
      }
      STAMP(1);
      u32x4 af[5][2];
#pragma unroll
      for (int s = 0; s < 5; ++s)
#pragma unroll
        for (int t = 0; t < 2; ++t) af[s][t] = ld16_dev_s(ra, abase, s * astr_k + t * astr_t);
      if (layer == 0) {
        const unsigned epoch = __hip_atomic_load((gu32*)(ws + WS_EPOCH), RLX_AGENT);
        wait_vbar<0>();
        tag = UNI(epoch) + 1u;
        if (wave >= 4) {
#pragma unroll 1
          for (int blk = 0; blk < 7; ++blk) wslice_block(wq + j * 80, lds0, wave - 4, lane, blk);
        }
      }
      bf16x8 ones;
#pragma unroll
      for (int i = 0; i < 8; ++i) ones[i] = (short)0x3f80;
      const f32x4 z4 = {0.f, 0.f, 0.f, 0.f};
      const int q = c16 >> 2, p = lane & 3;
      const int row = 32 * kq + 8 * g + q;
      const unsigned fsw = (((q >> 1) & 1) | ((g & 1) << 1)) * 32;
      const unsigned woff = L_W + row * 128 + 8 * p, toff = L_WT + row * 32 + 8 * p, xoff = L_ATT + row * 40 + 8 * p;
      LAS float* stat = (LAS float*)(ldw + L_DUMP);
      LAS float* csum = (LAS float*)(ldw + L_KVP + 20480);
      const int tk = j >> 1;
      const bool isv = j & 1;
      {
        f32x4 a1s[2] = {z4, z4}, a2s[2] = {z4, z4}, akv[2] = {z4, z4}, aok[2] = {z4, z4};
#pragma unroll
        for (int s = 0; s < 5; ++s) {
          const bf16x8 a0 = __builtin_bit_cast(bf16x8, af[s][0]), a1 = __builtin_bit_cast(bf16x8, af[s][1]);
          if (th == (tk >> 1)) {
            const bf16x8 a = (tk & 1) ? a1 : a0;
            const bf16x8 x0f = lds_tr8(lds + xoff + s * 5120, 160), x1f = lds_tr8(lds + xoff + s * 5120 + 32, 160);
            if (!isv) {
              akv[0] = MFMA16(x0f, a, akv[0]); akv[1] = MFMA16(x1f, a, akv[1]);
              aok[0] = MFMA16(x0f, ones, aok[0]); aok[1] = MFMA16(x1f, ones, aok[1]);
            } else {
              akv[0] = MFMA16(a, x0f, akv[0]); akv[1] = MFMA16(a, x1f, akv[1]);
              aok[0] = MFMA16(ones, x0f, aok[0]); aok[1] = MFMA16(ones, x1f, aok[1]);
            }
          }
          a1s[0] = MFMA16(ones, a0, a1s[0]); a1s[1] = MFMA16(ones, a1, a1s[1]);
          a2s[0] = MFMA16(a0, a0, a2s[0]); a2s[1] = MFMA16(a1, a1, a2s[1]);
        }
        if (g == q) {
#pragma unroll
          for (int t = 0; t < 2; ++t) {
            const float d = (p & 2) ? ((p & 1) ? a2s[t][3] : a2s[t][2]) : ((p & 1) ? a2s[t][1] : a2s[t][0]);
            *(LAS f32x2*)(stat + (kq * 64 + th * 32 + t * 16 + c16) * 2) = f32x2{a1s[t][0], d};
          }
        }
        if (th == (tk >> 1)) {
          const bool l2 = isv ? (c16 < 4) : (g == 0);
          const int i2 = isv ? g * 4 + c16 : c16;
          const int o = L_KVP + kq * 2560;
          *(LAS f32x4*)(ldw + o + lane * 16) = akv[0];
          *(LAS f32x4*)(ldw + o + 1280 + lane * 16) = aok[0];
          if (l2) { *(LAS f32x4*)(ldw + o + 1024 + i2 * 16) = akv[1]; *(LAS f32x4*)(ldw + o + 2304 + i2 * 16) = aok[1]; }
        }
      }
      wait_bar<0>();
      float mu, rs;
      {
        f32x2 sv[4];
#pragma unroll
        for (int k = 0; k < 4; ++k) sv[k] = *(const LAS f32x2*)(stat + (k * 64 + ea * 16 + c16) * 2);
        float var; ln_finish((sv[0][0] + sv[1][0]) + (sv[2][0] + sv[3][0]), (sv[0][1] + sv[1][1]) + (sv[2][1] + sv[3][1]), mu, var);
        mu = rnd1(mu); rs = ln_rsd(var);
      }
      if (ea == tk) {
        const int li = th == 0 ? lane : 64 + (isv ? g * 4 + (c16 & 3) : c16);
        f32x4 s = z4, cs = z4;
#pragma unroll
        for (int k = 0; k < 4; ++k) { s += ldf4(lds + L_KVP + k * 2560 + li * 16); cs += ldf4(lds + L_KVP + k * 2560 + 1280 + li * 16); }
        const unsigned tile = kvoff + (tb * 2 + (ea >> 1)) * KV_TILE;
        if (!isv) {
          u32x2 w; w[0] = pack_bf16((s[0] - mu * cs[0]) * rs, (s[1] - mu * cs[1]) * rs); w[1] = pack_bf16((s[2] - mu * cs[2]) * rs, (s[3] - mu * cs[3]) * rs);
          const int r32 = (ea & 1) * 16 + c16;
          if (th == 0) st8_dev(rws, tile + ((g >> 1) * 32 + r32) * 16 + (g & 1) * 8, w);
          else if (g == 0) st8_dev(rws, tile + KV_KT + r32 * 8, w);
        } else {
          float o[4];
#pragma unroll
          for (int e = 0; e < 4; ++e) o[e] = (s[e] - __shfl(mu, 4 * g + e) * cs[e]) * __shfl(rs, 4 * g + e);
          const int d = th * 16 + c16;
          u32x2 w; w[0] = pack_bf16(o[0], o[1]); w[1] = pack_bf16(o[2], o[3]);
          if (d == DH_) { w[0] = 0x3f803f80u; w[1] = 0x3f803f80u; }
          if (d <= DH_) st8_dev(rws, tile + KV_V + (ea & 1) * KV_VS + ((g & 1) * 21 + d) * 16 + (g >> 1) * 8, w);
        }
      }
      f32x4 acc[5][2], aon[3] = {z4, z4, z4};
#pragma unroll
      for (int i = 0; i < 5; ++i) { acc[i][0] = z4; acc[i][1] = z4; }
#pragma unroll
      for (int s = 0; s < 5; ++s) {
        const bf16x8 a0 = __builtin_bit_cast(bf16x8, af[s][0]), a1 = __builtin_bit_cast(bf16x8, af[s][1]);
        bf16x8 wf[5];
#pragma unroll
        for (int i = 0; i < 4; ++i) wf[i] = lds_tr8(lds + woff + s * 16384 + ((i * 32) ^ fsw), 512);
        wf[4] = lds_tr8(lds + toff + s * 4096, 128);
#pragma unroll
        for (int i = 0; i < 5; ++i) { acc[i][0] = MFMA16(wf[i], a0, acc[i][0]); acc[i][1] = MFMA16(wf[i], a1, acc[i][1]); }
        if (th == 0) { aon[0] = MFMA16(wf[0], ones, aon[0]); aon[1] = MFMA16(wf[1], ones, aon[1]); aon[2] = MFMA16(wf[2], ones, aon[2]); }
        else { aon[0] = MFMA16(wf[3], ones, aon[0]); aon[1] = MFMA16(wf[4], ones, aon[1]); }
        if (s == 1) {
          publish(ws, fl + FL_KV + (tb * 8 + j) * 4, 32, 1024, tag, wave, lane);
          if (wave >= 4) {
            const char* w1w = (const char*)(w1 + (has4 ? ft : 0) * 64) + (long)((idx & 3) * 512 + kq * 128) * (F_ * 2);
            touch(w1w, lane * (F_ * 2), sink); touch(w1w + 64 * (F_ * 2), lane * (F_ * 2), sink);
            touch((const char*)(w2 + tb * 64) + (long)(j * 216 + kq * 54) * (S_ * 2), (lane < 54 ? lane : 53) * (S_ * 2), sink);
          }
        }
      }
      bar_lds();
#pragma unroll
      for (int i = 0; i < 5; ++i)
#pragma unroll
        for (int t = 0; t < 2; ++t) *(LAS f32x4*)(ldw + ((kq * 5 + i) * 4 + th * 2 + t) * 1024 + lane * 16) = acc[i][t];
      if (c16 == 0) {
#pragma unroll
        for (int i = 0; i < 3; ++i) if (i < ewn) *(LAS f32x4*)(csum + (kq * 7 + ew0 + i) * 16 + 4 * g) = aon[i];
      }
      bar_lds();
      {
        const float r = rs * QSCALE;
        f32x4 s[3], cs[3];
#pragma unroll
        for (int i = 0; i < 3; ++i) {
          const int wt = min(ew0 + i, 4);
          s[i] = sum4(lds, wt * 4 + ea, 20, lane);
          cs[i] = z4;
#pragma unroll
          for (int k = 0; k < 4; ++k) cs[i] += *(const LAS f32x4*)(csum + (k * 7 + wt) * 16 + 4 * g);
        }
#pragma unroll
        for (int i = 0; i < 3; ++i) {
          if (i < ewn) {
            u32x2 w;
            w[0] = pack_bf16((s[i][0] - mu * cs[i][0]) * r, (s[i][1] - mu * cs[i][1]) * r);
            w[1] = pack_bf16((s[i][2] - mu * cs[i][2]) * r, (s[i][3] - mu * cs[i][3]) * r);
            *(LAS u32x2*)(ldw + L_QT + (ea * 16 + c16) * 160 + (ew0 + i) * 32 + 8 * g) = w;
          }
        }
      }
      bar_lds();
    }
    STAMP(2);
    bf16x8 w1r[16][2];
    {
      FRESH();
      const int r32 = lane & 31, hi = lane >> 5;
      const int hh = wave & 3, trow = th * 32 + r32;
      bf16x8 q16; bf16x4 q4;
      {
        const lds_cptr qp = lds + L_QT + trow * 160 + hh * 40;
        const u32x2 a = *(const LAS u32x2*)(qp + hi * 16), b = *(const LAS u32x2*)(qp + hi * 16 + 8), c = *(const LAS u32x2*)(qp + 32);
        u32x4 v = {a[0], a[1], b[0], b[1]};
        q16 = __builtin_bit_cast(bf16x8, v);
        u32x2 t = {hi ? 0u : c[0], hi ? 0u : c[1]};
        q4 = __builtin_bit_cast(bf16x4, t);
        PIN(q16); PIN(q4);
      }
      if (wave == 0) {
        unsigned spins = 0;
        while (!dead) {
          const u32x4 v = ld16_dev(rws, fl + FL_KV + (bid & 31) * 1024 + lane * 16);
          if (__all(v[0] == tag && v[1] == tag && v[2] == tag && v[3] == tag)) break;
          if (++spins > SPIN_CAP) dead = true;
          __builtin_amdgcn_s_sleep(1);
          asm volatile("" ::: "memory");
        }
      }
      bar_lds();
      STAMP(3);
      const unsigned koff = lane * 16;
      const char* KV = ws + kvoff + wave * 3072;
      auto issue_kv = [&](int ch) {
        const unsigned dst = lds0 + L_ATT + (ch % 3) * ATT_SLOT + wave * 3072;
        if (wave < 3) glds_lin<3, true>(KV + ch * ATT_CHB, dst, koff);
        else { glds1<true>(KV + ch * ATT_CHB, dst, koff); if (lane < 16) glds1<true>(KV + ch * ATT_CHB + 1024, dst + 1024, koff); }
      };
      const char* w1s = (const char*)(w1 + (has4 ? ft : 0) * 64) + (long)wave * 16 * (F_ * 2);
      unsigned sv0, sv1;
      {
        const int r = lane >> 3, pos = lane & 7;
        const unsigned pe = (((pos >> 1) ^ ((r >> 1) & 1)) << 1 | (pos & 1)) * 16;
        sv0 = r * (F_ * 2) + pe; sv1 = (r + 8) * (F_ * 2) + (pe ^ 64) - 1024;
      }
      const unsigned fsw = (((c16 >> 3) & 1) | ((g & 1) << 1)) * 32;
      const unsigned srow = (32 * kq + 8 * g + (c16 >> 2)) * 128 + 8 * (lane & 3);
      const unsigned sr0 = srow + ((64 * th) ^ fsw), sr1 = srow + ((64 * th + 32) ^ fsw);
      if (wave < 4) { issue_kv(0); issue_kv(1); }
      glds2(w1s, lds0 + L_STG + wave * 2048, sv0, sv1);
      const unsigned ktoff = KV_KT + r32 * 8, voff = KV_V + (hi * 21 + (r32 < 20 ? r32 : 20)) * 16;
      f32x16 o = {}, negm, sA, sB;
      bf16x8 k16, v0, v1; bf16x4 k4; u32x4 p0, p1;
      if (wave < 3) wait_bar<5>(); else if (wave == 3) wait_bar<4>(); else bar_lds();
      const lds_cptr ring = lds + L_ATT;
      {
        k16 = lds_b128(ring + koff);
        k4 = *(const LAS bf16x4*)(ring + ktoff);
        f32x16 z = {};
        z = MFMA32(k16, q16, z); z = MFMA32K8(k4, q4, z);
        k16 = lds_b128(ring + KV_TILE + koff);
        k4 = *(const LAS bf16x4*)(ring + KV_TILE + ktoff);
        v1 = lds_b128(ring + voff);
        const float m = rowmax16(z);
#pragma unroll
        for (int i = 0; i < 16; ++i) { negm[i] = -m; sA[i] = z[i] - m; }
        p1 = u32x4{0u, 0u, 0u, 0u};
      }
#define EX(x) __builtin_amdgcn_exp2f(x)
#define EXP4(X, i) do { X[i] = EX(X[i]); X[i + 1] = EX(X[i + 1]); X[i + 2] = EX(X[i + 2]); X[i + 3] = EX(X[i + 3]); } while (0)
#define BF8(x) __builtin_bit_cast(bf16x8, x)
#define PK4(P, X, i) do { P[0] = pack_bf16(X[i], X[i + 1]); P[1] = pack_bf16(X[i + 2], X[i + 3]); P[2] = pack_bf16(X[i + 4], X[i + 5]); P[3] = pack_bf16(X[i + 6], X[i + 7]); } while (0)
#define STEP(C, N, kn, vc) do { \
    N = MFMA32(k16, q16, negm); SBAR(); \
    v0 = lds_b128((vc) + voff); EXP4(C, 0); PIN(C); SBAR(); \
    o = MFMA32(v1, BF8(p1), o); SBAR(); \
    k16 = lds_b128((kn) + koff); EXP4(C, 4); PIN(C); SBAR(); PK4(p0, C, 0); PIN(p0); SBAR(); \
    N = MFMA32K8(k4, q4, N); SBAR(); \
    v1 = lds_b128((vc) + voff + KV_VS); EXP4(C, 8); PIN(C); SBAR(); \
    o = MFMA32(v0, BF8(p0), o); SBAR(); \
    k4 = *(const LAS bf16x4*)((kn) + ktoff); EXP4(C, 12); PIN(C); SBAR(); PK4(p1, C, 8); PIN(p1); SBAR(); \
  } while (0)
      static_for<0, ATT_NCH>([&](auto CH) {
        constexpr int ch = decltype(CH)::v;
        const lds_cptr cb = ring + (ch % 3) * ATT_SLOT;
        const lds_cptr nb = (ch + 1 < ATT_NCH) ? ring + ((ch + 1) % 3) * ATT_SLOT : cb;
        STEP(sA, sB, cb + 2 * KV_TILE, cb);
        STEP(sB, sA, cb + 3 * KV_TILE, cb + KV_TILE);
        if (wave < 4) wait_vbar<(ch >= 1 && ch <= 5) ? 4 : 0>(); else wait_vbar<0>();
        if (ch + 2 < ATT_NCH) { if (wave < 4) issue_kv(ch + 2); }
        if (ch + 1 < 16) glds2(w1s + (long)(ch + 1) * 128 * (F_ * 2), lds0 + ((ch & 1) ? L_STG : L_WT) + wave * 2048, sv0, sv1);
        if (ch < 5) { if (wave < 4) wslice_block(wo + j * 80, lds0, wave, lane, ch); }
        w1r[ch][0] = lds_tr8(lds + ((ch & 1) ? L_WT : L_STG) + sr0, 512); w1r[ch][1] = lds_tr8(lds + ((ch & 1) ? L_WT : L_STG) + sr1, 512);
        SBAR();
        STEP(sA, sB, nb, cb + 2 * KV_TILE);
        PIN(w1r[ch][0]); PIN(w1r[ch][1]);
        SBAR();
        STEP(sB, sA, nb + KV_TILE, cb + 3 * KV_TILE);
      });
      o = MFMA32(v1, BF8(p1), o);
      float l = hi ? o[8] : o[12];
      bool bad = !(l < 1.8446744e19f);
#pragma unroll
      for (int i = 0; i < 12; ++i) bad |= !(__builtin_fabsf(o[i]) < 3.0e38f);
      if (__builtin_expect(__any(bad), 0)) {
        float m = -INFINITY;
        o = f32x16{};
#pragma unroll 1
        for (int t = 0; t < S_ / 32; ++t) {
          const unsigned tp = kvoff + t * KV_TILE;
          const bf16x8 a16 = BF8(ld16_dev(rws, tp + koff));
          const u32x2 a4 = __builtin_amdgcn_raw_buffer_load_b64(rws, tp + ktoff, 0, 16);
          const bf16x8 w0 = BF8(ld16_dev(rws, tp + voff)), w1f = BF8(ld16_dev(rws, tp + voff + KV_VS));
          f32x16 z = {};
          z = MFMA32(a16, q16, z); z = MFMA32K8(__builtin_bit_cast(bf16x4, a4), q4, z);
          const float mn = __builtin_fmaxf(m, rowmax16(z));
          const float alpha = __builtin_amdgcn_exp2f(m - mn);
          m = mn;
#pragma unroll
          for (int i = 0; i < 16; ++i) { z[i] = __builtin_amdgcn_exp2f(z[i] - m); o[i] *= alpha; }
          bf16x8 a0, a1; pack_p(z, a0, a1);
          o = MFMA32(w0, a0, o); o = MFMA32(w1f, a1, o);
        }
        l = hi ? o[8] : o[12];
      }
      const float rl = __builtin_amdgcn_rcpf(l);
      const unsigned ob = WS_O + layer * XB + tb * 81920 + ((trow >> 4) * 64 + (trow & 15)) * 16;
#pragma unroll
      for (int gq = 0; gq < 3; ++gq) {
        const int d0 = 8 * gq + 4 * hi;
        if (d0 < DH_) {
          const int k = (j * 4 + hh) * DH_ + d0;
          u32x2 w; w[0] = pack_bf16(o[4 * gq] * rl, o[4 * gq + 1] * rl); w[1] = pack_bf16(o[4 * gq + 2] * rl, o[4 * gq + 3] * rl);
          st8_dev(rws, ob + (k >> 5) * 4096 + ((k & 31) >> 3) * 256 + (k & 7) * 2, w);
        }
      }
      STAMP(4);
      publish(ws, fl + FL_O + tb * 128 + j * 4, 1, 0, tag, wave, lane);
      asm volatile("" :: "v"(sink));
      if (wave >= 4) { wslice_block(wo + j * 80, lds0, kq, lane, 5); wslice_block(wo + j * 80, lds0, kq, lane, 6); }
    }
    {
      FRESH();
      if (wave == 0) poll_flags(ws, fl + FL_O + tb * 128, 8, tag, lane, dead);
      wait_vbar<0>();
      STAMP(5);
      const unsigned abase = WS_O + layer * XB + tb * 81920 + (kq * 4 + th * 2) * 1024 + lane * 16;
      u32x4 af[5][2];
#pragma unroll
      for (int s = 0; s < 5; ++s)
#pragma unroll
        for (int t = 0; t < 2; ++t) af[s][t] = ld16_dev_s(rws, abase + t * 1024, s * 16384);
      f32x4 acc[5][2];
#pragma unroll
      for (int i = 0; i < 5; ++i) { acc[i][0] = f32x4{0.f, 0.f, 0.f, 0.f}; acc[i][1] = acc[i][0]; }
      const int q = c16 >> 2, p = lane & 3;
      const int row = 32 * kq + 8 * g + q;
      const unsigned fsw = (((q >> 1) & 1) | ((g & 1) << 1)) * 32;
      const unsigned woff = L_W + row * 128 + 8 * p, toff = L_WT + row * 32 + 8 * p;
      bf16x8 wf[2][5];
#pragma unroll
      for (int i = 0; i < 5; ++i) wf[0][i] = i < 4 ? lds_tr8(lds + woff + ((i * 32) ^ fsw), 512) : lds_tr8(lds + toff, 128);
#pragma unroll
      for (int s = 0; s < 5; ++s) {
        const bf16x8 a0 = __builtin_bit_cast(bf16x8, af[s][0]), a1 = __builtin_bit_cast(bf16x8, af[s][1]);
#pragma unroll
        for (int i = 0; i < 5; ++i) {
          if (s < 4) wf[(s + 1) & 1][i] = i < 4 ? lds_tr8(lds + woff + (s + 1) * 16384 + ((i * 32) ^ fsw), 512) : lds_tr8(lds + toff + (s + 1) * 4096, 128);
          acc[i][0] = MFMA16(wf[s & 1][i], a0, acc[i][0]); acc[i][1] = MFMA16(wf[s & 1][i], a1, acc[i][1]);
          SBAR();
        }
      }
      bar_lds();
#pragma unroll
      for (int i = 0; i < 5; ++i)
#pragma unroll
        for (int t = 0; t < 2; ++t) *(LAS f32x4*)(ldw + ((kq * 5 + i) * 4 + th * 2 + t) * 1024 + lane * 16) = acc[i][t];
      bar_lds();
      gu64* gr = (gu64*)(ws + WS_STAT + layer * ST_LAYER + (tb * 4 + ea) * 4096);
      {
        f32x4 s[3]; u32x2 r[3];
#pragma unroll
        for (int i = 0; i < 3; ++i) {
          const int wt = min(ew0 + i, 4);
          s[i] = sum4(lds, wt * 4 + ea, 20, lane);
          r[i] = *(const LAS u32x2*)(lds + res_off + wt * 32);
        }
        float s1 = 0.f, s2 = 0.f;
#pragma unroll
        for (int i = 0; i < 3; ++i) {
          if (i < ewn) {
            u32x2 w;
            w[0] = pack_bf16(bf_lo(r[i][0]) + s[i][0], bf_hi(r[i][0]) + s[i][1]);
            w[1] = pack_bf16(bf_lo(r[i][1]) + s[i][2], bf_hi(r[i][1]) + s[i][3]);
            *(LAS u32x2*)(ldw + res_off + (ew0 + i) * 32) = w;
            const float a = bf_lo(w[0]), b = bf_hi(w[0]), c = bf_lo(w[1]), d = bf_hi(w[1]);
            s1 += (a + b) + (c + d);
            s2 += (a * a + b * b) + (c * c + d * d);
          }
        }
        s1 = xsum32(xsum16(s1)); s2 = xsum32(xsum16(s2));
        if (lane < 32) __hip_atomic_store(gr + (2 * j + th) * 32 + lane, ((unsigned long long)tag << 32) | __float_as_uint(g ? s2 : s1), RLX_AGENT);
      }
      bar_lds();
      if (wave >= 4) {
        const int r = lane >> 3, pos = lane & 7;
        const unsigned pe = (((pos >> 1) ^ ((r >> 1) & 1)) << 1 | (pos & 1)) * 16;
        const unsigned v0 = r * (S_ * 2) + pe + 4096, v1 = (r + 8) * (S_ * 2) + (pe ^ 64) + 3072, vstep = 16 * (S_ * 2) - 2048;
        const char* wsrc = (const char*)(w2 + tb * 64) - 4096 + (long)kq * 32 * (S_ * 2);
#pragma unroll 1
        for (int st = 0; st < 4; ++st) glds4<false>(wsrc + (long)st * 128 * (S_ * 2), lds0 + L_RING + st * 16384 + kq * 4096, v0, v1, v0 + vstep, v1 + vstep);
        asm volatile("s_barrier" ::: "memory");
      } else {
        u32x2 xv[5];
#pragma unroll
        for (int wt = 0; wt < 5; ++wt) xv[wt] = __builtin_bit_cast(u32x2, lds_tr(lds + L_RES + (ea * 16 + 4 * g + q) * 160 + wt * 32 + 8 * p));
        float tot;
        {
          unsigned spins = 0;
          for (;;) {
            bool ok = true; tot = 0.f;
#pragma unroll
            for (int i = 0; i < 8; ++i) {
              const unsigned long long v = __hip_atomic_load(gr + i * 64 + lane, RLX_AGENT);
              tot += __uint_as_float((unsigned)v); ok &= (unsigned)(v >> 32) == tag;
            }
            if (__all(ok) || dead) break;
            if (++spins > SPIN_CAP) dead = true;
            __builtin_amdgcn_s_sleep(1);
          }
        }
        STAMP(6);
        tot = xsum32(tot);
        float mu4[4], rr4[4], m24[4], r24[4];
#pragma unroll
        for (int e = 0; e < 4; ++e) { mu4[e] = __shfl(tot, 4 * g + e); rr4[e] = __shfl(tot, 16 + 4 * g + e); }
#pragma unroll
        for (int e = 0; e < 4; ++e) {
          float mu, var; ln_finish(mu4[e], rr4[e], mu, var);
          mu4[e] = rnd1(mu); rr4[e] = ln_rsd(var);
          m24[e] = rnd1((mu - mu4[e]) * rr4[e]); r24[e] = ln_rsd(var * rr4[e] * rr4[e]);
        }
        const unsigned hb = WS_H + layer * XB + j * 327680 + ((tb * 2 + (ea >> 1)) * 5) * 1024 + (c16 + 16 * ((ea & 1) * 2 + (g >> 1))) * 16 + (g & 1) * 8;
#pragma unroll
        for (int wt = 0; wt < 5; ++wt) {
          const u32x2 v = xv[wt];
          u32x2 w;
          w[0] = pack_bf16(bf_lo(v[0]) - mu4[0], bf_hi(v[0]) - mu4[1]); w[1] = pack_bf16(bf_lo(v[1]) - mu4[2], bf_hi(v[1]) - mu4[3]);
          w[0] = pack_bf16(bf_lo(w[0]) * rr4[0], bf_hi(w[0]) * rr4[1]); w[1] = pack_bf16(bf_lo(w[1]) * rr4[2], bf_hi(w[1]) * rr4[3]);
          w[0] = pack_bf16(bf_lo(w[0]) - m24[0], bf_hi(w[0]) - m24[1]); w[1] = pack_bf16(bf_lo(w[1]) - m24[2], bf_hi(w[1]) - m24[3]);
          w[0] = pack_bf16(bf_lo(w[0]) * r24[0], bf_hi(w[0]) * r24[1]); w[1] = pack_bf16(bf_lo(w[1]) * r24[2], bf_hi(w[1]) * r24[3]);
          st8_dev(rws, hb + wt * 1024, w);
        }
        publish(ws, fl + FL_H + j * 512 + tb * 4, 4, 128, tag, wave, lane);
      }
      STAMP(7);
    }
    {
      FRESH();
      if (has4) {
        const int f = ft * 64 + ea * 16 + 4 * g;
        const u32x2 bias = *(const u32x2*)(b1 + f);
        if (wave == 0) poll_flags(ws, fl + FL_H + panel4 * 512 + ((idx >> 2) & 3) * 128, 32, tag, lane, dead);
        asm volatile("s_barrier" ::: "memory");
        STAMP(8);
        const unsigned l16 = lane * 16;
        const int po = (th ? 12 + 2 * kq : 3 * kq) * 1024;
        const char* hsrc = ws + WS_H + layer * XB + panel4 * 327680 + po;
        auto issue = [&](int st) {
          if (th == 0) glds_lin<3, true>(hsrc + st * 20480, lds0 + (st & 3) * 20480 + po, l16);
          else glds_lin<2, true>(hsrc + st * 20480, lds0 + (st & 3) * 20480 + po, l16);
        };
        issue(0); issue(1); issue(2);
        f32x4 acc[2][5];
#pragma unroll
        for (int i = 0; i < 2; ++i)
#pragma unroll
          for (int a = 0; a < 5; ++a) acc[i][a] = f32x4{0.f, 0.f, 0.f, 0.f};
        const lds_cptr ab = lds + kq * 5120 + l16;
        static_for<0, 16>([&](auto ST) {
          constexpr int st = decltype(ST)::v;
          constexpr int ahead = (15 - st) < 2 ? (15 - st) : 2;
          if (th == 0) wait_bar<3 * ahead>(); else wait_bar<2 * ahead>();
          bf16x8 af[5];
#pragma unroll
          for (int a = 0; a < 5; ++a) af[a] = lds_b128(ab + (st & 3) * 20480 + a * 1024);
          if (st + 3 < 16) { if (th == 0) issue(st + 3); }
          SBAR();
#pragma unroll
          for (int a = 0; a < 5; ++a) { acc[0][a] = MFMA16(w1r[st][0], af[a], acc[0][a]); acc[1][a] = MFMA16(w1r[st][1], af[a], acc[1][a]); }
          SBAR();
          if (st + 3 < 16) { if (th == 1) issue(st + 3); }
        });
        bar_lds();
#pragma unroll
        for (int i = 0; i < 2; ++i)
#pragma unroll
          for (int a = 0; a < 5; ++a) *(LAS f32x4*)(ldw + ((kq * 4 + 2 * th + i) * 5 + a) * 1024 + l16) = acc[i][a];
        bar_lds();
        STAMP(9);
        const unsigned ub = WS_U + layer * UB + ((panel4 * U_KS + (f >> 5)) * 5) * 1024 + (c16 + 16 * ((f & 31) >> 3)) * 16 + (f & 7) * 2;
        f32x4 sv[3];
#pragma unroll
        for (int i = 0; i < 3; ++i) sv[i] = sum4(lds, ea * 5 + min(ew0 + i, 4), 20, lane);
#pragma unroll
        for (int i = 0; i < 3; ++i) {
          if (i < ewn) {
            const f32x4 s = sv[i];
            u32x2 w;
            w[0] = pack_bf16(gelu_tanh(s[0] + bf_lo(bias[0])), gelu_tanh(s[1] + bf_hi(bias[0])));
            w[1] = pack_bf16(gelu_tanh(s[2] + bf_lo(bias[1])), gelu_tanh(s[3] + bf_hi(bias[1])));
            st8_dev(rws, ub + (ew0 + i) * 1024, w);
          }
        }
        publish(ws, fl + FL_U + panel4 * 512 + ft * 4, 4, 128, tag, wave, lane);
      }
    }
    STAMP(10);
    {
      FRESH();
      const int tok = tb * 64 + ea * 16 + c16;
      const float bias = __uint_as_float((unsigned)b2[tok] << 16);
      if (wave >= 4) {
        const int r = lane >> 3, pos = lane & 7;
        const unsigned pe = (((pos >> 1) ^ ((r >> 1) & 1)) << 1 | (pos & 1)) * 16;
        const unsigned v0 = r * (S_ * 2) + pe + 4096, v1 = (r + 8) * (S_ * 2) + (pe ^ 64) + 3072, vstep = 16 * (S_ * 2) - 2048;
        const char* wsrc = (const char*)(w2 + tb * 64) - 4096 + (long)kq * 32 * (S_ * 2);
#pragma unroll 1
        for (int st = 4; st < 9; ++st) glds4<false>(wsrc + (long)st * 128 * (S_ * 2), lds0 + (st - 4) * 16384 + kq * 4096, v0, v1, v0 + vstep, v1 + vstep);
        asm volatile("s_barrier" ::: "memory");
        static_for<0, 14>([&](auto ST) {
          constexpr int st = decltype(ST)::v;
          constexpr int last = (st + 7 > 13) ? 13 : (st + 7 < 8 ? 8 : st + 7);
          wait_vbar<4 * (last - st)>();
          if (st >= 1 && st + 8 < 14) {
            const long o = (st + 8 == 13 && kq >= 2) ? (long)(2 * 13 - 1) * 64 * (S_ * 2) : (long)(st + 8) * 128 * (S_ * 2);
            glds4<false>(wsrc + o, lds0 + w2_slot(st + 8) + kq * 4096, v0, v1, v0 + vstep, v1 + vstep);
          }
        });
        asm volatile("s_barrier" ::: "memory");
      } else {
        if (wave == 0) poll_flags(ws, fl + FL_U + j * 512 + (tb & 3) * 128, 27, tag, lane, dead);
        asm volatile("s_barrier" ::: "memory");
        STAMP(11);
        const unsigned abase = WS_U + layer * UB + j * (U_KS * 5 * 1024) + kq * 5120 + lane * 16;
        const int q = c16 >> 2, p = lane & 3;
        const unsigned fsw = (((q >> 1) & 1) | ((g & 1) << 1)) * 32;
        const lds_cptr wrow = lds + (32 * kq + 8 * g + q) * 128 + 8 * p;
        u32x4 af[3][5];
#pragma unroll
        for (int st = 0; st < 3; ++st)
#pragma unroll
          for (int a = 0; a < 5; ++a) af[st][a] = ld16_dev_s(rws, abase + (a & 3) * 1024, st * 20480 + (a >> 2) * 4096);
        f32x4 acc[4][5];
#pragma unroll
        for (int i = 0; i < 4; ++i)
#pragma unroll
          for (int a = 0; a < 5; ++a) acc[i][a] = f32x4{0.f, 0.f, 0.f, 0.f};
        static_for<0, 14>([&](auto ST) {
          constexpr int st = decltype(ST)::v;
          bar_lds();
          if (st < 13 || kq < 2) {
            bf16x8 wf[4];
#pragma unroll
            for (int i = 0; i < 4; ++i) wf[i] = lds_tr8(wrow + w2_slot(st) + ((i * 32) ^ fsw), 512);
            SBAR();
#pragma unroll
            for (int a = 0; a < 5; ++a) {
              const bf16x8 x = __builtin_bit_cast(bf16x8, af[st % 3][a]);
#pragma unroll
              for (int i = 0; i < 4; ++i) acc[i][a] = MFMA16(x, wf[i], acc[i][a]);
              SBAR();
              if (st + 3 < 14) af[st % 3][a] = ld16_dev_s(rws, abase + (a & 3) * 1024, (st + 3) * 20480 + (a >> 2) * 4096);
              SBAR();
            }
          }
        });
        bar_lds();
#pragma unroll
        for (int i = 0; i < 4; ++i)
#pragma unroll
          for (int a = 0; a < 5; ++a) *(LAS f32x4*)(ldw + ((kq * 4 + i) * 5 + a) * 1024 + lane * 16) = acc[i][a];
      }
      bar_lds();
      STAMP(12);
      f32x4 sv[3]; u32x2 rv[3];
#pragma unroll
      for (int i = 0; i < 3; ++i) { const int at = min(ew0 + i, 4); sv[i] = sum4(lds, ea * 5 + at, 20, lane); rv[i] = *(const LAS u32x2*)(lds + res_off + at * 32); }
#pragma unroll
      for (int i = 0; i < 3; ++i) {
        if (i < ewn) {
          const int at = ew0 + i;
          const f32x4 s = sv[i];
          const u32x2 r = rv[i];
          u32x2 w;
          w[0] = pack_bf16(bf_lo(r[0]) + (s[0] + bias), bf_hi(r[0]) + (s[1] + bias));
          w[1] = pack_bf16(bf_lo(r[1]) + (s[2] + bias), bf_hi(r[1]) + (s[3] + bias));
          const int d = j * 80 + at * 16 + 4 * g;
          if (layer == 0) {
            *(LAS u32x2*)(ldw + res_off + at * 32) = w;
            st8_dev(rws, WS_X + tb * 81920 + (((d >> 5) * 4 + ea) * 64 + c16 + 16 * ((d & 31) >> 3)) * 16 + (d & 7) * 2, w);
          } else {
            *(u32x2*)(out + (long)tok * D_ + d) = w;
          }
        }
      }
      if (layer == 0) publish(ws, WS_FLAGS + FL_X + tb * 128 + j * 4, 1, 0, tag, wave, lane);
      STAMP(13);
    }
  }
  if (blockIdx.x == 0 && tid == 0) __hip_atomic_store((gu32*)(ws + WS_EPOCH), tag, RLX_AGENT);
}

extern "C" void kernel_launch(void* const* d_in, const int* in_sizes, int n_in, void* d_out, int out_size, void* d_ws,
                              size_t ws_size, hipStream_t stream) {
  (void)hipFuncSetAttribute((const void*)mega_kernel, hipFuncAttributeMaxDynamicSharedMemorySize, LDS_BYTES);
  mega_kernel<<<256, 512, LDS_BYTES, stream>>>((const u16*)d_in[0], (const u16*)d_in[1], (const u16*)d_in[2], (const u16*)d_in[3],
                                               (const u16*)d_in[4], (const u16*)d_in[6], (const u16*)d_in[7], (const u16*)d_in[8],
                                               (const u16*)d_in[9], (u16*)d_out, (char*)d_ws);
}
```

```cpp
#include <hip/hip_runtime.h>
#include <hip/hip_bf16.h>
#include <stdint.h>

typedef __attribute__((ext_vector_type(8))) short bf16x8;
typedef __attribute__((ext_vector_type(4))) short bf16x4;
typedef __attribute__((ext_vector_type(16))) float f32x16;
typedef __attribute__((ext_vector_type(4))) float f32x4;
typedef __attribute__((ext_vector_type(2))) float f32x2;
typedef __attribute__((ext_vector_type(2))) __bf16 bf2_t;
typedef __attribute__((ext_vector_type(4))) unsigned u32x4;
typedef __attribute__((ext_vector_type(2))) unsigned u32x2;
typedef unsigned short u16;
#define LAS __attribute__((address_space(3)))
#define GAS __attribute__((address_space(1)))
typedef LAS const char* lds_cptr;
typedef LAS char* lds_ptr;
typedef GAS unsigned gu32;
typedef GAS unsigned long long gu64;
#define DI __device__ __forceinline__
#define RLX_AGENT __ATOMIC_RELAXED, __HIP_MEMORY_SCOPE_AGENT

constexpr int S_ = 2048, D_ = 640, DH_ = 20, F_ = 1728;
constexpr float LN_EPS = 1e-5f;
constexpr float QSCALE = 0.22360679774997896f * 1.4426950408889634f;

constexpr int KV_TILE = 2688, KV_KT = 1024, KV_V = 1280, KV_VS = 704;

constexpr unsigned WS_EPOCH = 0;
constexpr unsigned WS_FLAGS = 4096, FL_LAYER = 65536;
constexpr unsigned FL_KV = 0;
constexpr unsigned FL_O = 32768;
constexpr unsigned FL_X = 36864;
constexpr unsigned FL_H = 40960;
constexpr unsigned FL_U = 45056;
constexpr unsigned WS_DIAG = 262144;
constexpr unsigned WS_STAT = 524288, ST_LAYER = 524288;
constexpr unsigned WS_KV = WS_STAT + 2 * ST_LAYER, KV_LAYER = 196608;
constexpr unsigned XB = S_ * D_ * 2;
constexpr unsigned WS_O = WS_KV + 2 * KV_LAYER;
constexpr unsigned WS_H = WS_O + 2 * XB;
constexpr int U_KS = 56;
constexpr unsigned UB = 8 * U_KS * 5 * 1024;
constexpr unsigned WS_U = WS_H + 2 * XB;
constexpr unsigned WS_X = WS_U + 2 * UB;
constexpr unsigned WS_END = WS_X + XB;

constexpr int L_W = 0;
constexpr int L_WT = 81920;
constexpr int L_RING = 81920;
constexpr int L_ATT = 102400;
constexpr int L_QT = 81920;
constexpr int L_KVP = 128064;
constexpr int L_STG = 134656;
constexpr int L_RES = 151040;
constexpr int L_DUMP = 161280;
constexpr int LDS_BYTES = 163840;
constexpr unsigned SPIN_CAP = 1u << 21;

DI unsigned pack_bf16(float lo, float hi) {
  f32x2 v = {lo, hi};
  bf2_t b = __builtin_convertvector(v, bf2_t);
  return __builtin_bit_cast(unsigned, b);
}
DI float bf_lo(unsigned u) { return __uint_as_float(u << 16); }
DI float bf_hi(unsigned u) { return __uint_as_float(u & 0xffff0000u); }
DI float rnd1(float v) { return bf_lo(pack_bf16(v, 0.f)); }
DI float ln_rsd(float var) { return __builtin_amdgcn_rcpf(rnd1(__builtin_amdgcn_sqrtf(rnd1(rnd1(var) + LN_EPS)))); }

#define GLDS_HEAD "s_mov_b32 m0, %1\n\ts_nop 4\n\t"
template <bool DEV> DI void glds1(const void* sbase, unsigned m0v, unsigned v0) {
  if (DEV) asm volatile(GLDS_HEAD "global_load_lds_dwordx4 %2, %0 sc1" ::"s"(sbase), "s"(m0v), "v"(v0) : "memory");
  else asm volatile(GLDS_HEAD "global_load_lds_dwordx4 %2, %0" ::"s"(sbase), "s"(m0v), "v"(v0) : "memory");
}
template <bool DEV> DI void glds4(const void* sbase, unsigned m0v, unsigned v0, unsigned v1, unsigned v2, unsigned v3) {
  if (DEV)
    asm volatile(GLDS_HEAD "global_load_lds_dwordx4 %2, %0 sc1\n\tglobal_load_lds_dwordx4 %3, %0 offset:1024 sc1\n\t"
                           "global_load_lds_dwordx4 %4, %0 offset:2048 sc1\n\tglobal_load_lds_dwordx4 %5, %0 offset:3072 sc1"
                 ::"s"(sbase), "s"(m0v), "v"(v0), "v"(v1), "v"(v2), "v"(v3) : "memory");
  else
    asm volatile(GLDS_HEAD "global_load_lds_dwordx4 %2, %0\n\tglobal_load_lds_dwordx4 %3, %0 offset:1024\n\t"
                           "global_load_lds_dwordx4 %4, %0 offset:2048\n\tglobal_load_lds_dwordx4 %5, %0 offset:3072"
                 ::"s"(sbase), "s"(m0v), "v"(v0), "v"(v1), "v"(v2), "v"(v3) : "memory");
}
template <int N, bool DEV> DI void glds_lin(const void* sbase, unsigned m0v, unsigned v) {
  if (N == 1) glds1<DEV>(sbase, m0v, v);
  else if (N == 4) glds4<DEV>(sbase, m0v, v, v, v, v);
  else if (N == 2) {
    if (DEV) asm volatile(GLDS_HEAD "global_load_lds_dwordx4 %2, %0 sc1\n\tglobal_load_lds_dwordx4 %2, %0 offset:1024 sc1" ::"s"(sbase), "s"(m0v), "v"(v) : "memory");
    else asm volatile(GLDS_HEAD "global_load_lds_dwordx4 %2, %0\n\tglobal_load_lds_dwordx4 %2, %0 offset:1024" ::"s"(sbase), "s"(m0v), "v"(v) : "memory");
  } else {
    if (DEV) asm volatile(GLDS_HEAD "global_load_lds_dwordx4 %2, %0 sc1\n\tglobal_load_lds_dwordx4 %2, %0 offset:1024 sc1\n\tglobal_load_lds_dwordx4 %2, %0 offset:2048 sc1" ::"s"(sbase), "s"(m0v), "v"(v) : "memory");
    else asm volatile(GLDS_HEAD "global_load_lds_dwordx4 %2, %0\n\tglobal_load_lds_dwordx4 %2, %0 offset:1024\n\tglobal_load_lds_dwordx4 %2, %0 offset:2048" ::"s"(sbase), "s"(m0v), "v"(v) : "memory");
  }
}
DI void glds2(const void* sbase, unsigned m0v, unsigned v0, unsigned v1) {
  asm volatile(GLDS_HEAD "global_load_lds_dwordx4 %2, %0\n\tglobal_load_lds_dwordx4 %3, %0 offset:1024" ::"s"(sbase), "s"(m0v), "v"(v0), "v"(v1) : "memory");
}
#define UNI(x) __builtin_amdgcn_readfirstlane(x)
template <int N> DI void wait_bar() { asm volatile("s_waitcnt vmcnt(%0) lgkmcnt(0)\n\ts_barrier" ::"i"(N) : "memory"); }
template <int N> DI void wait_vbar() { asm volatile("s_waitcnt vmcnt(%0)\n\ts_barrier" ::"i"(N) : "memory"); }
DI void bar_lds() { asm volatile("s_waitcnt lgkmcnt(0)\n\ts_barrier" ::: "memory"); }
typedef short v4i16_t __attribute__((ext_vector_type(4)));
DI bf16x4 lds_tr(lds_cptr p) {
  return __builtin_bit_cast(bf16x4, __builtin_amdgcn_ds_read_tr16_b64_v4i16((LAS v4i16_t*)p));
}
DI bf16x8 lds_tr8(lds_cptr p, int step) {
  const bf16x4 lo = lds_tr(p), hi = lds_tr(p + step);
  return __builtin_shufflevector(lo, hi, 0, 1, 2, 3, 4, 5, 6, 7);
}
DI bf16x8 lds_b128(lds_cptr p) { return *(const LAS bf16x8*)p; }
#define MFMA16(a, b, c) __builtin_amdgcn_mfma_f32_16x16x32_bf16((a), (b), (c), 0, 0, 0)
#define MFMA32(a, b, c) __builtin_amdgcn_mfma_f32_32x32x16_bf16((a), (b), (c), 0, 0, 0)
#define MFMA32K8(a, b, c) __builtin_amdgcn_mfma_f32_32x32x8bf16_1k((a), (b), (c), 0, 0, 0)
#define SBAR() __builtin_amdgcn_sched_barrier(0)
#define PIN(x) asm volatile("" : "+v"(x))
typedef __amdgpu_buffer_rsrc_t rsrc_t;
DI void st8_dev(rsrc_t r, unsigned off, u32x2 v) { __builtin_amdgcn_raw_buffer_store_b64(v, r, off, 0, 16); }
DI u32x4 ld16_dev(rsrc_t r, unsigned off) { return __builtin_amdgcn_raw_buffer_load_b128(r, off, 0, 16); }
DI u32x4 ld16_dev_s(rsrc_t r, unsigned off, unsigned soff) { return __builtin_amdgcn_raw_buffer_load_b128(r, off, soff, 16); }

DI void poll_flags(const char* ws, unsigned off, int n, unsigned tag, int lane, bool& dead) {
  gu32* f = (gu32*)(ws + off);
  unsigned spins = 0;
  while (!dead) {
    unsigned v = tag;
    if (lane < n) v = __hip_atomic_load(f + lane, RLX_AGENT);
    if (__all(v == tag)) break;
    if (++spins > SPIN_CAP) dead = true;
    __builtin_amdgcn_s_sleep(1);
  }
  asm volatile("" ::: "memory");
}
DI void publish(const char* ws, unsigned off, int nrep, unsigned rstride, unsigned tag, int wave, int lane) {
  wait_bar<0>();
  if (wave == 0 && lane < nrep) __hip_atomic_store((gu32*)(ws + off + lane * rstride), tag, RLX_AGENT);
}

DI float fmax3(float a, float b, float c) { return __builtin_fmaxf(__builtin_fmaxf(a, b), c); }
DI float rowmax16(const f32x16& s) {
  float a = fmax3(s[0], s[1], s[2]), b = fmax3(s[3], s[4], s[5]);
  a = fmax3(a, s[6], s[7]); b = fmax3(b, s[8], s[9]); a = fmax3(a, s[10], s[11]); b = fmax3(b, s[12], s[13]);
  a = fmax3(a, s[14], s[15]); a = __builtin_fmaxf(a, b);
  return __builtin_fmaxf(a, __shfl_xor(a, 32));
}
DI void pack_p(const f32x16& p, bf16x8& lo, bf16x8& hi) {
  u32x4 a, b;
#pragma unroll
  for (int i = 0; i < 4; ++i) { a[i] = pack_bf16(p[2 * i], p[2 * i + 1]); b[i] = pack_bf16(p[8 + 2 * i], p[9 + 2 * i]); }
  lo = __builtin_bit_cast(bf16x8, a); hi = __builtin_bit_cast(bf16x8, b);
}
DI float gelu_tanh(float u) {
  const float z = 0.7978845608028654f * (u + 0.044715f * u * u * u);
  const float e = __builtin_amdgcn_exp2f(-2.885390081777927f * z);
  return u * __builtin_amdgcn_rcpf(1.f + e);
}

DI void wslice_block(const u16* w, unsigned lds0, int lw, int lane, int blk) {
  const char* src = (const char*)w + lw * (160 * 1280);
  if (blk < 5) {
    const int r = lane >> 3, pos = lane & 7;
    const unsigned pe = (((pos >> 1) ^ ((r >> 1) & 1)) << 1 | (pos & 1)) * 16;
    const unsigned v0 = r * 1280 + pe, v1 = r * 1280 + (pe ^ 64) + 9216;
    glds4<false>(src + blk * (32 * 1280), lds0 + L_W + lw * 20480 + blk * 4096, v0, v1, v0 + 18432, v1 + 18432);
  } else {
    const unsigned t0 = (lane >> 1) * 1280 + 128 + (lane & 1) * 16;
    if (blk == 5) glds4<false>(src, lds0 + L_WT + lw * 5120, t0, t0 + 39936, t0 + 2 * 39936, t0 + 3 * 39936);
    else glds1<false>(src + 128 * 1280, lds0 + L_WT + lw * 5120 + 4096, t0);
  }
}

DI void touch(const void* sbase, unsigned voff, unsigned& sink) {
  asm volatile("s_nop 4\n\tglobal_load_dword %0, %1, %2" : "+v"(sink) : "v"(voff), "s"(sbase) : "memory");
}
DI float xsum16(float x) {
  auto r = __builtin_amdgcn_permlane16_swap(__float_as_uint(x), __float_as_uint(x), false, false);
  return __uint_as_float(r[0]) + __uint_as_float(r[1]);
}
DI float xsum32(float x) {
  auto r = __builtin_amdgcn_permlane32_swap(__float_as_uint(x), __float_as_uint(x), false, false);
  return __uint_as_float(r[0]) + __uint_as_float(r[1]);
}
DI f32x4 ldf4(lds_cptr p) { return *(const LAS f32x4*)p; }

DI f32x4 sum4(lds_cptr lds, int t, int nt, int lane) {
  f32x4 s = *(const LAS f32x4*)(lds + t * 1024 + lane * 16);
#pragma unroll
  for (int k = 1; k < 4; ++k) s += *(const LAS f32x4*)(lds + (k * nt + t) * 1024 + lane * 16);
  return s;
}
DI void ln_finish(float s1, float s2, float& mu, float& var) { mu = s1 * (1.f / D_); var = fmaxf(s2 * (1.f / D_) - mu * mu, 0.f); }

constexpr int ATT_CH = 4;
constexpr int ATT_CHB = ATT_CH * KV_TILE;
constexpr int ATT_SLOT = 10752;
constexpr int ATT_NCH = S_ / 32 / ATT_CH;
constexpr int w2_slot(int st) { return (st % 9) < 4 ? L_RING + (st % 9) * 16384 : ((st % 9) - 4) * 16384; }
template <int N> struct IC { static constexpr int v = N; };
template <int I, int N, class F> DI void static_for(F&& f) { if constexpr (I < N) { f(IC<I>{}); static_for<I + 1, N>(f); } }

#define STAMP(i) do { } while (0)

__global__ __launch_bounds__(512, 2) void mega_kernel(const u16* __restrict__ x0, const u16* __restrict__ wq, const u16* __restrict__ wk,
                                                      const u16* __restrict__ wv, const u16* __restrict__ wo, const u16* __restrict__ w1,
                                                      const u16* __restrict__ b1, const u16* __restrict__ w2, const u16* __restrict__ b2,
                                                      u16* __restrict__ out, char* ws) {
  extern __shared__ __attribute__((aligned(16))) char smem[];
  const int tid = threadIdx.x;
  const unsigned lds0 = (unsigned)(uintptr_t)smem;
  const lds_cptr lds = (lds_cptr)smem;
  const lds_ptr ldw = (lds_ptr)smem;
#define FRESH() \
  int lane = tid & 63; asm volatile("" : "+v"(lane)); \
  int wave = UNI(tid >> 6); asm volatile("" : "+s"(wave)); \
  int bid = blockIdx.x; asm volatile("" : "+s"(bid)); \
  const int xcd = bid & 7, idx = bid >> 3, tb = xcd * 4 + (idx >> 3), j = idx & 7; \
  const int g = lane >> 4, c16 = lane & 15, kq = wave & 3, th = wave >> 2; \
  const int ea = wave & 3, ew0 = th ? 3 : 0, ewn = th ? 2 : 3; \
  const unsigned res_off = L_RES + (ea * 16 + c16) * 160 + 8 * g; \
  const int panel4 = (xcd & 1) * 4 + (idx & 3), ft = (xcd >> 1) * 7 + (idx >> 2); \
  const bool has4 = idx < 28 && ft < 27; \
  (void)xcd; (void)g; (void)c16; (void)kq; (void)th; (void)ea; (void)ew0; (void)ewn; (void)res_off; (void)tb; (void)j; (void)panel4; (void)has4
  const rsrc_t rws = __builtin_amdgcn_make_buffer_rsrc(ws, 0, WS_END, 0x00020000);
  unsigned tag = 0;
  bool dead = false;

#pragma unroll 1
  for (int layer = 0; layer < 2; ++layer) {
    const unsigned fl = WS_FLAGS + layer * FL_LAYER;
    const unsigned kvoff = WS_KV + layer * KV_LAYER;
    unsigned sink = 0;
    {
      FRESH();
      STAMP(0);
      if (wave >= 4) {
        if (layer > 0) {
#pragma unroll 1
          for (int blk = 0; blk < 7; ++blk) wslice_block(wq + j * 80, lds0, wave - 4, lane, blk);
        }
      } else {
        {
          const char* xs = (const char*)((j & 1) ? wv : wk);
          if (wave < 3) {
            glds_lin<4, false>(xs + wave * 8192, lds0 + L_ATT + wave * 8192, lane * 16);
            glds_lin<4, false>(xs + wave * 8192 + 4096, lds0 + L_ATT + wave * 8192 + 4096, lane * 16);
          } else glds1<false>(xs + 24576, lds0 + L_ATT + 24576, lane * 16);
        }
        if (layer == 0) {
#pragma unroll 1
          for (int pc = wave; pc < 10; pc += 4) {
            const int n = pc * 64 + lane, row = n / 10, ch = n - row * 10;
            glds1<false>(x0 + (long)tb * 64 * D_ + j * 80, lds0 + L_RES + pc * 1024, row * 1280 + ch * 16);
          }
        }
      }
      rsrc_t ra = rws; unsigned abase, astr_t, astr_k;
      if (layer == 0) {
        ra = __builtin_amdgcn_make_buffer_rsrc((void*)x0, 0, XB, 0x00020000);
        abase = (tb * 64 + th * 32 + c16) * (D_ * 2) + kq * 64 + g * 16; astr_t = 16 * D_ * 2; astr_k = 256;
      } else {
        if (wave == 0) poll_flags(ws, WS_FLAGS + FL_X + tb * 128, 8, tag, lane, dead);
        wait_vbar<0>();
        abase = WS_X + tb * 81920 + (kq * 4 + th * 2) * 1024 + lane * 16; astr_t = 1024; astr_k = 16384;
      }
      STAMP(1);
      u32x4 af[5][2];
#pragma unroll
      for (int s = 0; s < 5; ++s)
#pragma unroll
        for (int t = 0; t < 2; ++t) af[s][t] = ld16_dev_s(ra, abase, s * astr_k + t * astr_t);
      if (layer == 0) {
        const unsigned epoch = __hip_atomic_load((gu32*)(ws + WS_EPOCH), RLX_AGENT);
        wait_vbar<0>();
        tag = UNI(epoch) + 1u;
        if (wave >= 4) {
#pragma unroll 1
          for (int blk = 0; blk < 7; ++blk) wslice_block(wq + j * 80, lds0, wave - 4, lane, blk);
        }
      }
      bf16x8 ones;
#pragma unroll
      for (int i = 0; i < 8; ++i) ones[i] = (short)0x3f80;
      const f32x4 z4 = {0.f, 0.f, 0.f, 0.f};
      const int q = c16 >> 2, p = lane & 3;
      const int row = 32 * kq + 8 * g + q;
      const unsigned fsw = (((q >> 1) & 1) | ((g & 1) << 1)) * 32;
      const unsigned woff = L_W + row * 128 + 8 * p, toff = L_WT + row * 32 + 8 * p, xoff = L_ATT + row * 40 + 8 * p;
      LAS float* stat = (LAS float*)(ldw + L_DUMP);
      LAS float* csum = (LAS float*)(ldw + L_KVP + 20480);
      const int tk = j >> 1;
      const bool isv = j & 1;
      {
        f32x4 a1s[2] = {z4, z4}, a2s[2] = {z4, z4}, akv[2] = {z4, z4}, aok[2] = {z4, z4};
#pragma unroll
        for (int s = 0; s < 5; ++s) {
          const bf16x8 a0 = __builtin_bit_cast(bf16x8, af[s][0]), a1 = __builtin_bit_cast(bf16x8, af[s][1]);
          if (th == (tk >> 1)) {
            const bf16x8 a = (tk & 1) ? a1 : a0;
            const bf16x8 x0f = lds_tr8(lds + xoff + s * 5120, 160), x1f = lds_tr8(lds + xoff + s * 5120 + 32, 160);
            if (!isv) {
              akv[0] = MFMA16(x0f, a, akv[0]); akv[1] = MFMA16(x1f, a, akv[1]);
              aok[0] = MFMA16(x0f, ones, aok[0]); aok[1] = MFMA16(x1f, ones, aok[1]);
            } else {
              akv[0] = MFMA16(a, x0f, akv[0]); akv[1] = MFMA16(a, x1f, akv[1]);
              aok[0] = MFMA16(ones, x0f, aok[0]); aok[1] = MFMA16(ones, x1f, aok[1]);
            }
          }
          a1s[0] = MFMA16(ones, a0, a1s[0]); a1s[1] = MFMA16(ones, a1, a1s[1]);
          a2s[0] = MFMA16(a0, a0, a2s[0]); a2s[1] = MFMA16(a1, a1, a2s[1]);
        }
        if (g == q) {
#pragma unroll
          for (int t = 0; t < 2; ++t) {
            const float d = (p & 2) ? ((p & 1) ? a2s[t][3] : a2s[t][2]) : ((p & 1) ? a2s[t][1] : a2s[t][0]);
            *(LAS f32x2*)(stat + (kq * 64 + th * 32 + t * 16 + c16) * 2) = f32x2{a1s[t][0], d};
          }
        }
        if (th == (tk >> 1)) {
          const bool l2 = isv ? (c16 < 4) : (g == 0);
          const int i2 = isv ? g * 4 + c16 : c16;
          const int o = L_KVP + kq * 2560;
          *(LAS f32x4*)(ldw + o + lane * 16) = akv[0];
          *(LAS f32x4*)(ldw + o + 1280 + lane * 16) = aok[0];
          if (l2) { *(LAS f32x4*)(ldw + o + 1024 + i2 * 16) = akv[1]; *(LAS f32x4*)(ldw + o + 2304 + i2 * 16) = aok[1]; }
        }
      }
      wait_bar<0>();
      float mu, rs;
      {
        f32x2 sv[4];
#pragma unroll
        for (int k = 0; k < 4; ++k) sv[k] = *(const LAS f32x2*)(stat + (k * 64 + ea * 16 + c16) * 2);
        float var; ln_finish((sv[0][0] + sv[1][0]) + (sv[2][0] + sv[3][0]), (sv[0][1] + sv[1][1]) + (sv[2][1] + sv[3][1]), mu, var);
        mu = rnd1(mu); rs = ln_rsd(var);
      }
      if (ea == tk) {
        const int li = th == 0 ? lane : 64 + (isv ? g * 4 + (c16 & 3) : c16);
        f32x4 s = z4, cs = z4;
#pragma unroll
        for (int k = 0; k < 4; ++k) { s += ldf4(lds + L_KVP + k * 2560 + li * 16); cs += ldf4(lds + L_KVP + k * 2560 + 1280 + li * 16); }
        const unsigned tile = kvoff + (tb * 2 + (ea >> 1)) * KV_TILE;
        if (!isv) {
          u32x2 w; w[0] = pack_bf16((s[0] - mu * cs[0]) * rs, (s[1] - mu * cs[1]) * rs); w[1] = pack_bf16((s[2] - mu * cs[2]) * rs, (s[3] - mu * cs[3]) * rs);
          const int r32 = (ea & 1) * 16 + c16;
          if (th == 0) st8_dev(rws, tile + ((g >> 1) * 32 + r32) * 16 + (g & 1) * 8, w);
          else if (g == 0) st8_dev(rws, tile + KV_KT + r32 * 8, w);
        } else {
          float o[4];
#pragma unroll
          for (int e = 0; e < 4; ++e) o[e] = (s[e] - __shfl(mu, 4 * g + e) * cs[e]) * __shfl(rs, 4 * g + e);
          const int d = th * 16 + c16;
          u32x2 w; w[0] = pack_bf16(o[0], o[1]); w[1] = pack_bf16(o[2], o[3]);
          if (d == DH_) { w[0] = 0x3f803f80u; w[1] = 0x3f803f80u; }
          if (d == DH_ + 1) { w[0] = 0u; w[1] = 0u; }
          if (d <= DH_ + 1) st8_dev(rws, tile + KV_V + (ea & 1) * KV_VS + ((g & 1) * 22 + d) * 16 + (g >> 1) * 8, w);
        }
      }
      f32x4 acc[5][2], aon[3] = {z4, z4, z4};
#pragma unroll
      for (int i = 0; i < 5; ++i) { acc[i][0] = z4; acc[i][1] = z4; }
#pragma unroll
      for (int s = 0; s < 5; ++s) {
        const bf16x8 a0 = __builtin_bit_cast(bf16x8, af[s][0]), a1 = __builtin_bit_cast(bf16x8, af[s][1]);
        bf16x8 wf[5];
#pragma unroll
        for (int i = 0; i < 4; ++i) wf[i] = lds_tr8(lds + woff + s * 16384 + ((i * 32) ^ fsw), 512);
        wf[4] = lds_tr8(lds + toff + s * 4096, 128);
#pragma unroll
        for (int i = 0; i < 5; ++i) { acc[i][0] = MFMA16(wf[i], a0, acc[i][0]); acc[i][1] = MFMA16(wf[i], a1, acc[i][1]); }
        if (th == 0) { aon[0] = MFMA16(wf[0], ones, aon[0]); aon[1] = MFMA16(wf[1], ones, aon[1]); aon[2] = MFMA16(wf[2], ones, aon[2]); }
        else { aon[0] = MFMA16(wf[3], ones, aon[0]); aon[1] = MFMA16(wf[4], ones, aon[1]); }
        if (s == 1) {
          publish(ws, fl + FL_KV + (tb * 8 + j) * 4, 32, 1024, tag, wave, lane);
          if (wave >= 4) {
            const char* w1w = (const char*)(w1 + (has4 ? ft : 0) * 64) + (long)((idx & 3) * 512 + kq * 128) * (F_ * 2);
            touch(w1w, lane * (F_ * 2), sink); touch(w1w + 64 * (F_ * 2), lane * (F_ * 2), sink);
            touch((const char*)(w2 + tb * 64) + (long)(j * 216 + kq * 54) * (S_ * 2), (lane < 54 ? lane : 53) * (S_ * 2), sink);
          }
        }
      }
      bar_lds();
#pragma unroll
      for (int i = 0; i < 5; ++i)
#pragma unroll
        for (int t = 0; t < 2; ++t) *(LAS f32x4*)(ldw + ((kq * 5 + i) * 4 + th * 2 + t) * 1024 + lane * 16) = acc[i][t];
      if (c16 == 0) {
#pragma unroll
        for (int i = 0; i < 3; ++i) if (i < ewn) *(LAS f32x4*)(csum + (kq * 7 + ew0 + i) * 16 + 4 * g) = aon[i];
      }
      bar_lds();
      {
        const float r = rs * QSCALE;
        f32x4 s[3], cs[3];
#pragma unroll
        for (int i = 0; i < 3; ++i) {
          const int wt = min(ew0 + i, 4);
          s[i] = sum4(lds, wt * 4 + ea, 20, lane);
          cs[i] = z4;
#pragma unroll
          for (int k = 0; k < 4; ++k) cs[i] += *(const LAS f32x4*)(csum + (k * 7 + wt) * 16 + 4 * g);
        }
#pragma unroll
        for (int i = 0; i < 3; ++i) {
          if (i < ewn) {
            u32x2 w;
            w[0] = pack_bf16((s[i][0] - mu * cs[i][0]) * r, (s[i][1] - mu * cs[i][1]) * r);
            w[1] = pack_bf16((s[i][2] - mu * cs[i][2]) * r, (s[i][3] - mu * cs[i][3]) * r);
            *(LAS u32x2*)(ldw + L_QT + (ea * 16 + c16) * 160 + (ew0 + i) * 32 + 8 * g) = w;
          }
        }
      }
      bar_lds();
    }
    STAMP(2);
    bf16x8 w1r[16][2];
    {
      FRESH();
      const int r32 = lane & 31, hi = lane >> 5;
      const int hh = wave & 3, trow = th * 32 + r32;
      bf16x8 q16; bf16x4 q4;
      {
        const lds_cptr qp = lds + L_QT + trow * 160 + hh * 40;
        const u32x2 a = *(const LAS u32x2*)(qp + hi * 16), b = *(const LAS u32x2*)(qp + hi * 16 + 8), c = *(const LAS u32x2*)(qp + 32);
        u32x4 v = {a[0], a[1], b[0], b[1]};
        q16 = __builtin_bit_cast(bf16x8, v);
        u32x2 t = {hi ? 0u : c[0], hi ? 0u : c[1]};
        q4 = __builtin_bit_cast(bf16x4, t);
        PIN(q16); PIN(q4);
      }
      if (wave == 0) {
        unsigned spins = 0;
        while (!dead) {
          const u32x4 v = ld16_dev(rws, fl + FL_KV + (bid & 31) * 1024 + lane * 16);
          if (__all(v[0] == tag && v[1] == tag && v[2] == tag && v[3] == tag)) break;
          if (++spins > SPIN_CAP) dead = true;
          __builtin_amdgcn_s_sleep(1);
          asm volatile("" ::: "memory");
        }
      }
      bar_lds();
      STAMP(3);
      const unsigned koff = lane * 16;
      const char* KV = ws + kvoff + wave * 3072;
      auto issue_kv = [&](int ch) {
        const unsigned dst = lds0 + L_ATT + (ch % 3) * ATT_SLOT + wave * 3072;
        if (wave < 3) glds_lin<3, true>(KV + ch * ATT_CHB, dst, koff);
        else { glds1<true>(KV + ch * ATT_CHB, dst, koff); if (lane < 32) glds1<true>(KV + ch * ATT_CHB + 1024, dst + 1024, koff); }
      };
      const char* w1s = (const char*)(w1 + (has4 ? ft : 0) * 64) + (long)wave * 16 * (F_ * 2);
      unsigned sv0, sv1;
      {
        const int r = lane >> 3, pos = lane & 7;
        const unsigned pe = (((pos >> 1) ^ ((r >> 1) & 1)) << 1 | (pos & 1)) * 16;
        sv0 = r * (F_ * 2) + pe; sv1 = (r + 8) * (F_ * 2) + (pe ^ 64) - 1024;
      }
      const unsigned fsw = (((c16 >> 3) & 1) | ((g & 1) << 1)) * 32;
      const unsigned srow = (32 * kq + 8 * g + (c16 >> 2)) * 128 + 8 * (lane & 3);
      const unsigned sr0 = srow + ((64 * th) ^ fsw), sr1 = srow + ((64 * th + 32) ^ fsw);
      if (wave < 4) { issue_kv(0); issue_kv(1); }
      glds2(w1s, lds0 + L_STG + wave * 2048, sv0, sv1);
      const unsigned ktoff = KV_KT + r32 * 8, voff = KV_V + (hi * 22 + (r32 < 21 ? r32 : 21)) * 16;
      f32x16 o = {}, negm, sA, sB;
      bf16x8 k16, v0, v1; bf16x4 k4; u32x4 p0, p1;
      if (wave < 3) wait_bar<5>(); else if (wave == 3) wait_bar<4>(); else bar_lds();
      const lds_cptr ring = lds + L_ATT;
      {
        k16 = lds_b128(ring + koff);
        k4 = *(const LAS bf16x4*)(ring + ktoff);
        f32x16 z = {};
        z = MFMA32(k16, q16, z); z = MFMA32K8(k4, q4, z);
        k16 = lds_b128(ring + KV_TILE + koff);
        k4 = *(const LAS bf16x4*)(ring + KV_TILE + ktoff);
        v1 = lds_b128(ring + voff);
        const float m = rowmax16(z);
#pragma unroll
        for (int i = 0; i < 16; ++i) { negm[i] = -m; sA[i] = z[i] - m; }
        p1 = u32x4{0u, 0u, 0u, 0u};
      }
#define EX(x) __builtin_amdgcn_exp2f(x)
#define EXP4(X, i) do { X[i] = EX(X[i]); X[i + 1] = EX(X[i + 1]); X[i + 2] = EX(X[i + 2]); X[i + 3] = EX(X[i + 3]); } while (0)
#define BF8(x) __builtin_bit_cast(bf16x8, x)
#define PK4(P, X, i) do { P[0] = pack_bf16(X[i], X[i + 1]); P[1] = pack_bf16(X[i + 2], X[i + 3]); P[2] = pack_bf16(X[i + 4], X[i + 5]); P[3] = pack_bf16(X[i + 6], X[i + 7]); } while (0)
#define STEP(C, N, kn, vc) do { \
    N = MFMA32(k16, q16, negm); SBAR(); \
    v0 = lds_b128((vc) + voff); EXP4(C, 0); PIN(C); SBAR(); \
    o = MFMA32(v1, BF8(p1), o); SBAR(); \
    k16 = lds_b128((kn) + koff); EXP4(C, 4); PIN(C); SBAR(); PK4(p0, C, 0); PIN(p0); SBAR(); \
    N = MFMA32K8(k4, q4, N); SBAR(); \
    v1 = lds_b128((vc) + voff + KV_VS); EXP4(C, 8); PIN(C); SBAR(); \
    o = MFMA32(v0, BF8(p0), o); SBAR(); \
    k4 = *(const LAS bf16x4*)((kn) + ktoff); EXP4(C, 12); PIN(C); SBAR(); PK4(p1, C, 8); PIN(p1); SBAR(); \
  } while (0)
      static_for<0, ATT_NCH>([&](auto CH) {
        constexpr int ch = decltype(CH)::v;
        const lds_cptr cb = ring + (ch % 3) * ATT_SLOT;
        const lds_cptr nb = (ch + 1 < ATT_NCH) ? ring + ((ch + 1) % 3) * ATT_SLOT : cb;
        __builtin_amdgcn_s_setprio(1);
        STEP(sA, sB, cb + 2 * KV_TILE, cb);
        __builtin_amdgcn_s_setprio(0);
        STEP(sB, sA, cb + 3 * KV_TILE, cb + KV_TILE);
        if (wave < 4) wait_vbar<(ch >= 1 && ch <= 5) ? 4 : 0>(); else wait_vbar<0>();
        if (ch + 2 < ATT_NCH) { if (wave < 4) issue_kv(ch + 2); }
        if (ch + 1 < 16) glds2(w1s + (long)(ch + 1) * 128 * (F_ * 2), lds0 + ((ch & 1) ? L_STG : L_WT) + wave * 2048, sv0, sv1);
        if (ch < 5) { if (wave < 4) wslice_block(wo + j * 80, lds0, wave, lane, ch); }
        w1r[ch][0] = lds_tr8(lds + ((ch & 1) ? L_WT : L_STG) + sr0, 512); w1r[ch][1] = lds_tr8(lds + ((ch & 1) ? L_WT : L_STG) + sr1, 512);
        __builtin_amdgcn_s_setprio(3);
        SBAR();
        STEP(sA, sB, nb, cb + 2 * KV_TILE);
        PIN(w1r[ch][0]); PIN(w1r[ch][1]);
        __builtin_amdgcn_s_setprio(2);
        SBAR();
        STEP(sB, sA, nb + KV_TILE, cb + 3 * KV_TILE);
      });
      __builtin_amdgcn_s_setprio(0);
      o = MFMA32(v1, BF8(p1), o);
      float l = xsum32(hi ? o[8] : 0.f);
      bool bad = !(l < 1.8446744e19f);
#pragma unroll
      for (int i = 0; i < 12; ++i) bad |= !(__builtin_fabsf(o[i]) < 3.0e38f);
      if (__builtin_expect(__any(bad), 0)) {
        float m = -INFINITY;
        o = f32x16{};
#pragma unroll 1
        for (int t = 0; t < S_ / 32; ++t) {
          const unsigned tp = kvoff + t * KV_TILE;
          const bf16x8 a16 = BF8(ld16_dev(rws, tp + koff));
          const u32x2 a4 = __builtin_amdgcn_raw_buffer_load_b64(rws, tp + ktoff, 0, 16);
          const bf16x8 w0 = BF8(ld16_dev(rws, tp + voff)), w1f = BF8(ld16_dev(rws, tp + voff + KV_VS));
          f32x16 z = {};
          z = MFMA32(a16, q16, z); z = MFMA32K8(__builtin_bit_cast(bf16x4, a4), q4, z);
          const float mn = __builtin_fmaxf(m, rowmax16(z));
          const float alpha = __builtin_amdgcn_exp2f(m - mn);
          m = mn;
#pragma unroll
          for (int i = 0; i < 16; ++i) { z[i] = __builtin_amdgcn_exp2f(z[i] - m); o[i] *= alpha; }
          bf16x8 a0, a1; pack_p(z, a0, a1);
          o = MFMA32(w0, a0, o); o = MFMA32(w1f, a1, o);
        }
        l = xsum32(hi ? o[8] : 0.f);
      }
      const float rl = __builtin_amdgcn_rcpf(l);
      const unsigned ob = WS_O + layer * XB + tb * 81920 + ((trow >> 4) * 64 + (trow & 15)) * 16;
#pragma unroll
      for (int gq = 0; gq < 3; ++gq) {
        const int d0 = 8 * gq + 4 * hi;
        if (d0 < DH_) {
          const int k = (j * 4 + hh) * DH_ + d0;
          u32x2 w; w[0] = pack_bf16(o[4 * gq] * rl, o[4 * gq + 1] * rl); w[1] = pack_bf16(o[4 * gq + 2] * rl, o[4 * gq + 3] * rl);
          st8_dev(rws, ob + (k >> 5) * 4096 + ((k & 31) >> 3) * 256 + (k & 7) * 2, w);
        }
      }
      STAMP(4);
      publish(ws, fl + FL_O + tb * 128 + j * 4, 1, 0, tag, wave, lane);
      asm volatile("" :: "v"(sink));
      if (wave >= 4) { wslice_block(wo + j * 80, lds0, kq, lane, 5); wslice_block(wo + j * 80, lds0, kq, lane, 6); }
    }
    {
      FRESH();
      if (wave == 0) poll_flags(ws, fl + FL_O + tb * 128, 8, tag, lane, dead);
      wait_vbar<0>();
      STAMP(5);
      const unsigned abase = WS_O + layer * XB + tb * 81920 + (kq * 4 + th * 2) * 1024 + lane * 16;
      u32x4 af[5][2];
#pragma unroll
      for (int s = 0; s < 5; ++s)
#pragma unroll
        for (int t = 0; t < 2; ++t) af[s][t] = ld16_dev_s(rws, abase + t * 1024, s * 16384);
      f32x4 acc[5][2];
#pragma unroll
      for (int i = 0; i < 5; ++i) { acc[i][0] = f32x4{0.f, 0.f, 0.f, 0.f}; acc[i][1] = acc[i][0]; }
      const int q = c16 >> 2, p = lane & 3;
      const int row = 32 * kq + 8 * g + q;
      const unsigned fsw = (((q >> 1) & 1) | ((g & 1) << 1)) * 32;
      const unsigned woff = L_W + row * 128 + 8 * p, toff = L_WT + row * 32 + 8 * p;
      bf16x8 wf[2][5];
#pragma unroll
      for (int i = 0; i < 5; ++i) wf[0][i] = i < 4 ? lds_tr8(lds + woff + ((i * 32) ^ fsw), 512) : lds_tr8(lds + toff, 128);
#pragma unroll
      for (int s = 0; s < 5; ++s) {
        const bf16x8 a0 = __builtin_bit_cast(bf16x8, af[s][0]), a1 = __builtin_bit_cast(bf16x8, af[s][1]);
#pragma unroll
        for (int i = 0; i < 5; ++i) {
          if (s < 4) wf[(s + 1) & 1][i] = i < 4 ? lds_tr8(lds + woff + (s + 1) * 16384 + ((i * 32) ^ fsw), 512) : lds_tr8(lds + toff + (s + 1) * 4096, 128);
          acc[i][0] = MFMA16(wf[s & 1][i], a0, acc[i][0]); acc[i][1] = MFMA16(wf[s & 1][i], a1, acc[i][1]);
          SBAR();
        }
      }
      bar_lds();
#pragma unroll
      for (int i = 0; i < 5; ++i)
#pragma unroll
        for (int t = 0; t < 2; ++t) *(LAS f32x4*)(ldw + ((kq * 5 + i) * 4 + th * 2 + t) * 1024 + lane * 16) = acc[i][t];
      bar_lds();
      gu64* gr = (gu64*)(ws + WS_STAT + layer * ST_LAYER + (tb * 4 + ea) * 4096);
      {
        f32x4 s[3]; u32x2 r[3];
#pragma unroll
        for (int i = 0; i < 3; ++i) {
          const int wt = min(ew0 + i, 4);
          s[i] = sum4(lds, wt * 4 + ea, 20, lane);
          r[i] = *(const LAS u32x2*)(lds + res_off + wt * 32);
        }
        float s1 = 0.f, s2 = 0.f;
#pragma unroll
        for (int i = 0; i < 3; ++i) {
          if (i < ewn) {
            u32x2 w;
            w[0] = pack_bf16(bf_lo(r[i][0]) + s[i][0], bf_hi(r[i][0]) + s[i][1]);
            w[1] = pack_bf16(bf_lo(r[i][1]) + s[i][2], bf_hi(r[i][1]) + s[i][3]);
            *(LAS u32x2*)(ldw + res_off + (ew0 + i) * 32) = w;
            const float a = bf_lo(w[0]), b = bf_hi(w[0]), c = bf_lo(w[1]), d = bf_hi(w[1]);
            s1 += (a + b) + (c + d);
            s2 += (a * a + b * b) + (c * c + d * d);
          }
        }
        s1 = xsum32(xsum16(s1)); s2 = xsum32(xsum16(s2));
        if (lane < 32) __hip_atomic_store(gr + (2 * j + th) * 32 + lane, ((unsigned long long)tag << 32) | __float_as_uint(g ? s2 : s1), RLX_AGENT);
      }
      bar_lds();
      if (wave >= 4) {
        const int r = lane >> 3, pos = lane & 7;
        const unsigned pe = (((pos >> 1) ^ ((r >> 1) & 1)) << 1 | (pos & 1)) * 16;
        const unsigned v0 = r * (S_ * 2) + pe + 4096, v1 = (r + 8) * (S_ * 2) + (pe ^ 64) + 3072, vstep = 16 * (S_ * 2) - 2048;
        const char* wsrc = (const char*)(w2 + tb * 64) - 4096 + (long)kq * 32 * (S_ * 2);
#pragma unroll 1
        for (int st = 0; st < 4; ++st) glds4<false>(wsrc + (long)st * 128 * (S_ * 2), lds0 + L_RING + st * 16384 + kq * 4096, v0, v1, v0 + vstep, v1 + vstep);
        asm volatile("s_barrier" ::: "memory");
      } else {
        u32x2 xv[5];
#pragma unroll
        for (int wt = 0; wt < 5; ++wt) xv[wt] = __builtin_bit_cast(u32x2, lds_tr(lds + L_RES + (ea * 16 + 4 * g + q) * 160 + wt * 32 + 8 * p));
        float tot;
        {
          unsigned spins = 0;
          for (;;) {
            bool ok = true; tot = 0.f;
#pragma unroll
            for (int i = 0; i < 8; ++i) {
              const unsigned long long v = __hip_atomic_load(gr + i * 64 + lane, RLX_AGENT);
              tot += __uint_as_float((unsigned)v); ok &= (unsigned)(v >> 32) == tag;
            }
            if (__all(ok) || dead) break;
            if (++spins > SPIN_CAP) dead = true;
            __builtin_amdgcn_s_sleep(1);
          }
        }
        STAMP(6);
        tot = xsum32(tot);
        float mu4[4], rr4[4], m24[4], r24[4];
#pragma unroll
        for (int e = 0; e < 4; ++e) { mu4[e] = __shfl(tot, 4 * g + e); rr4[e] = __shfl(tot, 16 + 4 * g + e); }
#pragma unroll
        for (int e = 0; e < 4; ++e) {
          float mu, var; ln_finish(mu4[e], rr4[e], mu, var);
          mu4[e] = rnd1(mu); rr4[e] = ln_rsd(var);
          m24[e] = rnd1((mu - mu4[e]) * rr4[e]); r24[e] = ln_rsd(var * rr4[e] * rr4[e]);
        }
        const unsigned hb = WS_H + layer * XB + j * 327680 + ((tb * 2 + (ea >> 1)) * 5) * 1024 + (c16 + 16 * ((ea & 1) * 2 + (g >> 1))) * 16 + (g & 1) * 8;
#pragma unroll
        for (int wt = 0; wt < 5; ++wt) {
          const u32x2 v = xv[wt];
          u32x2 w;
          w[0] = pack_bf16(bf_lo(v[0]) - mu4[0], bf_hi(v[0]) - mu4[1]); w[1] = pack_bf16(bf_lo(v[1]) - mu4[2], bf_hi(v[1]) - mu4[3]);
          w[0] = pack_bf16(bf_lo(w[0]) * rr4[0], bf_hi(w[0]) * rr4[1]); w[1] = pack_bf16(bf_lo(w[1]) * rr4[2], bf_hi(w[1]) * rr4[3]);
          w[0] = pack_bf16(bf_lo(w[0]) - m24[0], bf_hi(w[0]) - m24[1]); w[1] = pack_bf16(bf_lo(w[1]) - m24[2], bf_hi(w[1]) - m24[3]);
          w[0] = pack_bf16(bf_lo(w[0]) * r24[0], bf_hi(w[0]) * r24[1]); w[1] = pack_bf16(bf_lo(w[1]) * r24[2], bf_hi(w[1]) * r24[3]);
          st8_dev(rws, hb + wt * 1024, w);
        }
        publish(ws, fl + FL_H + j * 512 + tb * 4, 4, 128, tag, wave, lane);
      }
      STAMP(7);
    }
    {
      FRESH();
      if (has4) {
        const int f = ft * 64 + ea * 16 + 4 * g;
        const u32x2 bias = *(const u32x2*)(b1 + f);
        if (wave == 0) poll_flags(ws, fl + FL_H + panel4 * 512 + ((idx >> 2) & 3) * 128, 32, tag, lane, dead);
        asm volatile("s_barrier" ::: "memory");
        STAMP(8);
        const unsigned l16 = lane * 16;
        const int po = (th ? 12 + 2 * kq : 3 * kq) * 1024;
        const char* hsrc = ws + WS_H + layer * XB + panel4 * 327680 + po;
        auto issue = [&](int st) {
          if (th == 0) glds_lin<3, true>(hsrc + st * 20480, lds0 + (st & 3) * 20480 + po, l16);
          else glds_lin<2, true>(hsrc + st * 20480, lds0 + (st & 3) * 20480 + po, l16);
        };
        issue(0); issue(1); issue(2);
        f32x4 acc[2][5];
#pragma unroll
        for (int i = 0; i < 2; ++i)
#pragma unroll
          for (int a = 0; a < 5; ++a) acc[i][a] = f32x4{0.f, 0.f, 0.f, 0.f};
        const lds_cptr ab = lds + kq * 5120 + l16;
        static_for<0, 16>([&](auto ST) {
          constexpr int st = decltype(ST)::v;
          constexpr int ahead = (15 - st) < 2 ? (15 - st) : 2;
          if (th == 0) wait_bar<3 * ahead>(); else wait_bar<2 * ahead>();
          bf16x8 af[5];
#pragma unroll
          for (int a = 0; a < 5; ++a) af[a] = lds_b128(ab + (st & 3) * 20480 + a * 1024);
          if (st + 3 < 16) { if (th == 0) issue(st + 3); }
          SBAR();
#pragma unroll
          for (int a = 0; a < 5; ++a) { acc[0][a] = MFMA16(w1r[st][0], af[a], acc[0][a]); acc[1][a] = MFMA16(w1r[st][1], af[a], acc[1][a]); }
          SBAR();
          if (st + 3 < 16) { if (th == 1) issue(st + 3); }
        });
        bar_lds();
#pragma unroll
        for (int i = 0; i < 2; ++i)
#pragma unroll
          for (int a = 0; a < 5; ++a) *(LAS f32x4*)(ldw + ((kq * 4 + 2 * th + i) * 5 + a) * 1024 + l16) = acc[i][a];
        bar_lds();
        STAMP(9);
        const unsigned ub = WS_U + layer * UB + ((panel4 * U_KS + (f >> 5)) * 5) * 1024 + (c16 + 16 * ((f & 31) >> 3)) * 16 + (f & 7) * 2;
        f32x4 sv[3];
#pragma unroll
        for (int i = 0; i < 3; ++i) sv[i] = sum4(lds, ea * 5 + min(ew0 + i, 4), 20, lane);
#pragma unroll
        for (int i = 0; i < 3; ++i) {
          if (i < ewn) {
            const f32x4 s = sv[i];
            u32x2 w;
            w[0] = pack_bf16(gelu_tanh(s[0] + bf_lo(bias[0])), gelu_tanh(s[1] + bf_hi(bias[0])));
            w[1] = pack_bf16(gelu_tanh(s[2] + bf_lo(bias[1])), gelu_tanh(s[3] + bf_hi(bias[1])));
            st8_dev(rws, ub + (ew0 + i) * 1024, w);
          }
        }
        publish(ws, fl + FL_U + panel4 * 512 + ft * 4, 4, 128, tag, wave, lane);
      }
    }
    STAMP(10);
    {
      FRESH();
      const int tok = tb * 64 + ea * 16 + c16;
      const float bias = __uint_as_float((unsigned)b2[tok] << 16);
      if (wave >= 4) {
        const int r = lane >> 3, pos = lane & 7;
        const unsigned pe = (((pos >> 1) ^ ((r >> 1) & 1)) << 1 | (pos & 1)) * 16;
        const unsigned v0 = r * (S_ * 2) + pe + 4096, v1 = (r + 8) * (S_ * 2) + (pe ^ 64) + 3072, vstep = 16 * (S_ * 2) - 2048;
        const char* wsrc = (const char*)(w2 + tb * 64) - 4096 + (long)kq * 32 * (S_ * 2);
#pragma unroll 1
        for (int st = 4; st < 9; ++st) glds4<false>(wsrc + (long)st * 128 * (S_ * 2), lds0 + (st - 4) * 16384 + kq * 4096, v0, v1, v0 + vstep, v1 + vstep);
        asm volatile("s_barrier" ::: "memory");
        static_for<0, 14>([&](auto ST) {
          constexpr int st = decltype(ST)::v;
          constexpr int last = (st + 7 > 13) ? 13 : (st + 7 < 8 ? 8 : st + 7);
          wait_vbar<4 * (last - st)>();
          if (st >= 1 && st + 8 < 14) {
            const long o = (st + 8 == 13 && kq >= 2) ? (long)(2 * 13 - 1) * 64 * (S_ * 2) : (long)(st + 8) * 128 * (S_ * 2);
            glds4<false>(wsrc + o, lds0 + w2_slot(st + 8) + kq * 4096, v0, v1, v0 + vstep, v1 + vstep);
          }
        });
        asm volatile("s_barrier" ::: "memory");
      } else {
        if (wave == 0) poll_flags(ws, fl + FL_U + j * 512 + (tb & 3) * 128, 27, tag, lane, dead);
        asm volatile("s_barrier" ::: "memory");
        STAMP(11);
        const unsigned abase = WS_U + layer * UB + j * (U_KS * 5 * 1024) + kq * 5120 + lane * 16;
        const int q = c16 >> 2, p = lane & 3;
        const unsigned fsw = (((q >> 1) & 1) | ((g & 1) << 1)) * 32;
        const lds_cptr wrow = lds + (32 * kq + 8 * g + q) * 128 + 8 * p;
        u32x4 af[3][5];
#pragma unroll
        for (int st = 0; st < 3; ++st)
#pragma unroll
          for (int a = 0; a < 5; ++a) af[st][a] = ld16_dev_s(rws, abase + (a & 3) * 1024, st * 20480 + (a >> 2) * 4096);
        f32x4 acc[4][5];
#pragma unroll
        for (int i = 0; i < 4; ++i)
#pragma unroll
          for (int a = 0; a < 5; ++a) acc[i][a] = f32x4{0.f, 0.f, 0.f, 0.f};
        static_for<0, 14>([&](auto ST) {
          constexpr int st = decltype(ST)::v;
          bar_lds();
          if (st < 13 || kq < 2) {
            bf16x8 wf[4];
#pragma unroll
            for (int i = 0; i < 4; ++i) wf[i] = lds_tr8(wrow + w2_slot(st) + ((i * 32) ^ fsw), 512);
            SBAR();
#pragma unroll
            for (int a = 0; a < 5; ++a) {
              const bf16x8 x = __builtin_bit_cast(bf16x8, af[st % 3][a]);
#pragma unroll
              for (int i = 0; i < 4; ++i) acc[i][a] = MFMA16(x, wf[i], acc[i][a]);
              SBAR();
              if (st + 3 < 14) af[st % 3][a] = ld16_dev_s(rws, abase + (a & 3) * 1024, (st + 3) * 20480 + (a >> 2) * 4096);
              SBAR();
            }
          }
        });
        bar_lds();
#pragma unroll
        for (int i = 0; i < 4; ++i)
#pragma unroll
          for (int a = 0; a < 5; ++a) *(LAS f32x4*)(ldw + ((kq * 4 + i) * 5 + a) * 1024 + lane * 16) = acc[i][a];
      }
      bar_lds();
      STAMP(12);
      f32x4 sv[3]; u32x2 rv[3];
#pragma unroll
      for (int i = 0; i < 3; ++i) { const int at = min(ew0 + i, 4); sv[i] = sum4(lds, ea * 5 + at, 20, lane); rv[i] = *(const LAS u32x2*)(lds + res_off + at * 32); }
#pragma unroll
      for (int i = 0; i < 3; ++i) {
        if (i < ewn) {
          const int at = ew0 + i;
          const f32x4 s = sv[i];
          const u32x2 r = rv[i];
          u32x2 w;
          w[0] = pack_bf16(bf_lo(r[0]) + (s[0] + bias), bf_hi(r[0]) + (s[1] + bias));
          w[1] = pack_bf16(bf_lo(r[1]) + (s[2] + bias), bf_hi(r[1]) + (s[3] + bias));
          const int d = j * 80 + at * 16 + 4 * g;
          if (layer == 0) {
            *(LAS u32x2*)(ldw + res_off + at * 32) = w;
            st8_dev(rws, WS_X + tb * 81920 + (((d >> 5) * 4 + ea) * 64 + c16 + 16 * ((d & 31) >> 3)) * 16 + (d & 7) * 2, w);
          } else {
            *(u32x2*)(out + (long)tok * D_ + d) = w;
          }
        }
      }
      if (layer == 0) publish(ws, WS_FLAGS + FL_X + tb * 128 + j * 4, 1, 0, tag, wave, lane);
      STAMP(13);
    }
  }
  if (blockIdx.x == 0 && tid == 0) __hip_atomic_store((gu32*)(ws + WS_EPOCH), tag, RLX_AGENT);
}

extern "C" void kernel_launch(void* const* d_in, const int* in_sizes, int n_in, void* d_out, int out_size, void* d_ws,
                              size_t ws_size, hipStream_t stream) {
  (void)hipFuncSetAttribute((const void*)mega_kernel, hipFuncAttributeMaxDynamicSharedMemorySize, LDS_BYTES);
  mega_kernel<<<256, 512, LDS_BYTES, stream>>>((const u16*)d_in[0], (const u16*)d_in[1], (const u16*)d_in[2], (const u16*)d_in[3],
                                               (const u16*)d_in[4], (const u16*)d_in[6], (const u16*)d_in[7], (const u16*)d_in[8],
                                               (const u16*)d_in[9], (u16*)d_out, (char*)d_ws);
}
```

```cpp
#include <hip/hip_runtime.h>
#include <hip/hip_bf16.h>
#include <stdint.h>

typedef __attribute__((ext_vector_type(8))) short bf16x8;
typedef __attribute__((ext_vector_type(4))) short bf16x4;
typedef __attribute__((ext_vector_type(16))) float f32x16;
typedef __attribute__((ext_vector_type(4))) float f32x4;
typedef __attribute__((ext_vector_type(2))) float f32x2;
typedef __attribute__((ext_vector_type(2))) __bf16 bf2_t;
typedef __attribute__((ext_vector_type(4))) unsigned u32x4;
typedef __attribute__((ext_vector_type(2))) unsigned u32x2;
typedef unsigned short u16;
#define LAS __attribute__((address_space(3)))
#define GAS __attribute__((address_space(1)))
typedef LAS const char* lds_cptr;
typedef LAS char* lds_ptr;
typedef GAS unsigned gu32;
typedef GAS unsigned long long gu64;
#define DI __device__ __forceinline__
#define RLX_AGENT __ATOMIC_RELAXED, __HIP_MEMORY_SCOPE_AGENT

constexpr int S_ = 2048, D_ = 640, DH_ = 20, F_ = 1728;
constexpr float LN_EPS = 1e-5f;
constexpr float QSCALE = 0.22360679774997896f * 1.4426950408889634f;

constexpr int KV_TILE = 2688, KV_KT = 1024, KV_V = 1280, KV_VS = 704;

constexpr unsigned WS_EPOCH = 0;
constexpr unsigned WS_FLAGS = 4096, FL_LAYER = 65536;
constexpr unsigned FL_KV = 0;
constexpr unsigned FL_O = 32768;
constexpr unsigned FL_X = 36864;
constexpr unsigned FL_H = 40960;
constexpr unsigned FL_U = 45056;
constexpr unsigned WS_DIAG = 262144;
constexpr unsigned WS_STAT = 524288, ST_LAYER = 524288;
constexpr unsigned WS_KV = WS_STAT + 2 * ST_LAYER, KV_LAYER = 196608;
constexpr unsigned XB = S_ * D_ * 2;
constexpr unsigned WS_O = WS_KV + 2 * KV_LAYER;
constexpr unsigned WS_H = WS_O + 2 * XB;
constexpr int U_KS = 56;
constexpr unsigned UB = 8 * U_KS * 5 * 1024;
constexpr unsigned WS_U = WS_H + 2 * XB;
constexpr unsigned WS_X = WS_U + 2 * UB;
constexpr unsigned WS_END = WS_X + XB;

constexpr int L_W = 0;
constexpr int L_WT = 81920;
constexpr int L_RING = 81920;
constexpr int L_ATT = 102400;
constexpr int L_QT = 81920;
constexpr int L_KVP = 128064;
constexpr int L_STG = 134656;
constexpr int L_RES = 151040;
constexpr int L_DUMP = 161280;
constexpr int LDS_BYTES = 163840;
constexpr unsigned SPIN_CAP = 1u << 21;

DI unsigned pack_bf16(float lo, float hi) {
  f32x2 v = {lo, hi};
  bf2_t b = __builtin_convertvector(v, bf2_t);
  return __builtin_bit_cast(unsigned, b);
}
DI float bf_lo(unsigned u) { return __uint_as_float(u << 16); }
DI float bf_hi(unsigned u) { return __uint_as_float(u & 0xffff0000u); }
DI float rnd1(float v) { return bf_lo(pack_bf16(v, 0.f)); }
DI float ln_rsd(float var) { return __builtin_amdgcn_rcpf(rnd1(__builtin_amdgcn_sqrtf(rnd1(rnd1(var) + LN_EPS)))); }

#define GLDS_HEAD "s_mov_b32 m0, %1\n\ts_nop 4\n\t"
template <bool DEV> DI void glds1(const void* sbase, unsigned m0v, unsigned v0) {
  if (DEV) asm volatile(GLDS_HEAD "global_load_lds_dwordx4 %2, %0 sc1" ::"s"(sbase), "s"(m0v), "v"(v0) : "memory");
  else asm volatile(GLDS_HEAD "global_load_lds_dwordx4 %2, %0" ::"s"(sbase), "s"(m0v), "v"(v0) : "memory");
}
template <bool DEV> DI void glds4(const void* sbase, unsigned m0v, unsigned v0, unsigned v1, unsigned v2, unsigned v3) {
  if (DEV)
    asm volatile(GLDS_HEAD "global_load_lds_dwordx4 %2, %0 sc1\n\tglobal_load_lds_dwordx4 %3, %0 offset:1024 sc1\n\t"
                           "global_load_lds_dwordx4 %4, %0 offset:2048 sc1\n\tglobal_load_lds_dwordx4 %5, %0 offset:3072 sc1"
                 ::"s"(sbase), "s"(m0v), "v"(v0), "v"(v1), "v"(v2), "v"(v3) : "memory");
  else
    asm volatile(GLDS_HEAD "global_load_lds_dwordx4 %2, %0\n\tglobal_load_lds_dwordx4 %3, %0 offset:1024\n\t"
                           "global_load_lds_dwordx4 %4, %0 offset:2048\n\tglobal_load_lds_dwordx4 %5, %0 offset:3072"
                 ::"s"(sbase), "s"(m0v), "v"(v0), "v"(v1), "v"(v2), "v"(v3) : "memory");
}
template <int N, bool DEV> DI void glds_lin(const void* sbase, unsigned m0v, unsigned v) {
  if (N == 1) glds1<DEV>(sbase, m0v, v);
  else if (N == 4) glds4<DEV>(sbase, m0v, v, v, v, v);
  else if (N == 2) {
    if (DEV) asm volatile(GLDS_HEAD "global_load_lds_dwordx4 %2, %0 sc1\n\tglobal_load_lds_dwordx4 %2, %0 offset:1024 sc1" ::"s"(sbase), "s"(m0v), "v"(v) : "memory");
    else asm volatile(GLDS_HEAD "global_load_lds_dwordx4 %2, %0\n\tglobal_load_lds_dwordx4 %2, %0 offset:1024" ::"s"(sbase), "s"(m0v), "v"(v) : "memory");
  } else {
    if (DEV) asm volatile(GLDS_HEAD "global_load_lds_dwordx4 %2, %0 sc1\n\tglobal_load_lds_dwordx4 %2, %0 offset:1024 sc1\n\tglobal_load_lds_dwordx4 %2, %0 offset:2048 sc1" ::"s"(sbase), "s"(m0v), "v"(v) : "memory");
    else asm volatile(GLDS_HEAD "global_load_lds_dwordx4 %2, %0\n\tglobal_load_lds_dwordx4 %2, %0 offset:1024\n\tglobal_load_lds_dwordx4 %2, %0 offset:2048" ::"s"(sbase), "s"(m0v), "v"(v) : "memory");
  }
}
DI void glds2(const void* sbase, unsigned m0v, unsigned v0, unsigned v1) {
  asm volatile(GLDS_HEAD "global_load_lds_dwordx4 %2, %0\n\tglobal_load_lds_dwordx4 %3, %0 offset:1024" ::"s"(sbase), "s"(m0v), "v"(v0), "v"(v1) : "memory");
}
#define UNI(x) __builtin_amdgcn_readfirstlane(x)
template <int N> DI void wait_bar() { asm volatile("s_waitcnt vmcnt(%0) lgkmcnt(0)\n\ts_barrier" ::"i"(N) : "memory"); }
template <int N> DI void wait_vbar() { asm volatile("s_waitcnt vmcnt(%0)\n\ts_barrier" ::"i"(N) : "memory"); }
DI void bar_lds() { asm volatile("s_waitcnt lgkmcnt(0)\n\ts_barrier" ::: "memory"); }
typedef short v4i16_t __attribute__((ext_vector_type(4)));
DI bf16x4 lds_tr(lds_cptr p) {
  return __builtin_bit_cast(bf16x4, __builtin_amdgcn_ds_read_tr16_b64_v4i16((LAS v4i16_t*)p));
}
DI bf16x8 lds_tr8(lds_cptr p, int step) {
  const bf16x4 lo = lds_tr(p), hi = lds_tr(p + step);
  return __builtin_shufflevector(lo, hi, 0, 1, 2, 3, 4, 5, 6, 7);
}
DI bf16x8 lds_b128(lds_cptr p) { return *(const LAS bf16x8*)p; }
#define MFMA16(a, b, c) __builtin_amdgcn_mfma_f32_16x16x32_bf16((a), (b), (c), 0, 0, 0)
#define MFMA32(a, b, c) __builtin_amdgcn_mfma_f32_32x32x16_bf16((a), (b), (c), 0, 0, 0)
#define MFMA32K8(a, b, c) __builtin_amdgcn_mfma_f32_32x32x8bf16_1k((a), (b), (c), 0, 0, 0)
#define SBAR() __builtin_amdgcn_sched_barrier(0)
#define PIN(x) asm volatile("" : "+v"(x))
typedef __amdgpu_buffer_rsrc_t rsrc_t;
DI void st8_dev(rsrc_t r, unsigned off, u32x2 v) { __builtin_amdgcn_raw_buffer_store_b64(v, r, off, 0, 16); }
DI u32x4 ld16_dev(rsrc_t r, unsigned off) { return __builtin_amdgcn_raw_buffer_load_b128(r, off, 0, 16); }
DI u32x4 ld16_dev_s(rsrc_t r, unsigned off, unsigned soff) { return __builtin_amdgcn_raw_buffer_load_b128(r, off, soff, 16); }

DI void poll_flags(const char* ws, unsigned off, int n, unsigned tag, int lane, bool& dead) {
  gu32* f = (gu32*)(ws + off);
  unsigned spins = 0;
  while (!dead) {
    unsigned v = tag;
    if (lane < n) v = __hip_atomic_load(f + lane, RLX_AGENT);
    if (__all(v == tag)) break;
    if (++spins > SPIN_CAP) dead = true;
    __builtin_amdgcn_s_sleep(1);
  }
  asm volatile("" ::: "memory");
}
DI void publish(const char* ws, unsigned off, int nrep, unsigned rstride, unsigned tag, int wave, int lane) {
  wait_bar<0>();
  if (wave == 0 && lane < nrep) __hip_atomic_store((gu32*)(ws + off + lane * rstride), tag, RLX_AGENT);
}

DI float fmax3(float a, float b, float c) { return __builtin_fmaxf(__builtin_fmaxf(a, b), c); }
DI float rowmax16(const f32x16& s) {
  float a = fmax3(s[0], s[1], s[2]), b = fmax3(s[3], s[4], s[5]);
  a = fmax3(a, s[6], s[7]); b = fmax3(b, s[8], s[9]); a = fmax3(a, s[10], s[11]); b = fmax3(b, s[12], s[13]);
  a = fmax3(a, s[14], s[15]); a = __builtin_fmaxf(a, b);
  return __builtin_fmaxf(a, __shfl_xor(a, 32));
}
DI void pack_p(const f32x16& p, bf16x8& lo, bf16x8& hi) {
  u32x4 a, b;
#pragma unroll
  for (int i = 0; i < 4; ++i) { a[i] = pack_bf16(p[2 * i], p[2 * i + 1]); b[i] = pack_bf16(p[8 + 2 * i], p[9 + 2 * i]); }
  lo = __builtin_bit_cast(bf16x8, a); hi = __builtin_bit_cast(bf16x8, b);
}
DI float gelu_tanh(float u) {
  const float z = 0.7978845608028654f * (u + 0.044715f * u * u * u);
  const float e = __builtin_amdgcn_exp2f(-2.885390081777927f * z);
  return u * __builtin_amdgcn_rcpf(1.f + e);
}

DI void wslice_block(const u16* w, unsigned lds0, int lw, int lane, int blk) {
  const char* src = (const char*)w + lw * (160 * 1280);
  if (blk < 5) {
    const int r = lane >> 3, pos = lane & 7;
    const unsigned pe = (((pos >> 1) ^ ((r >> 1) & 1)) << 1 | (pos & 1)) * 16;
    const unsigned v0 = r * 1280 + pe, v1 = r * 1280 + (pe ^ 64) + 9216;
    glds4<false>(src + blk * (32 * 1280), lds0 + L_W + lw * 20480 + blk * 4096, v0, v1, v0 + 18432, v1 + 18432);
  } else {
    const unsigned t0 = (lane >> 1) * 1280 + 128 + (lane & 1) * 16;
    if (blk == 5) glds4<false>(src, lds0 + L_WT + lw * 5120, t0, t0 + 39936, t0 + 2 * 39936, t0 + 3 * 39936);
    else glds1<false>(src + 128 * 1280, lds0 + L_WT + lw * 5120 + 4096, t0);
  }
}

DI void touch(const void* sbase, unsigned voff, unsigned& sink) {
  asm volatile("s_nop 4\n\tglobal_load_dword %0, %1, %2" : "+v"(sink) : "v"(voff), "s"(sbase) : "memory");
}
DI float xsum16(float x) {
  auto r = __builtin_amdgcn_permlane16_swap(__float_as_uint(x), __float_as_uint(x), false, false);
  return __uint_as_float(r[0]) + __uint_as_float(r[1]);
}
DI float xsum32(float x) {
  auto r = __builtin_amdgcn_permlane32_swap(__float_as_uint(x), __float_as_uint(x), false, false);
  return __uint_as_float(r[0]) + __uint_as_float(r[1]);
}
DI f32x4 ldf4(lds_cptr p) { return *(const LAS f32x4*)p; }

DI f32x4 sum4(lds_cptr lds, int t, int nt, int lane) {
  f32x4 s = *(const LAS f32x4*)(lds + t * 1024 + lane * 16);
#pragma unroll
  for (int k = 1; k < 4; ++k) s += *(const LAS f32x4*)(lds + (k * nt + t) * 1024 + lane * 16);
  return s;
}
DI void ln_finish(float s1, float s2, float& mu, float& var) { mu = s1 * (1.f / D_); var = fmaxf(s2 * (1.f / D_) - mu * mu, 0.f); }

constexpr int ATT_CH = 4;
constexpr int ATT_CHB = ATT_CH * KV_TILE;
constexpr int ATT_SLOT = 10752;
constexpr int ATT_NCH = S_ / 32 / ATT_CH;
constexpr int w2_slot(int st) { return (st % 9) < 4 ? L_RING + (st % 9) * 16384 : ((st % 9) - 4) * 16384; }
template <int N> struct IC { static constexpr int v = N; };
template <int I, int N, class F> DI void static_for(F&& f) { if constexpr (I < N) { f(IC<I>{}); static_for<I + 1, N>(f); } }

#define STAMP(i) do { } while (0)

__global__ __launch_bounds__(512, 2) void mega_kernel(const u16* __restrict__ x0, const u16* __restrict__ wq, const u16* __restrict__ wk,
                                                      const u16* __restrict__ wv, const u16* __restrict__ wo, const u16* __restrict__ w1,
                                                      const u16* __restrict__ b1, const u16* __restrict__ w2, const u16* __restrict__ b2,
                                                      u16* __restrict__ out, char* ws) {
  extern __shared__ __attribute__((aligned(16))) char smem[];
  const int tid = threadIdx.x;
  const unsigned lds0 = (unsigned)(uintptr_t)smem;
  const lds_cptr lds = (lds_cptr)smem;
  const lds_ptr ldw = (lds_ptr)smem;
#define FRESH() \
  int lane = tid & 63; asm volatile("" : "+v"(lane)); \
  int wave = UNI(tid >> 6); asm volatile("" : "+s"(wave)); \
  int bid = blockIdx.x; asm volatile("" : "+s"(bid)); \
  const int xcd = bid & 7, idx = bid >> 3, tb = xcd * 4 + (idx >> 3), j = idx & 7; \
  const int g = lane >> 4, c16 = lane & 15, kq = wave & 3, th = wave >> 2; \
  const int ea = wave & 3, ew0 = th ? 3 : 0, ewn = th ? 2 : 3; \
  const unsigned res_off = L_RES + (ea * 16 + c16) * 160 + 8 * g; \
  const int panel4 = (xcd & 1) * 4 + (idx & 3), ft = (xcd >> 1) * 7 + (idx >> 2); \
  const bool has4 = idx < 28 && ft < 27; \
  (void)xcd; (void)g; (void)c16; (void)kq; (void)th; (void)ea; (void)ew0; (void)ewn; (void)res_off; (void)tb; (void)j; (void)panel4; (void)has4
  const rsrc_t rws = __builtin_amdgcn_make_buffer_rsrc(ws, 0, WS_END, 0x00020000);
  unsigned tag = 0;
  bool dead = false;

#pragma unroll 1
  for (int layer = 0; layer < 2; ++layer) {
    const unsigned fl = WS_FLAGS + layer * FL_LAYER;
    const unsigned kvoff = WS_KV + layer * KV_LAYER;
    unsigned sink = 0;
    {
      FRESH();
      STAMP(0);
      auto load_kvw = [&]() {
        const char* xs = (const char*)((j & 1) ? wv : wk);
        if (wave < 3) {
          glds_lin<4, false>(xs + wave * 8192, lds0 + L_ATT + wave * 8192, lane * 16);
          glds_lin<4, false>(xs + wave * 8192 + 4096, lds0 + L_ATT + wave * 8192 + 4096, lane * 16);
        } else glds1<false>(xs + 24576, lds0 + L_ATT + 24576, lane * 16);
      };
      u32x4 af[5][2];
      if (layer == 0) {
        const rsrc_t ra = __builtin_amdgcn_make_buffer_rsrc((void*)x0, 0, XB, 0x00020000);
        const unsigned abase = (tb * 64 + th * 32 + c16) * (D_ * 2) + kq * 64 + g * 16;
#pragma unroll
        for (int s = 0; s < 5; ++s)
#pragma unroll
          for (int t = 0; t < 2; ++t) af[s][t] = __builtin_amdgcn_raw_buffer_load_b128(ra, abase + s * 256, t * (16 * D_ * 2), 0);
        const unsigned epoch = __hip_atomic_load((gu32*)(ws + WS_EPOCH), RLX_AGENT);
        if (wave < 4) load_kvw();
        else touch(wq, min((idx * 4 + kq) * 50 + lane, 6399) * 128, sink);
        STAMP(1);
        wait_vbar<0>();
        tag = UNI(epoch) + 1u;
        if (wave >= 4) {
#pragma unroll 1
          for (int blk = 0; blk < 7; ++blk) wslice_block(wq + j * 80, lds0, kq, lane, blk);
        } else {
#pragma unroll 1
          for (int pc = wave; pc < 10; pc += 4) {
            const int n = pc * 64 + lane, row = n / 10, ch = n - row * 10;
            glds1<false>(x0 + (long)tb * 64 * D_ + j * 80, lds0 + L_RES + pc * 1024, row * 1280 + ch * 16);
          }
        }
      } else {
        if (wave >= 4) {
#pragma unroll 1
          for (int blk = 0; blk < 7; ++blk) wslice_block(wq + j * 80, lds0, kq, lane, blk);
        } else load_kvw();
        if (wave == 0) poll_flags(ws, WS_FLAGS + FL_X + tb * 128, 8, tag, lane, dead);
        wait_vbar<0>();
        STAMP(1);
        const unsigned abase = WS_X + tb * 81920 + (kq * 4 + th * 2) * 1024 + lane * 16;
#pragma unroll
        for (int s = 0; s < 5; ++s)
#pragma unroll
          for (int t = 0; t < 2; ++t) af[s][t] = ld16_dev_s(rws, abase + t * 1024, s * 16384);
      }
      bf16x8 ones;
#pragma unroll
      for (int i = 0; i < 8; ++i) ones[i] = (short)0x3f80;
      const f32x4 z4 = {0.f, 0.f, 0.f, 0.f};
      const int q = c16 >> 2, p = lane & 3;
      const int row = 32 * kq + 8 * g + q;
      const unsigned fsw = (((q >> 1) & 1) | ((g & 1) << 1)) * 32;
      const unsigned woff = L_W + row * 128 + 8 * p, toff = L_WT + row * 32 + 8 * p, xoff = L_ATT + row * 40 + 8 * p;
      LAS float* stat = (LAS float*)(ldw + L_DUMP);
      LAS float* csum = (LAS float*)(ldw + L_KVP + 20480);
      const int tk = j >> 1;
      const bool isv = j & 1;
      {
        f32x4 a1s[2] = {z4, z4}, a2s[2] = {z4, z4}, akv[2] = {z4, z4}, aok[2] = {z4, z4};
#pragma unroll
        for (int s = 0; s < 5; ++s) {
          const bf16x8 a0 = __builtin_bit_cast(bf16x8, af[s][0]), a1 = __builtin_bit_cast(bf16x8, af[s][1]);
          if (th == (tk >> 1)) {
            const bf16x8 a = (tk & 1) ? a1 : a0;
            const bf16x8 x0f = lds_tr8(lds + xoff + s * 5120, 160), x1f = lds_tr8(lds + xoff + s * 5120 + 32, 160);
            if (!isv) {
              akv[0] = MFMA16(x0f, a, akv[0]); akv[1] = MFMA16(x1f, a, akv[1]);
              aok[0] = MFMA16(x0f, ones, aok[0]); aok[1] = MFMA16(x1f, ones, aok[1]);
            } else {
              akv[0] = MFMA16(a, x0f, akv[0]); akv[1] = MFMA16(a, x1f, akv[1]);
              aok[0] = MFMA16(ones, x0f, aok[0]); aok[1] = MFMA16(ones, x1f, aok[1]);
            }
          }
          a1s[0] = MFMA16(ones, a0, a1s[0]); a1s[1] = MFMA16(ones, a1, a1s[1]);
          a2s[0] = MFMA16(a0, a0, a2s[0]); a2s[1] = MFMA16(a1, a1, a2s[1]);
        }
        if (g == q) {
#pragma unroll
          for (int t = 0; t < 2; ++t) {
            const float d = (p & 2) ? ((p & 1) ? a2s[t][3] : a2s[t][2]) : ((p & 1) ? a2s[t][1] : a2s[t][0]);
            *(LAS f32x2*)(stat + (kq * 64 + th * 32 + t * 16 + c16) * 2) = f32x2{a1s[t][0], d};
          }
        }
        if (th == (tk >> 1)) {
          const bool l2 = isv ? (c16 < 4) : (g == 0);
          const int i2 = isv ? g * 4 + c16 : c16;
          const int o = L_KVP + kq * 2560;
          *(LAS f32x4*)(ldw + o + lane * 16) = akv[0];
          *(LAS f32x4*)(ldw + o + 1280 + lane * 16) = aok[0];
          if (l2) { *(LAS f32x4*)(ldw + o + 1024 + i2 * 16) = akv[1]; *(LAS f32x4*)(ldw + o + 2304 + i2 * 16) = aok[1]; }
        }
      }
      wait_bar<0>();
      float mu, rs;
      {
        f32x2 sv[4];
#pragma unroll
        for (int k = 0; k < 4; ++k) sv[k] = *(const LAS f32x2*)(stat + (k * 64 + ea * 16 + c16) * 2);
        float var; ln_finish((sv[0][0] + sv[1][0]) + (sv[2][0] + sv[3][0]), (sv[0][1] + sv[1][1]) + (sv[2][1] + sv[3][1]), mu, var);
        mu = rnd1(mu); rs = ln_rsd(var);
      }
      if (ea == tk) {
        const int li = th == 0 ? lane : 64 + (isv ? g * 4 + (c16 & 3) : c16);
        f32x4 s = z4, cs = z4;
#pragma unroll
        for (int k = 0; k < 4; ++k) { s += ldf4(lds + L_KVP + k * 2560 + li * 16); cs += ldf4(lds + L_KVP + k * 2560 + 1280 + li * 16); }
        const unsigned tile = kvoff + (tb * 2 + (ea >> 1)) * KV_TILE;
        if (!isv) {
          u32x2 w; w[0] = pack_bf16((s[0] - mu * cs[0]) * rs, (s[1] - mu * cs[1]) * rs); w[1] = pack_bf16((s[2] - mu * cs[2]) * rs, (s[3] - mu * cs[3]) * rs);
          const int r32 = (ea & 1) * 16 + c16;
          if (th == 0) st8_dev(rws, tile + ((g >> 1) * 32 + r32) * 16 + (g & 1) * 8, w);
          else if (g == 0) st8_dev(rws, tile + KV_KT + r32 * 8, w);
        } else {
          float o[4];
#pragma unroll
          for (int e = 0; e < 4; ++e) o[e] = (s[e] - __shfl(mu, 4 * g + e) * cs[e]) * __shfl(rs, 4 * g + e);
          const int d = th * 16 + c16;
          u32x2 w; w[0] = pack_bf16(o[0], o[1]); w[1] = pack_bf16(o[2], o[3]);
          if (d == DH_) { w[0] = 0x3f803f80u; w[1] = 0x3f803f80u; }
          if (d == DH_ + 1) { w[0] = 0u; w[1] = 0u; }
          if (d <= DH_ + 1) st8_dev(rws, tile + KV_V + (ea & 1) * KV_VS + ((g & 1) * 22 + d) * 16 + (g >> 1) * 8, w);
        }
      }
      f32x4 acc[5][2], aon[3] = {z4, z4, z4};
#pragma unroll
      for (int i = 0; i < 5; ++i) { acc[i][0] = z4; acc[i][1] = z4; }
#pragma unroll
      for (int s = 0; s < 5; ++s) {
        const bf16x8 a0 = __builtin_bit_cast(bf16x8, af[s][0]), a1 = __builtin_bit_cast(bf16x8, af[s][1]);
        bf16x8 wf[5];
#pragma unroll
        for (int i = 0; i < 4; ++i) wf[i] = lds_tr8(lds + woff + s * 16384 + ((i * 32) ^ fsw), 512);
        wf[4] = lds_tr8(lds + toff + s * 4096, 128);
#pragma unroll
        for (int i = 0; i < 5; ++i) { acc[i][0] = MFMA16(wf[i], a0, acc[i][0]); acc[i][1] = MFMA16(wf[i], a1, acc[i][1]); }
        if (th == 0) { aon[0] = MFMA16(wf[0], ones, aon[0]); aon[1] = MFMA16(wf[1], ones, aon[1]); aon[2] = MFMA16(wf[2], ones, aon[2]); }
        else { aon[0] = MFMA16(wf[3], ones, aon[0]); aon[1] = MFMA16(wf[4], ones, aon[1]); }
        if (s == 1) {
          publish(ws, fl + FL_KV + (tb * 8 + j) * 4, 32, 1024, tag, wave, lane);
          if (wave >= 4) {
            const char* w1w = (const char*)(w1 + (has4 ? ft : 0) * 64) + (long)((idx & 3) * 512 + kq * 128) * (F_ * 2);
            touch(w1w, lane * (F_ * 2), sink); touch(w1w + 64 * (F_ * 2), lane * (F_ * 2), sink);
            touch((const char*)(w2 + tb * 64) + (long)(j * 216 + kq * 54) * (S_ * 2), (lane < 54 ? lane : 53) * (S_ * 2), sink);
          }
        }
      }
      bar_lds();
#pragma unroll
      for (int i = 0; i < 5; ++i)
#pragma unroll
        for (int t = 0; t < 2; ++t) *(LAS f32x4*)(ldw + ((kq * 5 + i) * 4 + th * 2 + t) * 1024 + lane * 16) = acc[i][t];
      if (c16 == 0) {
#pragma unroll
        for (int i = 0; i < 3; ++i) if (i < ewn) *(LAS f32x4*)(csum + (kq * 7 + ew0 + i) * 16 + 4 * g) = aon[i];
      }
      bar_lds();
      {
        const float r = rs * QSCALE;
        f32x4 s[3], cs[3];
#pragma unroll
        for (int i = 0; i < 3; ++i) {
          const int wt = min(ew0 + i, 4);
          s[i] = sum4(lds, wt * 4 + ea, 20, lane);
          cs[i] = z4;
#pragma unroll
          for (int k = 0; k < 4; ++k) cs[i] += *(const LAS f32x4*)(csum + (k * 7 + wt) * 16 + 4 * g);
        }
#pragma unroll
        for (int i = 0; i < 3; ++i) {
          if (i < ewn) {
            u32x2 w;
            w[0] = pack_bf16((s[i][0] - mu * cs[i][0]) * r, (s[i][1] - mu * cs[i][1]) * r);
            w[1] = pack_bf16((s[i][2] - mu * cs[i][2]) * r, (s[i][3] - mu * cs[i][3]) * r);
            *(LAS u32x2*)(ldw + L_QT + (ea * 16 + c16) * 160 + (ew0 + i) * 32 + 8 * g) = w;
          }
        }
      }
      bar_lds();
    }
    STAMP(2);
    bf16x8 w1r[16][2];
    {
      FRESH();
      const int r32 = lane & 31, hi = lane >> 5;
      const int hh = wave & 3, trow = th * 32 + r32;
      bf16x8 q16; bf16x4 q4;
      {
        const lds_cptr qp = lds + L_QT + trow * 160 + hh * 40;
        const u32x2 a = *(const LAS u32x2*)(qp + hi * 16), b = *(const LAS u32x2*)(qp + hi * 16 + 8), c = *(const LAS u32x2*)(qp + 32);
        u32x4 v = {a[0], a[1], b[0], b[1]};
        q16 = __builtin_bit_cast(bf16x8, v);
        u32x2 t = {hi ? 0u : c[0], hi ? 0u : c[1]};
        q4 = __builtin_bit_cast(bf16x4, t);
        PIN(q16); PIN(q4);
      }
      if (wave == 0) {
        unsigned spins = 0;
        while (!dead) {
          const u32x4 v = ld16_dev(rws, fl + FL_KV + (bid & 31) * 1024 + lane * 16);
          if (__all(v[0] == tag && v[1] == tag && v[2] == tag && v[3] == tag)) break;
          if (++spins > SPIN_CAP) dead = true;
          __builtin_amdgcn_s_sleep(1);
          asm volatile("" ::: "memory");
        }
      }
      bar_lds();
      STAMP(3);
      const unsigned koff = lane * 16;
      const char* KV = ws + kvoff + wave * 3072;
      auto issue_kv = [&](int ch) {
        const unsigned dst = lds0 + L_ATT + (ch % 3) * ATT_SLOT + wave * 3072;
        if (wave < 3) glds_lin<3, true>(KV + ch * ATT_CHB, dst, koff);
        else { glds1<true>(KV + ch * ATT_CHB, dst, koff); if (lane < 32) glds1<true>(KV + ch * ATT_CHB + 1024, dst + 1024, koff); }
      };
      const char* w1s = (const char*)(w1 + (has4 ? ft : 0) * 64) + (long)wave * 16 * (F_ * 2);
      unsigned sv0, sv1;
      {
        const int r = lane >> 3, pos = lane & 7;
        const unsigned pe = (((pos >> 1) ^ ((r >> 1) & 1)) << 1 | (pos & 1)) * 16;
        sv0 = r * (F_ * 2) + pe; sv1 = (r + 8) * (F_ * 2) + (pe ^ 64) - 1024;
      }
      const unsigned fsw = (((c16 >> 3) & 1) | ((g & 1) << 1)) * 32;
      const unsigned srow = (32 * kq + 8 * g + (c16 >> 2)) * 128 + 8 * (lane & 3);
      const unsigned sr0 = srow + ((64 * th) ^ fsw), sr1 = srow + ((64 * th + 32) ^ fsw);
      if (wave < 4) { issue_kv(0); issue_kv(1); }
      glds2(w1s, lds0 + L_STG + wave * 2048, sv0, sv1);
      const unsigned ktoff = KV_KT + r32 * 8, voff = KV_V + (hi * 22 + (r32 < 21 ? r32 : 21)) * 16;
      f32x16 o = {}, negm, sA, sB;
      bf16x8 k16, v0, v1; bf16x4 k4; u32x4 p0, p1;
      if (wave < 3) wait_bar<5>(); else if (wave == 3) wait_bar<4>(); else bar_lds();
      const lds_cptr ring = lds + L_ATT;
      {
        k16 = lds_b128(ring + koff);
        k4 = *(const LAS bf16x4*)(ring + ktoff);
        f32x16 z = {};
        z = MFMA32(k16, q16, z); z = MFMA32K8(k4, q4, z);
        k16 = lds_b128(ring + KV_TILE + koff);
        k4 = *(const LAS bf16x4*)(ring + KV_TILE + ktoff);
        v1 = lds_b128(ring + voff);
        const float m = rowmax16(z);
#pragma unroll
        for (int i = 0; i < 16; ++i) { negm[i] = -m; sA[i] = z[i] - m; }
        p1 = u32x4{0u, 0u, 0u, 0u};
      }
#define EX(x) __builtin_amdgcn_exp2f(x)
#define EXP4(X, i) do { X[i] = EX(X[i]); X[i + 1] = EX(X[i + 1]); X[i + 2] = EX(X[i + 2]); X[i + 3] = EX(X[i + 3]); } while (0)
#define BF8(x) __builtin_bit_cast(bf16x8, x)
#define PK4(P, X, i) do { P[0] = pack_bf16(X[i], X[i + 1]); P[1] = pack_bf16(X[i + 2], X[i + 3]); P[2] = pack_bf16(X[i + 4], X[i + 5]); P[3] = pack_bf16(X[i + 6], X[i + 7]); } while (0)
#define STEP(C, N, kn, vc) do { \
    N = MFMA32(k16, q16, negm); SBAR(); \
    v0 = lds_b128((vc) + voff); EXP4(C, 0); PIN(C); SBAR(); \
    o = MFMA32(v1, BF8(p1), o); SBAR(); \
    k16 = lds_b128((kn) + koff); EXP4(C, 4); PIN(C); SBAR(); PK4(p0, C, 0); PIN(p0); SBAR(); \
    N = MFMA32K8(k4, q4, N); SBAR(); \
    v1 = lds_b128((vc) + voff + KV_VS); EXP4(C, 8); PIN(C); SBAR(); \
    o = MFMA32(v0, BF8(p0), o); SBAR(); \
    k4 = *(const LAS bf16x4*)((kn) + ktoff); EXP4(C, 12); PIN(C); SBAR(); PK4(p1, C, 8); PIN(p1); SBAR(); \
  } while (0)
      static_for<0, ATT_NCH>([&](auto CH) {
        constexpr int ch = decltype(CH)::v;
        const lds_cptr cb = ring + (ch % 3) * ATT_SLOT;
        const lds_cptr nb = (ch + 1 < ATT_NCH) ? ring + ((ch + 1) % 3) * ATT_SLOT : cb;
        __builtin_amdgcn_s_setprio(1);
        STEP(sA, sB, cb + 2 * KV_TILE, cb);
        __builtin_amdgcn_s_setprio(0);
        STEP(sB, sA, cb + 3 * KV_TILE, cb + KV_TILE);
        if (wave < 4) wait_vbar<(ch >= 1 && ch <= 5) ? 4 : 0>(); else wait_vbar<0>();
        if (ch + 2 < ATT_NCH) { if (wave < 4) issue_kv(ch + 2); }
        if (ch + 1 < 16) glds2(w1s + (long)(ch + 1) * 128 * (F_ * 2), lds0 + ((ch & 1) ? L_STG : L_WT) + wave * 2048, sv0, sv1);
        if (ch < 5) { if (wave < 4) wslice_block(wo + j * 80, lds0, wave, lane, ch); }
        w1r[ch][0] = lds_tr8(lds + ((ch & 1) ? L_WT : L_STG) + sr0, 512); w1r[ch][1] = lds_tr8(lds + ((ch & 1) ? L_WT : L_STG) + sr1, 512);
        __builtin_amdgcn_s_setprio(3);
        SBAR();
        STEP(sA, sB, nb, cb + 2 * KV_TILE);
        PIN(w1r[ch][0]); PIN(w1r[ch][1]);
        __builtin_amdgcn_s_setprio(2);
        SBAR();
        STEP(sB, sA, nb + KV_TILE, cb + 3 * KV_TILE);
      });
      __builtin_amdgcn_s_setprio(0);
      o = MFMA32(v1, BF8(p1), o);
      float l = xsum32(hi ? o[8] : 0.f);
      bool bad = !(l < 1.8446744e19f);
#pragma unroll
      for (int i = 0; i < 12; ++i) bad |= !(__builtin_fabsf(o[i]) < 3.0e38f);
      if (__builtin_expect(__any(bad), 0)) {
        float m = -INFINITY;
        o = f32x16{};
#pragma unroll 1
        for (int t = 0; t < S_ / 32; ++t) {
          const unsigned tp = kvoff + t * KV_TILE;
          const bf16x8 a16 = BF8(ld16_dev(rws, tp + koff));
          const u32x2 a4 = __builtin_amdgcn_raw_buffer_load_b64(rws, tp + ktoff, 0, 16);
          const bf16x8 w0 = BF8(ld16_dev(rws, tp + voff)), w1f = BF8(ld16_dev(rws, tp + voff + KV_VS));
          f32x16 z = {};
          z = MFMA32(a16, q16, z); z = MFMA32K8(__builtin_bit_cast(bf16x4, a4), q4, z);
          const float mn = __builtin_fmaxf(m, rowmax16(z));
          const float alpha = __builtin_amdgcn_exp2f(m - mn);
          m = mn;
#pragma unroll
          for (int i = 0; i < 16; ++i) { z[i] = __builtin_amdgcn_exp2f(z[i] - m); o[i] *= alpha; }
          bf16x8 a0, a1; pack_p(z, a0, a1);
          o = MFMA32(w0, a0, o); o = MFMA32(w1f, a1, o);
        }
        l = xsum32(hi ? o[8] : 0.f);
      }
      const float rl = __builtin_amdgcn_rcpf(l);
      const unsigned ob = WS_O + layer * XB + tb * 81920 + ((trow >> 4) * 64 + (trow & 15)) * 16;
#pragma unroll
      for (int gq = 0; gq < 3; ++gq) {
        const int d0 = 8 * gq + 4 * hi;
        if (d0 < DH_) {
          const int k = (j * 4 + hh) * DH_ + d0;
          u32x2 w; w[0] = pack_bf16(o[4 * gq] * rl, o[4 * gq + 1] * rl); w[1] = pack_bf16(o[4 * gq + 2] * rl, o[4 * gq + 3] * rl);
          st8_dev(rws, ob + (k >> 5) * 4096 + ((k & 31) >> 3) * 256 + (k & 7) * 2, w);
        }
      }
      STAMP(4);
      publish(ws, fl + FL_O + tb * 128 + j * 4, 1, 0, tag, wave, lane);
      asm volatile("" :: "v"(sink));
      if (wave >= 4) { wslice_block(wo + j * 80, lds0, kq, lane, 5); wslice_block(wo + j * 80, lds0, kq, lane, 6); }
    }
    {
      FRESH();
      if (wave == 0) poll_flags(ws, fl + FL_O + tb * 128, 8, tag, lane, dead);
      wait_vbar<0>();
      STAMP(5);
      const unsigned abase = WS_O + layer * XB + tb * 81920 + (kq * 4 + th * 2) * 1024 + lane * 16;
      u32x4 af[5][2];
#pragma unroll
      for (int s = 0; s < 5; ++s)
#pragma unroll
        for (int t = 0; t < 2; ++t) af[s][t] = ld16_dev_s(rws, abase + t * 1024, s * 16384);
      f32x4 acc[5][2];
#pragma unroll
      for (int i = 0; i < 5; ++i) { acc[i][0] = f32x4{0.f, 0.f, 0.f, 0.f}; acc[i][1] = acc[i][0]; }
      const int q = c16 >> 2, p = lane & 3;
      const int row = 32 * kq + 8 * g + q;
      const unsigned fsw = (((q >> 1) & 1) | ((g & 1) << 1)) * 32;
      const unsigned woff = L_W + row * 128 + 8 * p, toff = L_WT + row * 32 + 8 * p;
      bf16x8 wf[2][5];
#pragma unroll
      for (int i = 0; i < 5; ++i) wf[0][i] = i < 4 ? lds_tr8(lds + woff + ((i * 32) ^ fsw), 512) : lds_tr8(lds + toff, 128);
#pragma unroll
      for (int s = 0; s < 5; ++s) {
        const bf16x8 a0 = __builtin_bit_cast(bf16x8, af[s][0]), a1 = __builtin_bit_cast(bf16x8, af[s][1]);
#pragma unroll
        for (int i = 0; i < 5; ++i) {
          if (s < 4) wf[(s + 1) & 1][i] = i < 4 ? lds_tr8(lds + woff + (s + 1) * 16384 + ((i * 32) ^ fsw), 512) : lds_tr8(lds + toff + (s + 1) * 4096, 128);
          acc[i][0] = MFMA16(wf[s & 1][i], a0, acc[i][0]); acc[i][1] = MFMA16(wf[s & 1][i], a1, acc[i][1]);
          SBAR();
        }
      }
      bar_lds();
#pragma unroll
      for (int i = 0; i < 5; ++i)
#pragma unroll
        for (int t = 0; t < 2; ++t) *(LAS f32x4*)(ldw + ((kq * 5 + i) * 4 + th * 2 + t) * 1024 + lane * 16) = acc[i][t];
      bar_lds();
      gu64* gr = (gu64*)(ws + WS_STAT + layer * ST_LAYER + (tb * 4 + ea) * 4096);
      {
        f32x4 s[3]; u32x2 r[3];
#pragma unroll
        for (int i = 0; i < 3; ++i) {
          const int wt = min(ew0 + i, 4);
          s[i] = sum4(lds, wt * 4 + ea, 20, lane);
          r[i] = *(const LAS u32x2*)(lds + res_off + wt * 32);
        }
        float s1 = 0.f, s2 = 0.f;
#pragma unroll
        for (int i = 0; i < 3; ++i) {
          if (i < ewn) {
            u32x2 w;
            w[0] = pack_bf16(bf_lo(r[i][0]) + s[i][0], bf_hi(r[i][0]) + s[i][1]);
            w[1] = pack_bf16(bf_lo(r[i][1]) + s[i][2], bf_hi(r[i][1]) + s[i][3]);
            *(LAS u32x2*)(ldw + res_off + (ew0 + i) * 32) = w;
            const float a = bf_lo(w[0]), b = bf_hi(w[0]), c = bf_lo(w[1]), d = bf_hi(w[1]);
            s1 += (a + b) + (c + d);
            s2 += (a * a + b * b) + (c * c + d * d);
          }
        }
        s1 = xsum32(xsum16(s1)); s2 = xsum32(xsum16(s2));
        if (lane < 32) __hip_atomic_store(gr + (2 * j + th) * 32 + lane, ((unsigned long long)tag << 32) | __float_as_uint(g ? s2 : s1), RLX_AGENT);
      }
      bar_lds();
      if (wave >= 4) {
        const int r = lane >> 3, pos = lane & 7;
        const unsigned pe = (((pos >> 1) ^ ((r >> 1) & 1)) << 1 | (pos & 1)) * 16;
        const unsigned v0 = r * (S_ * 2) + pe + 4096, v1 = (r + 8) * (S_ * 2) + (pe ^ 64) + 3072, vstep = 16 * (S_ * 2) - 2048;
        const char* wsrc = (const char*)(w2 + tb * 64) - 4096 + (long)kq * 32 * (S_ * 2);
#pragma unroll 1
        for (int st = 0; st < 4; ++st) glds4<false>(wsrc + (long)st * 128 * (S_ * 2), lds0 + L_RING + st * 16384 + kq * 4096, v0, v1, v0 + vstep, v1 + vstep);
        asm volatile("s_barrier" ::: "memory");
      } else {
        u32x2 xv[5];
#pragma unroll
        for (int wt = 0; wt < 5; ++wt) xv[wt] = __builtin_bit_cast(u32x2, lds_tr(lds + L_RES + (ea * 16 + 4 * g + q) * 160 + wt * 32 + 8 * p));
        float tot;
        {
          unsigned spins = 0;
          for (;;) {
            bool ok = true; tot = 0.f;
#pragma unroll
            for (int i = 0; i < 8; ++i) {
              const unsigned long long v = __hip_atomic_load(gr + i * 64 + lane, RLX_AGENT);
              tot += __uint_as_float((unsigned)v); ok &= (unsigned)(v >> 32) == tag;
            }
            if (__all(ok) || dead) break;
            if (++spins > SPIN_CAP) dead = true;
            __builtin_amdgcn_s_sleep(1);
          }
        }
        STAMP(6);
        tot = xsum32(tot);
        float mu4[4], rr4[4], m24[4], r24[4];
#pragma unroll
        for (int e = 0; e < 4; ++e) { mu4[e] = __shfl(tot, 4 * g + e); rr4[e] = __shfl(tot, 16 + 4 * g + e); }
#pragma unroll
        for (int e = 0; e < 4; ++e) {
          float mu, var; ln_finish(mu4[e], rr4[e], mu, var);
          mu4[e] = rnd1(mu); rr4[e] = ln_rsd(var);
          m24[e] = rnd1((mu - mu4[e]) * rr4[e]); r24[e] = ln_rsd(var * rr4[e] * rr4[e]);
        }
        const unsigned hb = WS_H + layer * XB + j * 327680 + ((tb * 2 + (ea >> 1)) * 5) * 1024 + (c16 + 16 * ((ea & 1) * 2 + (g >> 1))) * 16 + (g & 1) * 8;
#pragma unroll
        for (int wt = 0; wt < 5; ++wt) {
          const u32x2 v = xv[wt];
          u32x2 w;
          w[0] = pack_bf16(bf_lo(v[0]) - mu4[0], bf_hi(v[0]) - mu4[1]); w[1] = pack_bf16(bf_lo(v[1]) - mu4[2], bf_hi(v[1]) - mu4[3]);
          w[0] = pack_bf16(bf_lo(w[0]) * rr4[0], bf_hi(w[0]) * rr4[1]); w[1] = pack_bf16(bf_lo(w[1]) * rr4[2], bf_hi(w[1]) * rr4[3]);
          w[0] = pack_bf16(bf_lo(w[0]) - m24[0], bf_hi(w[0]) - m24[1]); w[1] = pack_bf16(bf_lo(w[1]) - m24[2], bf_hi(w[1]) - m24[3]);
          w[0] = pack_bf16(bf_lo(w[0]) * r24[0], bf_hi(w[0]) * r24[1]); w[1] = pack_bf16(bf_lo(w[1]) * r24[2], bf_hi(w[1]) * r24[3]);
          st8_dev(rws, hb + wt * 1024, w);
        }
        publish(ws, fl + FL_H + j * 512 + tb * 4, 4, 128, tag, wave, lane);
      }
      STAMP(7);
    }
    {
      FRESH();
      if (has4) {
        const int f = ft * 64 + ea * 16 + 4 * g;
        const u32x2 bias = *(const u32x2*)(b1 + f);
        if (wave == 0) poll_flags(ws, fl + FL_H + panel4 * 512 + ((idx >> 2) & 3) * 128, 32, tag, lane, dead);
        asm volatile("s_barrier" ::: "memory");
        STAMP(8);
        const unsigned l16 = lane * 16;
        const int po = (th ? 12 + 2 * kq : 3 * kq) * 1024;
        const char* hsrc = ws + WS_H + layer * XB + panel4 * 327680 + po;
        auto issue = [&](int st) {
          if (th == 0) glds_lin<3, true>(hsrc + st * 20480, lds0 + (st & 3) * 20480 + po, l16);
          else glds_lin<2, true>(hsrc + st * 20480, lds0 + (st & 3) * 20480 + po, l16);
        };
        issue(0); issue(1); issue(2);
        f32x4 acc[2][5];
#pragma unroll
        for (int i = 0; i < 2; ++i)
#pragma unroll
          for (int a = 0; a < 5; ++a) acc[i][a] = f32x4{0.f, 0.f, 0.f, 0.f};
        const lds_cptr ab = lds + kq * 5120 + l16;
        static_for<0, 16>([&](auto ST) {
          constexpr int st = decltype(ST)::v;
          constexpr int ahead = (15 - st) < 2 ? (15 - st) : 2;
          if (th == 0) wait_bar<3 * ahead>(); else wait_bar<2 * ahead>();
          bf16x8 af[5];
#pragma unroll
          for (int a = 0; a < 5; ++a) af[a] = lds_b128(ab + (st & 3) * 20480 + a * 1024);
          if (st + 3 < 16) { if (th == 0) issue(st + 3); }
          SBAR();
#pragma unroll
          for (int a = 0; a < 5; ++a) { acc[0][a] = MFMA16(w1r[st][0], af[a], acc[0][a]); acc[1][a] = MFMA16(w1r[st][1], af[a], acc[1][a]); }
          SBAR();
          if (st + 3 < 16) { if (th == 1) issue(st + 3); }
        });
        bar_lds();
#pragma unroll
        for (int i = 0; i < 2; ++i)
#pragma unroll
          for (int a = 0; a < 5; ++a) *(LAS f32x4*)(ldw + ((kq * 4 + 2 * th + i) * 5 + a) * 1024 + l16) = acc[i][a];
        bar_lds();
        STAMP(9);
        const unsigned ub = WS_U + layer * UB + ((panel4 * U_KS + (f >> 5)) * 5) * 1024 + (c16 + 16 * ((f & 31) >> 3)) * 16 + (f & 7) * 2;
        f32x4 sv[3];
#pragma unroll
        for (int i = 0; i < 3; ++i) sv[i] = sum4(lds, ea * 5 + min(ew0 + i, 4), 20, lane);
#pragma unroll
        for (int i = 0; i < 3; ++i) {
          if (i < ewn) {
            const f32x4 s = sv[i];
            u32x2 w;
            w[0] = pack_bf16(gelu_tanh(s[0] + bf_lo(bias[0])), gelu_tanh(s[1] + bf_hi(bias[0])));
            w[1] = pack_bf16(gelu_tanh(s[2] + bf_lo(bias[1])), gelu_tanh(s[3] + bf_hi(bias[1])));
            st8_dev(rws, ub + (ew0 + i) * 1024, w);
          }
        }
        publish(ws, fl + FL_U + panel4 * 512 + ft * 4, 4, 128, tag, wave, lane);
      }
    }
    STAMP(10);
    {
      FRESH();
      const int tok = tb * 64 + ea * 16 + c16;
      const float bias = __uint_as_float((unsigned)b2[tok] << 16);
      if (wave >= 4) {
        const int r = lane >> 3, pos = lane & 7;
        const unsigned pe = (((pos >> 1) ^ ((r >> 1) & 1)) << 1 | (pos & 1)) * 16;
        const unsigned v0 = r * (S_ * 2) + pe + 4096, v1 = (r + 8) * (S_ * 2) + (pe ^ 64) + 3072, vstep = 16 * (S_ * 2) - 2048;
        const char* wsrc = (const char*)(w2 + tb * 64) - 4096 + (long)kq * 32 * (S_ * 2);
#pragma unroll 1
        for (int st = 4; st < 9; ++st) glds4<false>(wsrc + (long)st * 128 * (S_ * 2), lds0 + (st - 4) * 16384 + kq * 4096, v0, v1, v0 + vstep, v1 + vstep);
        asm volatile("s_barrier" ::: "memory");
        static_for<0, 14>([&](auto ST) {
          constexpr int st = decltype(ST)::v;
          constexpr int last = (st + 7 > 13) ? 13 : (st + 7 < 8 ? 8 : st + 7);
          wait_vbar<4 * (last - st)>();
          if (st >= 1 && st + 8 < 14) {
            const long o = (st + 8 == 13 && kq >= 2) ? (long)(2 * 13 - 1) * 64 * (S_ * 2) : (long)(st + 8) * 128 * (S_ * 2);
            glds4<false>(wsrc + o, lds0 + w2_slot(st + 8) + kq * 4096, v0, v1, v0 + vstep, v1 + vstep);
          }
        });
        asm volatile("s_barrier" ::: "memory");
      } else {
        if (wave == 0) poll_flags(ws, fl + FL_U + j * 512 + (tb & 3) * 128, 27, tag, lane, dead);
        asm volatile("s_barrier" ::: "memory");
        STAMP(11);
        const unsigned abase = WS_U + layer * UB + j * (U_KS * 5 * 1024) + kq * 5120 + lane * 16;
        const int q = c16 >> 2, p = lane & 3;
        const unsigned fsw = (((q >> 1) & 1) | ((g & 1) << 1)) * 32;
        const lds_cptr wrow = lds + (32 * kq + 8 * g + q) * 128 + 8 * p;
        u32x4 af[3][5];
#pragma unroll
        for (int st = 0; st < 3; ++st)
#pragma unroll
          for (int a = 0; a < 5; ++a) af[st][a] = ld16_dev_s(rws, abase + (a & 3) * 1024, st * 20480 + (a >> 2) * 4096);
        f32x4 acc[4][5];
#pragma unroll
        for (int i = 0; i < 4; ++i)
#pragma unroll
          for (int a = 0; a < 5; ++a) acc[i][a] = f32x4{0.f, 0.f, 0.f, 0.f};
        static_for<0, 14>([&](auto ST) {
          constexpr int st = decltype(ST)::v;
          bar_lds();
          if (st < 13 || kq < 2) {
            bf16x8 wf[4];
#pragma unroll
            for (int i = 0; i < 4; ++i) wf[i] = lds_tr8(wrow + w2_slot(st) + ((i * 32) ^ fsw), 512);
            SBAR();
#pragma unroll
            for (int a = 0; a < 5; ++a) {
              const bf16x8 x = __builtin_bit_cast(bf16x8, af[st % 3][a]);
#pragma unroll
              for (int i = 0; i < 4; ++i) acc[i][a] = MFMA16(x, wf[i], acc[i][a]);
              SBAR();
              if (st + 3 < 14) af[st % 3][a] = ld16_dev_s(rws, abase + (a & 3) * 1024, (st + 3) * 20480 + (a >> 2) * 4096);
              SBAR();
            }
          }
        });
        bar_lds();
#pragma unroll
        for (int i = 0; i < 4; ++i)
#pragma unroll
          for (int a = 0; a < 5; ++a) *(LAS f32x4*)(ldw + ((kq * 4 + i) * 5 + a) * 1024 + lane * 16) = acc[i][a];
      }
      bar_lds();
      STAMP(12);
      f32x4 sv[3]; u32x2 rv[3];
#pragma unroll
      for (int i = 0; i < 3; ++i) { const int at = min(ew0 + i, 4); sv[i] = sum4(lds, ea * 5 + at, 20, lane); rv[i] = *(const LAS u32x2*)(lds + res_off + at * 32); }
#pragma unroll
      for (int i = 0; i < 3; ++i) {
        if (i < ewn) {
          const int at = ew0 + i;
          const f32x4 s = sv[i];
          const u32x2 r = rv[i];
          u32x2 w;
          w[0] = pack_bf16(bf_lo(r[0]) + (s[0] + bias), bf_hi(r[0]) + (s[1] + bias));
          w[1] = pack_bf16(bf_lo(r[1]) + (s[2] + bias), bf_hi(r[1]) + (s[3] + bias));
          const int d = j * 80 + at * 16 + 4 * g;
          if (layer == 0) {
            *(LAS u32x2*)(ldw + res_off + at * 32) = w;
            st8_dev(rws, WS_X + tb * 81920 + (((d >> 5) * 4 + ea) * 64 + c16 + 16 * ((d & 31) >> 3)) * 16 + (d & 7) * 2, w);
          } else {
            *(u32x2*)(out + (long)tok * D_ + d) = w;
          }
        }
      }
      if (layer == 0) publish(ws, WS_FLAGS + FL_X + tb * 128 + j * 4, 1, 0, tag, wave, lane);
      STAMP(13);
    }
  }
  if (blockIdx.x == 0 && tid == 0) __hip_atomic_store((gu32*)(ws + WS_EPOCH), tag, RLX_AGENT);
}

extern "C" void kernel_launch(void* const* d_in, const int* in_sizes, int n_in, void* d_out, int out_size, void* d_ws,
                              size_t ws_size, hipStream_t stream) {
  (void)hipFuncSetAttribute((const void*)mega_kernel, hipFuncAttributeMaxDynamicSharedMemorySize, LDS_BYTES);
  mega_kernel<<<256, 512, LDS_BYTES, stream>>>((const u16*)d_in[0], (const u16*)d_in[1], (const u16*)d_in[2], (const u16*)d_in[3],
                                               (const u16*)d_in[4], (const u16*)d_in[6], (const u16*)d_in[7], (const u16*)d_in[8],
                                               (const u16*)d_in[9], (u16*)d_out, (char*)d_ws);
}
```

```cpp
#include <hip/hip_runtime.h>
#include <hip/hip_bf16.h>
#include <stdint.h>

typedef __attribute__((ext_vector_type(8))) short bf16x8;
typedef __attribute__((ext_vector_type(4))) short bf16x4;
typedef __attribute__((ext_vector_type(16))) float f32x16;
typedef __attribute__((ext_vector_type(4))) float f32x4;
typedef __attribute__((ext_vector_type(2))) float f32x2;
typedef __attribute__((ext_vector_type(2))) __bf16 bf2_t;
typedef __attribute__((ext_vector_type(4))) unsigned u32x4;
typedef __attribute__((ext_vector_type(2))) unsigned u32x2;
typedef unsigned short u16;
#define LAS __attribute__((address_space(3)))
#define GAS __attribute__((address_space(1)))
typedef LAS const char* lds_cptr;
typedef LAS char* lds_ptr;
typedef GAS unsigned gu32;
typedef GAS unsigned long long gu64;
#define DI __device__ __forceinline__
#define RLX_AGENT __ATOMIC_RELAXED, __HIP_MEMORY_SCOPE_AGENT

constexpr int S_ = 2048, D_ = 640, DH_ = 20, F_ = 1728;
constexpr float LN_EPS = 1e-5f;
constexpr float QSCALE = 0.22360679774997896f * 1.4426950408889634f;

constexpr int KV_TILE = 2688, KV_KT = 1024, KV_V = 1280, KV_VS = 704;

constexpr unsigned WS_EPOCH = 0;
constexpr unsigned WS_FLAGS = 4096, FL_LAYER = 65536;
constexpr unsigned FL_KV = 0;
constexpr unsigned FL_O = 32768;
constexpr unsigned FL_X = 36864;
constexpr unsigned FL_H = 40960;
constexpr unsigned FL_U = 45056;
constexpr unsigned WS_DIAG = 262144;
constexpr unsigned WS_STAT = 524288, ST_LAYER = 524288;
constexpr unsigned WS_KV = WS_STAT + 2 * ST_LAYER, KV_LAYER = 196608;
constexpr unsigned XB = S_ * D_ * 2;
constexpr unsigned WS_O = WS_KV + 2 * KV_LAYER;
constexpr unsigned WS_H = WS_O + 2 * XB;
constexpr int U_KS = 56;
constexpr unsigned UB = 8 * U_KS * 5 * 1024;
constexpr unsigned WS_U = WS_H + 2 * XB;
constexpr unsigned WS_X = WS_U + 2 * UB;
constexpr unsigned WS_END = WS_X + XB;

constexpr int L_W = 0;
constexpr int L_WT = 81920;
constexpr int L_RING = 81920;
constexpr int L_ATT = 102400;
constexpr int L_QT = 81920;
constexpr int L_KVP = 128064;
constexpr int L_STG = 134656;
constexpr int L_RES = 151040;
constexpr int L_DUMP = 161280;
constexpr int LDS_BYTES = 163840;
constexpr unsigned SPIN_CAP = 1u << 21;

DI unsigned pack_bf16(float lo, float hi) {
  f32x2 v = {lo, hi};
  bf2_t b = __builtin_convertvector(v, bf2_t);
  return __builtin_bit_cast(unsigned, b);
}
DI float bf_lo(unsigned u) { return __uint_as_float(u << 16); }
DI float bf_hi(unsigned u) { return __uint_as_float(u & 0xffff0000u); }
DI float rnd1(float v) { return bf_lo(pack_bf16(v, 0.f)); }
DI float ln_rsd(float var) { return __builtin_amdgcn_rcpf(rnd1(__builtin_amdgcn_sqrtf(rnd1(rnd1(var) + LN_EPS)))); }

#define GLDS_HEAD "s_mov_b32 m0, %1\n\ts_nop 4\n\t"
template <bool DEV> DI void glds1(const void* sbase, unsigned m0v, unsigned v0) {
  if (DEV) asm volatile(GLDS_HEAD "global_load_lds_dwordx4 %2, %0 sc1" ::"s"(sbase), "s"(m0v), "v"(v0) : "memory");
  else asm volatile(GLDS_HEAD "global_load_lds_dwordx4 %2, %0" ::"s"(sbase), "s"(m0v), "v"(v0) : "memory");
}
template <bool DEV> DI void glds4(const void* sbase, unsigned m0v, unsigned v0, unsigned v1, unsigned v2, unsigned v3) {
  if (DEV)
    asm volatile(GLDS_HEAD "global_load_lds_dwordx4 %2, %0 sc1\n\tglobal_load_lds_dwordx4 %3, %0 offset:1024 sc1\n\t"
                           "global_load_lds_dwordx4 %4, %0 offset:2048 sc1\n\tglobal_load_lds_dwordx4 %5, %0 offset:3072 sc1"
                 ::"s"(sbase), "s"(m0v), "v"(v0), "v"(v1), "v"(v2), "v"(v3) : "memory");
  else
    asm volatile(GLDS_HEAD "global_load_lds_dwordx4 %2, %0\n\tglobal_load_lds_dwordx4 %3, %0 offset:1024\n\t"
                           "global_load_lds_dwordx4 %4, %0 offset:2048\n\tglobal_load_lds_dwordx4 %5, %0 offset:3072"
                 ::"s"(sbase), "s"(m0v), "v"(v0), "v"(v1), "v"(v2), "v"(v3) : "memory");
}
template <int N, bool DEV> DI void glds_lin(const void* sbase, unsigned m0v, unsigned v) {
  if (N == 1) glds1<DEV>(sbase, m0v, v);
  else if (N == 4) glds4<DEV>(sbase, m0v, v, v, v, v);
  else if (N == 2) {
    if (DEV) asm volatile(GLDS_HEAD "global_load_lds_dwordx4 %2, %0 sc1\n\tglobal_load_lds_dwordx4 %2, %0 offset:1024 sc1" ::"s"(sbase), "s"(m0v), "v"(v) : "memory");
    else asm volatile(GLDS_HEAD "global_load_lds_dwordx4 %2, %0\n\tglobal_load_lds_dwordx4 %2, %0 offset:1024" ::"s"(sbase), "s"(m0v), "v"(v) : "memory");
  } else {
    if (DEV) asm volatile(GLDS_HEAD "global_load_lds_dwordx4 %2, %0 sc1\n\tglobal_load_lds_dwordx4 %2, %0 offset:1024 sc1\n\tglobal_load_lds_dwordx4 %2, %0 offset:2048 sc1" ::"s"(sbase), "s"(m0v), "v"(v) : "memory");
    else asm volatile(GLDS_HEAD "global_load_lds_dwordx4 %2, %0\n\tglobal_load_lds_dwordx4 %2, %0 offset:1024\n\tglobal_load_lds_dwordx4 %2, %0 offset:2048" ::"s"(sbase), "s"(m0v), "v"(v) : "memory");
  }
}
DI void glds2(const void* sbase, unsigned m0v, unsigned v0, unsigned v1) {
  asm volatile(GLDS_HEAD "global_load_lds_dwordx4 %2, %0\n\tglobal_load_lds_dwordx4 %3, %0 offset:1024" ::"s"(sbase), "s"(m0v), "v"(v0), "v"(v1) : "memory");
}
#define UNI(x) __builtin_amdgcn_readfirstlane(x)
template <int N> DI void wait_bar() { asm volatile("s_waitcnt vmcnt(%0) lgkmcnt(0)\n\ts_barrier" ::"i"(N) : "memory"); }
template <int N> DI void wait_vbar() { asm volatile("s_waitcnt vmcnt(%0)\n\ts_barrier" ::"i"(N) : "memory"); }
DI void bar_lds() { asm volatile("s_waitcnt lgkmcnt(0)\n\ts_barrier" ::: "memory"); }
typedef short v4i16_t __attribute__((ext_vector_type(4)));
DI bf16x4 lds_tr(lds_cptr p) {
  return __builtin_bit_cast(bf16x4, __builtin_amdgcn_ds_read_tr16_b64_v4i16((LAS v4i16_t*)p));
}
DI bf16x8 lds_tr8(lds_cptr p, int step) {
  const bf16x4 lo = lds_tr(p), hi = lds_tr(p + step);
  return __builtin_shufflevector(lo, hi, 0, 1, 2, 3, 4, 5, 6, 7);
}
DI bf16x8 lds_b128(lds_cptr p) { return *(const LAS bf16x8*)p; }
#define MFMA16(a, b, c) __builtin_amdgcn_mfma_f32_16x16x32_bf16((a), (b), (c), 0, 0, 0)
#define MFMA32(a, b, c) __builtin_amdgcn_mfma_f32_32x32x16_bf16((a), (b), (c), 0, 0, 0)
#define MFMA32K8(a, b, c) __builtin_amdgcn_mfma_f32_32x32x8bf16_1k((a), (b), (c), 0, 0, 0)
#define SBAR() __builtin_amdgcn_sched_barrier(0)
#define PIN(x) asm volatile("" : "+v"(x))
typedef __amdgpu_buffer_rsrc_t rsrc_t;
DI void st8_dev(rsrc_t r, unsigned off, u32x2 v) { __builtin_amdgcn_raw_buffer_store_b64(v, r, off, 0, 16); }
DI u32x4 ld16_dev(rsrc_t r, unsigned off) { return __builtin_amdgcn_raw_buffer_load_b128(r, off, 0, 16); }
DI u32x4 ld16_dev_s(rsrc_t r, unsigned off, unsigned soff) { return __builtin_amdgcn_raw_buffer_load_b128(r, off, soff, 16); }

DI void poll_flags(const char* ws, unsigned off, int n, unsigned tag, int lane, bool& dead) {
  gu32* f = (gu32*)(ws + off);
  unsigned spins = 0;
  while (!dead) {
    unsigned v = tag;
    if (lane < n) v = __hip_atomic_load(f + lane, RLX_AGENT);
    if (__all(v == tag)) break;
    if (++spins > SPIN_CAP) dead = true;
    __builtin_amdgcn_s_sleep(1);
  }
  asm volatile("" ::: "memory");
}
DI void publish(const char* ws, unsigned off, int nrep, unsigned rstride, unsigned tag, int wave, int lane) {
  wait_bar<0>();
  if (wave == 0 && lane < nrep) __hip_atomic_store((gu32*)(ws + off + lane * rstride), tag, RLX_AGENT);
}

DI float fmax3(float a, float b, float c) { return __builtin_fmaxf(__builtin_fmaxf(a, b), c); }
DI float rowmax16(const f32x16& s) {
  float a = fmax3(s[0], s[1], s[2]), b = fmax3(s[3], s[4], s[5]);
  a = fmax3(a, s[6], s[7]); b = fmax3(b, s[8], s[9]); a = fmax3(a, s[10], s[11]); b = fmax3(b, s[12], s[13]);
  a = fmax3(a, s[14], s[15]); a = __builtin_fmaxf(a, b);
  return __builtin_fmaxf(a, __shfl_xor(a, 32));
}
DI void pack_p(const f32x16& p, bf16x8& lo, bf16x8& hi) {
  u32x4 a, b;
#pragma unroll
  for (int i = 0; i < 4; ++i) { a[i] = pack_bf16(p[2 * i], p[2 * i + 1]); b[i] = pack_bf16(p[8 + 2 * i], p[9 + 2 * i]); }
  lo = __builtin_bit_cast(bf16x8, a); hi = __builtin_bit_cast(bf16x8, b);
}
DI float gelu_tanh(float u) {
  const float z = 0.7978845608028654f * (u + 0.044715f * u * u * u);
  const float e = __builtin_amdgcn_exp2f(-2.885390081777927f * z);
  return u * __builtin_amdgcn_rcpf(1.f + e);
}

DI void wslice_block(const u16* w, unsigned lds0, int lw, int lane, int blk) {
  const char* src = (const char*)w + lw * (160 * 1280);
  if (blk < 5) {
    const int r = lane >> 3, pos = lane & 7;
    const unsigned pe = (((pos >> 1) ^ ((r >> 1) & 1)) << 1 | (pos & 1)) * 16;
    const unsigned v0 = r * 1280 + pe, v1 = r * 1280 + (pe ^ 64) + 9216;
    glds4<false>(src + blk * (32 * 1280), lds0 + L_W + lw * 20480 + blk * 4096, v0, v1, v0 + 18432, v1 + 18432);
  } else {
    const unsigned t0 = (lane >> 1) * 1280 + 128 + (lane & 1) * 16;
    if (blk == 5) glds4<false>(src, lds0 + L_WT + lw * 5120, t0, t0 + 39936, t0 + 2 * 39936, t0 + 3 * 39936);
    else glds1<false>(src + 128 * 1280, lds0 + L_WT + lw * 5120 + 4096, t0);
  }
}

DI void touch(const void* sbase, unsigned voff, unsigned& sink) {
  asm volatile("s_nop 4\n\tglobal_load_dword %0, %1, %2" : "+v"(sink) : "v"(voff), "s"(sbase) : "memory");
}
DI float xsum16(float x) {
  auto r = __builtin_amdgcn_permlane16_swap(__float_as_uint(x), __float_as_uint(x), false, false);
  return __uint_as_float(r[0]) + __uint_as_float(r[1]);
}
DI float xsum32(float x) {
  auto r = __builtin_amdgcn_permlane32_swap(__float_as_uint(x), __float_as_uint(x), false, false);
  return __uint_as_float(r[0]) + __uint_as_float(r[1]);
}
DI f32x4 ldf4(lds_cptr p) { return *(const LAS f32x4*)p; }

DI f32x4 sum4(lds_cptr lds, int t, int nt, int lane) {
  f32x4 s = *(const LAS f32x4*)(lds + t * 1024 + lane * 16);
#pragma unroll
  for (int k = 1; k < 4; ++k) s += *(const LAS f32x4*)(lds + (k * nt + t) * 1024 + lane * 16);
  return s;
}
DI void ln_finish(float s1, float s2, float& mu, float& var) { mu = s1 * (1.f / D_); var = fmaxf(s2 * (1.f / D_) - mu * mu, 0.f); }

constexpr int ATT_CH = 4;
constexpr int ATT_CHB = ATT_CH * KV_TILE;
constexpr int ATT_SLOT = 10752;
constexpr int ATT_NCH = S_ / 32 / ATT_CH;
constexpr int w2_slot(int st) { return (st % 9) < 4 ? L_RING + (st % 9) * 16384 : ((st % 9) - 4) * 16384; }
template <int N> struct IC { static constexpr int v = N; };
template <int I, int N, class F> DI void static_for(F&& f) { if constexpr (I < N) { f(IC<I>{}); static_for<I + 1, N>(f); } }

#define STAMP(i) do { } while (0)

__global__ __launch_bounds__(512, 2) void mega_kernel(const u16* __restrict__ x0, const u16* __restrict__ wq, const u16* __restrict__ wk,
                                                      const u16* __restrict__ wv, const u16* __restrict__ wo, const u16* __restrict__ w1,
                                                      const u16* __restrict__ b1, const u16* __restrict__ w2, const u16* __restrict__ b2,
                                                      u16* __restrict__ out, char* ws) {
  extern __shared__ __attribute__((aligned(16))) char smem[];
  const int tid = threadIdx.x;
  const unsigned lds0 = (unsigned)(uintptr_t)smem;
  const lds_cptr lds = (lds_cptr)smem;
  const lds_ptr ldw = (lds_ptr)smem;
#define FRESH() \
  int lane = tid & 63; asm volatile("" : "+v"(lane)); \
  int wave = UNI(tid >> 6); asm volatile("" : "+s"(wave)); \
  int bid = blockIdx.x; asm volatile("" : "+s"(bid)); \
  const int xcd = bid & 7, idx = bid >> 3, tb = xcd * 4 + (idx >> 3), j = idx & 7; \
  const int g = lane >> 4, c16 = lane & 15, kq = wave & 3, th = wave >> 2; \
  const int ea = wave & 3, ew0 = th ? 3 : 0, ewn = th ? 2 : 3; \
  const unsigned res_off = L_RES + (ea * 16 + c16) * 160 + 8 * g; \
  const int panel4 = (xcd & 1) * 4 + (idx & 3), ft = (xcd >> 1) * 7 + (idx >> 2); \
  const bool has4 = idx < 28 && ft < 27; \
  (void)xcd; (void)g; (void)c16; (void)kq; (void)th; (void)ea; (void)ew0; (void)ewn; (void)res_off; (void)tb; (void)j; (void)panel4; (void)has4
  const rsrc_t rws = __builtin_amdgcn_make_buffer_rsrc(ws, 0, WS_END, 0x00020000);
  unsigned tag = 0;
  bool dead = false;

#pragma unroll 1
  for (int layer = 0; layer < 2; ++layer) {
    const unsigned fl = WS_FLAGS + layer * FL_LAYER;
    const unsigned kvoff = WS_KV + layer * KV_LAYER;
    unsigned sink = 0;
    {
      FRESH();
      STAMP(0);
      auto load_kvw = [&]() {
        const char* xs = (const char*)((j & 1) ? wv : wk);
        if (wave < 3) {
          glds_lin<4, false>(xs + wave * 8192, lds0 + L_ATT + wave * 8192, lane * 16);
          glds_lin<4, false>(xs + wave * 8192 + 4096, lds0 + L_ATT + wave * 8192 + 4096, lane * 16);
        } else glds1<false>(xs + 24576, lds0 + L_ATT + 24576, lane * 16);
      };
      u32x4 af[5][2];
      if (layer == 0) {
        const rsrc_t ra = __builtin_amdgcn_make_buffer_rsrc((void*)x0, 0, XB, 0x00020000);
        const unsigned abase = (tb * 64 + th * 32 + c16) * (D_ * 2) + kq * 64 + g * 16;
#pragma unroll
        for (int s = 0; s < 5; ++s)
#pragma unroll
          for (int t = 0; t < 2; ++t) af[s][t] = __builtin_amdgcn_raw_buffer_load_b128(ra, abase + s * 256, t * (16 * D_ * 2), 0);
        const unsigned epoch = __hip_atomic_load((gu32*)(ws + WS_EPOCH), RLX_AGENT);
        if (wave < 4) load_kvw();
        else touch(wq, min((idx * 4 + kq) * 50 + lane, 6399) * 128, sink);
        STAMP(1);
        wait_vbar<0>();
        tag = UNI(epoch) + 1u;
        if (wave >= 4) {
#pragma unroll 1
          for (int blk = 0; blk < 7; ++blk) wslice_block(wq + j * 80, lds0, kq, lane, blk);
        } else {
#pragma unroll 1
          for (int pc = wave; pc < 10; pc += 4) {
            const int n = pc * 64 + lane, row = n / 10, ch = n - row * 10;
            glds1<false>(x0 + (long)tb * 64 * D_ + j * 80, lds0 + L_RES + pc * 1024, row * 1280 + ch * 16);
          }
        }
      } else {
        if (wave >= 4) {
#pragma unroll 1
          for (int blk = 0; blk < 7; ++blk) wslice_block(wq + j * 80, lds0, kq, lane, blk);
        } else load_kvw();
        if (wave == 0) poll_flags(ws, WS_FLAGS + FL_X + tb * 128, 8, tag, lane, dead);
        wait_vbar<0>();
        STAMP(1);
        const unsigned abase = WS_X + tb * 81920 + (kq * 4 + th * 2) * 1024 + lane * 16;
#pragma unroll
        for (int s = 0; s < 5; ++s)
#pragma unroll
          for (int t = 0; t < 2; ++t) af[s][t] = ld16_dev_s(rws, abase + t * 1024, s * 16384);
      }
      bf16x8 ones;
#pragma unroll
      for (int i = 0; i < 8; ++i) ones[i] = (short)0x3f80;
      const f32x4 z4 = {0.f, 0.f, 0.f, 0.f};
      const int q = c16 >> 2, p = lane & 3;
      const int row = 32 * kq + 8 * g + q;
      const unsigned fsw = (((q >> 1) & 1) | ((g & 1) << 1)) * 32;
      const unsigned woff = L_W + row * 128 + 8 * p, toff = L_WT + row * 32 + 8 * p, xoff = L_ATT + row * 40 + 8 * p;
      LAS float* stat = (LAS float*)(ldw + L_DUMP);
      LAS float* csum = (LAS float*)(ldw + L_KVP + 20480);
      const int tk = j >> 1;
      const bool isv = j & 1;
      {
        f32x4 a1s[2] = {z4, z4}, a2s[2] = {z4, z4}, akv[2] = {z4, z4}, aok[2] = {z4, z4};
#pragma unroll
        for (int s = 0; s < 5; ++s) {
          const bf16x8 a0 = __builtin_bit_cast(bf16x8, af[s][0]), a1 = __builtin_bit_cast(bf16x8, af[s][1]);
          if (th == (tk >> 1)) {
            const bf16x8 a = (tk & 1) ? a1 : a0;
            const bf16x8 x0f = lds_tr8(lds + xoff + s * 5120, 160), x1f = lds_tr8(lds + xoff + s * 5120 + 32, 160);
            if (!isv) {
              akv[0] = MFMA16(x0f, a, akv[0]); akv[1] = MFMA16(x1f, a, akv[1]);
              aok[0] = MFMA16(x0f, ones, aok[0]); aok[1] = MFMA16(x1f, ones, aok[1]);
            } else {
              akv[0] = MFMA16(a, x0f, akv[0]); akv[1] = MFMA16(a, x1f, akv[1]);
              aok[0] = MFMA16(ones, x0f, aok[0]); aok[1] = MFMA16(ones, x1f, aok[1]);
            }
          }
          a1s[0] = MFMA16(ones, a0, a1s[0]); a1s[1] = MFMA16(ones, a1, a1s[1]);
          a2s[0] = MFMA16(a0, a0, a2s[0]); a2s[1] = MFMA16(a1, a1, a2s[1]);
        }
        if (g == q) {
#pragma unroll
          for (int t = 0; t < 2; ++t) {
            const float d = (p & 2) ? ((p & 1) ? a2s[t][3] : a2s[t][2]) : ((p & 1) ? a2s[t][1] : a2s[t][0]);
            *(LAS f32x2*)(stat + (kq * 64 + th * 32 + t * 16 + c16) * 2) = f32x2{a1s[t][0], d};
          }
        }
        if (th == (tk >> 1)) {
          const bool l2 = isv ? (c16 < 4) : (g == 0);
          const int i2 = isv ? g * 4 + c16 : c16;
          const int o = L_KVP + kq * 2560;
          *(LAS f32x4*)(ldw + o + lane * 16) = akv[0];
          *(LAS f32x4*)(ldw + o + 1280 + lane * 16) = aok[0];
          if (l2) { *(LAS f32x4*)(ldw + o + 1024 + i2 * 16) = akv[1]; *(LAS f32x4*)(ldw + o + 2304 + i2 * 16) = aok[1]; }
        }
      }
      wait_bar<0>();
      float mu, rs;
      {
        f32x2 sv[4];
#pragma unroll
        for (int k = 0; k < 4; ++k) sv[k] = *(const LAS f32x2*)(stat + (k * 64 + ea * 16 + c16) * 2);
        float var; ln_finish((sv[0][0] + sv[1][0]) + (sv[2][0] + sv[3][0]), (sv[0][1] + sv[1][1]) + (sv[2][1] + sv[3][1]), mu, var);
        mu = rnd1(mu); rs = ln_rsd(var);
      }
      if (ea == tk) {
        const int li = th == 0 ? lane : 64 + (isv ? g * 4 + (c16 & 3) : c16);
        f32x4 s = z4, cs = z4;
#pragma unroll
        for (int k = 0; k < 4; ++k) { s += ldf4(lds + L_KVP + k * 2560 + li * 16); cs += ldf4(lds + L_KVP + k * 2560 + 1280 + li * 16); }
        const unsigned tile = kvoff + (tb * 2 + (ea >> 1)) * KV_TILE;
        if (!isv) {
          u32x2 w; w[0] = pack_bf16((s[0] - mu * cs[0]) * rs, (s[1] - mu * cs[1]) * rs); w[1] = pack_bf16((s[2] - mu * cs[2]) * rs, (s[3] - mu * cs[3]) * rs);
          const int r32 = (ea & 1) * 16 + c16;
          if (th == 0) st8_dev(rws, tile + ((g >> 1) * 32 + r32) * 16 + (g & 1) * 8, w);
          else if (g == 0) st8_dev(rws, tile + KV_KT + r32 * 8, w);
        } else {
          float o[4];
#pragma unroll
          for (int e = 0; e < 4; ++e) o[e] = (s[e] - __shfl(mu, 4 * g + e) * cs[e]) * __shfl(rs, 4 * g + e);
          const int d = th * 16 + c16;
          u32x2 w; w[0] = pack_bf16(o[0], o[1]); w[1] = pack_bf16(o[2], o[3]);
          if (d == DH_) { w[0] = 0x3f803f80u; w[1] = 0x3f803f80u; }
          if (d == DH_ + 1) { w[0] = 0u; w[1] = 0u; }
          if (d <= DH_ + 1) st8_dev(rws, tile + KV_V + (ea & 1) * KV_VS + ((g & 1) * 22 + d) * 16 + (g >> 1) * 8, w);
        }
      }
      f32x4 acc[5][2], aon[3] = {z4, z4, z4};
#pragma unroll
      for (int i = 0; i < 5; ++i) { acc[i][0] = z4; acc[i][1] = z4; }
      bf16x8 wq2[2][5];
#pragma unroll
      for (int i = 0; i < 5; ++i) wq2[0][i] = i < 4 ? lds_tr8(lds + woff + ((i * 32) ^ fsw), 512) : lds_tr8(lds + toff, 128);
#pragma unroll
      for (int s = 0; s < 5; ++s) {
        const bf16x8 a0 = __builtin_bit_cast(bf16x8, af[s][0]), a1 = __builtin_bit_cast(bf16x8, af[s][1]);
        const bf16x8* wf = wq2[s & 1];
#pragma unroll
        for (int i = 0; i < 5; ++i) {
          if (s < 4) wq2[(s + 1) & 1][i] = i < 4 ? lds_tr8(lds + woff + (s + 1) * 16384 + ((i * 32) ^ fsw), 512) : lds_tr8(lds + toff + (s + 1) * 4096, 128);
          acc[i][0] = MFMA16(wf[i], a0, acc[i][0]); acc[i][1] = MFMA16(wf[i], a1, acc[i][1]);
          SBAR();
        }
        if (th == 0) { aon[0] = MFMA16(wf[0], ones, aon[0]); aon[1] = MFMA16(wf[1], ones, aon[1]); aon[2] = MFMA16(wf[2], ones, aon[2]); }
        else { aon[0] = MFMA16(wf[3], ones, aon[0]); aon[1] = MFMA16(wf[4], ones, aon[1]); }
        if (s == 1) {
          publish(ws, fl + FL_KV + (tb * 8 + j) * 4, 32, 1024, tag, wave, lane);
          if (wave >= 4) {
            const char* w1w = (const char*)(w1 + (has4 ? ft : 0) * 64) + (long)((idx & 3) * 512 + kq * 128) * (F_ * 2);
            touch(w1w, lane * (F_ * 2), sink); touch(w1w + 64 * (F_ * 2), lane * (F_ * 2), sink);
            touch((const char*)(w2 + tb * 64) + (long)(j * 216 + kq * 54) * (S_ * 2), (lane < 54 ? lane : 53) * (S_ * 2), sink);
          }
        }
      }
      bar_lds();
#pragma unroll
      for (int i = 0; i < 5; ++i)
#pragma unroll
        for (int t = 0; t < 2; ++t) *(LAS f32x4*)(ldw + ((kq * 5 + i) * 4 + th * 2 + t) * 1024 + lane * 16) = acc[i][t];
      if (c16 == 0) {
#pragma unroll
        for (int i = 0; i < 3; ++i) if (i < ewn) *(LAS f32x4*)(csum + (kq * 7 + ew0 + i) * 16 + 4 * g) = aon[i];
      }
      bar_lds();
      {
        const float r = rs * QSCALE;
        f32x4 s[3], cs[3];
#pragma unroll
        for (int i = 0; i < 3; ++i) {
          const int wt = min(ew0 + i, 4);
          s[i] = sum4(lds, wt * 4 + ea, 20, lane);
          cs[i] = z4;
#pragma unroll
          for (int k = 0; k < 4; ++k) cs[i] += *(const LAS f32x4*)(csum + (k * 7 + wt) * 16 + 4 * g);
        }
#pragma unroll
        for (int i = 0; i < 3; ++i) {
          if (i < ewn) {
            u32x2 w;
            w[0] = pack_bf16((s[i][0] - mu * cs[i][0]) * r, (s[i][1] - mu * cs[i][1]) * r);
            w[1] = pack_bf16((s[i][2] - mu * cs[i][2]) * r, (s[i][3] - mu * cs[i][3]) * r);
            *(LAS u32x2*)(ldw + L_QT + (ea * 16 + c16) * 160 + (ew0 + i) * 32 + 8 * g) = w;
          }
        }
      }
      bar_lds();
    }
    STAMP(2);
    bf16x8 w1r[16][2];
    {
      FRESH();
      const int r32 = lane & 31, hi = lane >> 5;
      const int hh = wave & 3, trow = th * 32 + r32;
      bf16x8 q16; bf16x4 q4;
      {
        const lds_cptr qp = lds + L_QT + trow * 160 + hh * 40;
        const u32x2 a = *(const LAS u32x2*)(qp + hi * 16), b = *(const LAS u32x2*)(qp + hi * 16 + 8), c = *(const LAS u32x2*)(qp + 32);
        u32x4 v = {a[0], a[1], b[0], b[1]};
        q16 = __builtin_bit_cast(bf16x8, v);
        u32x2 t = {hi ? 0u : c[0], hi ? 0u : c[1]};
        q4 = __builtin_bit_cast(bf16x4, t);
        PIN(q16); PIN(q4);
      }
      if (wave == 0) {
        unsigned spins = 0;
        while (!dead) {
          const u32x4 v = ld16_dev(rws, fl + FL_KV + (bid & 31) * 1024 + lane * 16);
          if (__all(v[0] == tag && v[1] == tag && v[2] == tag && v[3] == tag)) break;
          if (++spins > SPIN_CAP) dead = true;
          __builtin_amdgcn_s_sleep(1);
          asm volatile("" ::: "memory");
        }
      }
      bar_lds();
      STAMP(3);
      const unsigned koff = lane * 16;
      const char* KV = ws + kvoff + wave * 3072;
      auto issue_kv = [&](int ch) {
        const unsigned dst = lds0 + L_ATT + (ch % 3) * ATT_SLOT + wave * 3072;
        if (wave < 3) glds_lin<3, true>(KV + ch * ATT_CHB, dst, koff);
        else { glds1<true>(KV + ch * ATT_CHB, dst, koff); if (lane < 32) glds1<true>(KV + ch * ATT_CHB + 1024, dst + 1024, koff); }
      };
      const char* w1s = (const char*)(w1 + (has4 ? ft : 0) * 64) + (long)wave * 16 * (F_ * 2);
      unsigned sv0, sv1;
      {
        const int r = lane >> 3, pos = lane & 7;
        const unsigned pe = (((pos >> 1) ^ ((r >> 1) & 1)) << 1 | (pos & 1)) * 16;
        sv0 = r * (F_ * 2) + pe; sv1 = (r + 8) * (F_ * 2) + (pe ^ 64) - 1024;
      }
      const unsigned fsw = (((c16 >> 3) & 1) | ((g & 1) << 1)) * 32;
      const unsigned srow = (32 * kq + 8 * g + (c16 >> 2)) * 128 + 8 * (lane & 3);
      const unsigned sr0 = srow + ((64 * th) ^ fsw), sr1 = srow + ((64 * th + 32) ^ fsw);
      if (wave < 4) { issue_kv(0); issue_kv(1); }
      glds2(w1s, lds0 + L_STG + wave * 2048, sv0, sv1);
      const unsigned ktoff = KV_KT + r32 * 8, voff = KV_V + (hi * 22 + (r32 < 21 ? r32 : 21)) * 16;
      f32x16 o = {}, negm, sA, sB;
      bf16x8 k16, v0, v1; bf16x4 k4; u32x4 p0, p1;
      if (wave < 3) wait_bar<5>(); else if (wave == 3) wait_bar<4>(); else bar_lds();
      const lds_cptr ring = lds + L_ATT;
      {
        k16 = lds_b128(ring + koff);
        k4 = *(const LAS bf16x4*)(ring + ktoff);
        f32x16 z = {};
        z = MFMA32(k16, q16, z); z = MFMA32K8(k4, q4, z);
        k16 = lds_b128(ring + KV_TILE + koff);
        k4 = *(const LAS bf16x4*)(ring + KV_TILE + ktoff);
        v1 = lds_b128(ring + voff);
        const float m = rowmax16(z);
#pragma unroll
        for (int i = 0; i < 16; ++i) { negm[i] = -m; sA[i] = z[i] - m; }
        p1 = u32x4{0u, 0u, 0u, 0u};
      }
#define EX(x) __builtin_amdgcn_exp2f(x)
#define EXP4(X, i) do { X[i] = EX(X[i]); X[i + 1] = EX(X[i + 1]); X[i + 2] = EX(X[i + 2]); X[i + 3] = EX(X[i + 3]); } while (0)
#define BF8(x) __builtin_bit_cast(bf16x8, x)
#define PK4(P, X, i) do { P[0] = pack_bf16(X[i], X[i + 1]); P[1] = pack_bf16(X[i + 2], X[i + 3]); P[2] = pack_bf16(X[i + 4], X[i + 5]); P[3] = pack_bf16(X[i + 6], X[i + 7]); } while (0)
#define STEP(C, N, kn, vc) do { \
    N = MFMA32(k16, q16, negm); SBAR(); \
    v0 = lds_b128((vc) + voff); EXP4(C, 0); PIN(C); SBAR(); \
    o = MFMA32(v1, BF8(p1), o); SBAR(); \
    k16 = lds_b128((kn) + koff); EXP4(C, 4); PIN(C); SBAR(); PK4(p0, C, 0); PIN(p0); SBAR(); \
    N = MFMA32K8(k4, q4, N); SBAR(); \
    v1 = lds_b128((vc) + voff + KV_VS); EXP4(C, 8); PIN(C); SBAR(); \
    o = MFMA32(v0, BF8(p0), o); SBAR(); \
    k4 = *(const LAS bf16x4*)((kn) + ktoff); EXP4(C, 12); PIN(C); SBAR(); PK4(p1, C, 8); PIN(p1); SBAR(); \
  } while (0)
      static_for<0, ATT_NCH>([&](auto CH) {
        constexpr int ch = decltype(CH)::v;
        const lds_cptr cb = ring + (ch % 3) * ATT_SLOT;
        const lds_cptr nb = (ch + 1 < ATT_NCH) ? ring + ((ch + 1) % 3) * ATT_SLOT : cb;
        __builtin_amdgcn_s_setprio(1);
        STEP(sA, sB, cb + 2 * KV_TILE, cb);
        __builtin_amdgcn_s_setprio(0);
        STEP(sB, sA, cb + 3 * KV_TILE, cb + KV_TILE);
        if (wave < 4) wait_vbar<(ch >= 1 && ch <= 5) ? 4 : 0>(); else wait_vbar<0>();
        if (ch + 2 < ATT_NCH) { if (wave < 4) issue_kv(ch + 2); }
        if (ch + 1 < 16) glds2(w1s + (long)(ch + 1) * 128 * (F_ * 2), lds0 + ((ch & 1) ? L_STG : L_WT) + wave * 2048, sv0, sv1);
        if (ch < 5) { if (wave < 4) wslice_block(wo + j * 80, lds0, wave, lane, ch); }
        w1r[ch][0] = lds_tr8(lds + ((ch & 1) ? L_WT : L_STG) + sr0, 512); w1r[ch][1] = lds_tr8(lds + ((ch & 1) ? L_WT : L_STG) + sr1, 512);
        __builtin_amdgcn_s_setprio(3);
        SBAR();
        STEP(sA, sB, nb, cb + 2 * KV_TILE);
        PIN(w1r[ch][0]); PIN(w1r[ch][1]);
        __builtin_amdgcn_s_setprio(2);
        SBAR();
        STEP(sB, sA, nb + KV_TILE, cb + 3 * KV_TILE);
      });
      __builtin_amdgcn_s_setprio(0);
      o = MFMA32(v1, BF8(p1), o);
      float l = xsum32(hi ? o[8] : 0.f);
      bool bad = !(l < 1.8446744e19f);
#pragma unroll
      for (int i = 0; i < 12; ++i) bad |= !(__builtin_fabsf(o[i]) < 3.0e38f);
      if (__builtin_expect(__any(bad), 0)) {
        float m = -INFINITY;
        o = f32x16{};
#pragma unroll 1
        for (int t = 0; t < S_ / 32; ++t) {
          const unsigned tp = kvoff + t * KV_TILE;
          const bf16x8 a16 = BF8(ld16_dev(rws, tp + koff));
          const u32x2 a4 = __builtin_amdgcn_raw_buffer_load_b64(rws, tp + ktoff, 0, 16);
          const bf16x8 w0 = BF8(ld16_dev(rws, tp + voff)), w1f = BF8(ld16_dev(rws, tp + voff + KV_VS));
          f32x16 z = {};
          z = MFMA32(a16, q16, z); z = MFMA32K8(__builtin_bit_cast(bf16x4, a4), q4, z);
          const float mn = __builtin_fmaxf(m, rowmax16(z));
          const float alpha = __builtin_amdgcn_exp2f(m - mn);
          m = mn;
#pragma unroll
          for (int i = 0; i < 16; ++i) { z[i] = __builtin_amdgcn_exp2f(z[i] - m); o[i] *= alpha; }
          bf16x8 a0, a1; pack_p(z, a0, a1);
          o = MFMA32(w0, a0, o); o = MFMA32(w1f, a1, o);
        }
        l = xsum32(hi ? o[8] : 0.f);
      }
      const float rl = __builtin_amdgcn_rcpf(l);
      const unsigned ob = WS_O + layer * XB + tb * 81920 + ((trow >> 4) * 64 + (trow & 15)) * 16;
#pragma unroll
      for (int gq = 0; gq < 3; ++gq) {
        const int d0 = 8 * gq + 4 * hi;
        if (d0 < DH_) {
          const int k = (j * 4 + hh) * DH_ + d0;
          u32x2 w; w[0] = pack_bf16(o[4 * gq] * rl, o[4 * gq + 1] * rl); w[1] = pack_bf16(o[4 * gq + 2] * rl, o[4 * gq + 3] * rl);
          st8_dev(rws, ob + (k >> 5) * 4096 + ((k & 31) >> 3) * 256 + (k & 7) * 2, w);
        }
      }
      STAMP(4);
      publish(ws, fl + FL_O + tb * 128 + j * 4, 1, 0, tag, wave, lane);
      asm volatile("" :: "v"(sink));
      if (wave >= 4) { wslice_block(wo + j * 80, lds0, kq, lane, 5); wslice_block(wo + j * 80, lds0, kq, lane, 6); }
    }
    {
      FRESH();
      if (wave == 0) poll_flags(ws, fl + FL_O + tb * 128, 8, tag, lane, dead);
      wait_vbar<0>();
      STAMP(5);
      const unsigned abase = WS_O + layer * XB + tb * 81920 + (kq * 4 + th * 2) * 1024 + lane * 16;
      u32x4 af[5][2];
#pragma unroll
      for (int s = 0; s < 5; ++s)
#pragma unroll
        for (int t = 0; t < 2; ++t) af[s][t] = ld16_dev_s(rws, abase + t * 1024, s * 16384);
      f32x4 acc[5][2];
#pragma unroll
      for (int i = 0; i < 5; ++i) { acc[i][0] = f32x4{0.f, 0.f, 0.f, 0.f}; acc[i][1] = acc[i][0]; }
      const int q = c16 >> 2, p = lane & 3;
      const int row = 32 * kq + 8 * g + q;
      const unsigned fsw = (((q >> 1) & 1) | ((g & 1) << 1)) * 32;
      const unsigned woff = L_W + row * 128 + 8 * p, toff = L_WT + row * 32 + 8 * p;
      bf16x8 wf[2][5];
#pragma unroll
      for (int i = 0; i < 5; ++i) wf[0][i] = i < 4 ? lds_tr8(lds + woff + ((i * 32) ^ fsw), 512) : lds_tr8(lds + toff, 128);
#pragma unroll
      for (int s = 0; s < 5; ++s) {
        const bf16x8 a0 = __builtin_bit_cast(bf16x8, af[s][0]), a1 = __builtin_bit_cast(bf16x8, af[s][1]);
#pragma unroll
        for (int i = 0; i < 5; ++i) {
          if (s < 4) wf[(s + 1) & 1][i] = i < 4 ? lds_tr8(lds + woff + (s + 1) * 16384 + ((i * 32) ^ fsw), 512) : lds_tr8(lds + toff + (s + 1) * 4096, 128);
          acc[i][0] = MFMA16(wf[s & 1][i], a0, acc[i][0]); acc[i][1] = MFMA16(wf[s & 1][i], a1, acc[i][1]);
          SBAR();
        }
      }
      bar_lds();
#pragma unroll
      for (int i = 0; i < 5; ++i)
#pragma unroll
        for (int t = 0; t < 2; ++t) *(LAS f32x4*)(ldw + ((kq * 5 + i) * 4 + th * 2 + t) * 1024 + lane * 16) = acc[i][t];
      bar_lds();
      gu64* gr = (gu64*)(ws + WS_STAT + layer * ST_LAYER + (tb * 4 + ea) * 4096);
      {
        f32x4 s[3]; u32x2 r[3];
#pragma unroll
        for (int i = 0; i < 3; ++i) {
          const int wt = min(ew0 + i, 4);
          s[i] = sum4(lds, wt * 4 + ea, 20, lane);
          r[i] = *(const LAS u32x2*)(lds + res_off + wt * 32);
        }
        float s1 = 0.f, s2 = 0.f;
#pragma unroll
        for (int i = 0; i < 3; ++i) {
          if (i < ewn) {
            u32x2 w;
            w[0] = pack_bf16(bf_lo(r[i][0]) + s[i][0], bf_hi(r[i][0]) + s[i][1]);
            w[1] = pack_bf16(bf_lo(r[i][1]) + s[i][2], bf_hi(r[i][1]) + s[i][3]);
            *(LAS u32x2*)(ldw + res_off + (ew0 + i) * 32) = w;
            const float a = bf_lo(w[0]), b = bf_hi(w[0]), c = bf_lo(w[1]), d = bf_hi(w[1]);
            s1 += (a + b) + (c + d);
            s2 += (a * a + b * b) + (c * c + d * d);
          }
        }
        s1 = xsum32(xsum16(s1)); s2 = xsum32(xsum16(s2));
        if (lane < 32) __hip_atomic_store(gr + (2 * j + th) * 32 + lane, ((unsigned long long)tag << 32) | __float_as_uint(g ? s2 : s1), RLX_AGENT);
      }
      bar_lds();
      if (wave >= 4) {
        const int r = lane >> 3, pos = lane & 7;
        const unsigned pe = (((pos >> 1) ^ ((r >> 1) & 1)) << 1 | (pos & 1)) * 16;
        const unsigned v0 = r * (S_ * 2) + pe + 4096, v1 = (r + 8) * (S_ * 2) + (pe ^ 64) + 3072, vstep = 16 * (S_ * 2) - 2048;
        const char* wsrc = (const char*)(w2 + tb * 64) - 4096 + (long)kq * 32 * (S_ * 2);
#pragma unroll 1
        for (int st = 0; st < 4; ++st) glds4<false>(wsrc + (long)st * 128 * (S_ * 2), lds0 + L_RING + st * 16384 + kq * 4096, v0, v1, v0 + vstep, v1 + vstep);
        asm volatile("s_barrier" ::: "memory");
      } else {
        u32x2 xv[5];
#pragma unroll
        for (int wt = 0; wt < 5; ++wt) xv[wt] = __builtin_bit_cast(u32x2, lds_tr(lds + L_RES + (ea * 16 + 4 * g + q) * 160 + wt * 32 + 8 * p));
        float tot;
        {
          unsigned spins = 0;
          for (;;) {
            bool ok = true; tot = 0.f;
#pragma unroll
            for (int i = 0; i < 8; ++i) {
              const unsigned long long v = __hip_atomic_load(gr + i * 64 + lane, RLX_AGENT);
              tot += __uint_as_float((unsigned)v); ok &= (unsigned)(v >> 32) == tag;
            }
            if (__all(ok) || dead) break;
            if (++spins > SPIN_CAP) dead = true;
            __builtin_amdgcn_s_sleep(1);
          }
        }
        STAMP(6);
        tot = xsum32(tot);
        float mu4[4], rr4[4], m24[4], r24[4];
#pragma unroll
        for (int e = 0; e < 4; ++e) { mu4[e] = __shfl(tot, 4 * g + e); rr4[e] = __shfl(tot, 16 + 4 * g + e); }
#pragma unroll
        for (int e = 0; e < 4; ++e) {
          float mu, var; ln_finish(mu4[e], rr4[e], mu, var);
          mu4[e] = rnd1(mu); rr4[e] = ln_rsd(var);
          m24[e] = rnd1((mu - mu4[e]) * rr4[e]); r24[e] = ln_rsd(var * rr4[e] * rr4[e]);
        }
        const unsigned hb = WS_H + layer * XB + j * 327680 + ((tb * 2 + (ea >> 1)) * 5) * 1024 + (c16 + 16 * ((ea & 1) * 2 + (g >> 1))) * 16 + (g & 1) * 8;
#pragma unroll
        for (int wt = 0; wt < 5; ++wt) {
          const u32x2 v = xv[wt];
          u32x2 w;
          w[0] = pack_bf16(bf_lo(v[0]) - mu4[0], bf_hi(v[0]) - mu4[1]); w[1] = pack_bf16(bf_lo(v[1]) - mu4[2], bf_hi(v[1]) - mu4[3]);
          w[0] = pack_bf16(bf_lo(w[0]) * rr4[0], bf_hi(w[0]) * rr4[1]); w[1] = pack_bf16(bf_lo(w[1]) * rr4[2], bf_hi(w[1]) * rr4[3]);
          w[0] = pack_bf16(bf_lo(w[0]) - m24[0], bf_hi(w[0]) - m24[1]); w[1] = pack_bf16(bf_lo(w[1]) - m24[2], bf_hi(w[1]) - m24[3]);
          w[0] = pack_bf16(bf_lo(w[0]) * r24[0], bf_hi(w[0]) * r24[1]); w[1] = pack_bf16(bf_lo(w[1]) * r24[2], bf_hi(w[1]) * r24[3]);
          st8_dev(rws, hb + wt * 1024, w);
        }
        publish(ws, fl + FL_H + j * 512 + tb * 4, 4, 128, tag, wave, lane);
      }
      STAMP(7);
    }
    {
      FRESH();
      if (has4) {
        const int f = ft * 64 + ea * 16 + 4 * g;
        const u32x2 bias = *(const u32x2*)(b1 + f);
        if (wave == 0) poll_flags(ws, fl + FL_H + panel4 * 512 + ((idx >> 2) & 3) * 128, 32, tag, lane, dead);
        asm volatile("s_barrier" ::: "memory");
        STAMP(8);
        const unsigned l16 = lane * 16;
        const int po = (th ? 12 + 2 * kq : 3 * kq) * 1024;
        const char* hsrc = ws + WS_H + layer * XB + panel4 * 327680 + po;
        auto issue = [&](int st) {
          if (th == 0) glds_lin<3, true>(hsrc + st * 20480, lds0 + (st & 3) * 20480 + po, l16);
          else glds_lin<2, true>(hsrc + st * 20480, lds0 + (st & 3) * 20480 + po, l16);
        };
        issue(0); issue(1); issue(2); issue(3);
        f32x4 acc[2][5];
#pragma unroll
        for (int i = 0; i < 2; ++i)
#pragma unroll
          for (int a = 0; a < 5; ++a) acc[i][a] = f32x4{0.f, 0.f, 0.f, 0.f};
        const lds_cptr ab = lds + kq * 5120 + l16;
        bf16x8 af[2][5];
        if (th == 0) wait_vbar<9>(); else wait_vbar<6>();
#pragma unroll
        for (int a = 0; a < 5; ++a) af[0][a] = lds_b128(ab + a * 1024);
        static_for<0, 16>([&](auto ST) {
          constexpr int st = decltype(ST)::v;
          if constexpr (st < 15) {
            constexpr int ahead = (14 - st) < 2 ? (14 - st) : 2;
            if (th == 0) wait_bar<3 * ahead>(); else wait_bar<2 * ahead>();
#pragma unroll
            for (int a = 0; a < 5; ++a) af[(st + 1) & 1][a] = lds_b128(ab + ((st + 1) & 3) * 20480 + a * 1024);
          }
          if (st + 4 < 16) { if (th == 0) issue(st + 4); }
          SBAR();
#pragma unroll
          for (int a = 0; a < 5; ++a) { acc[0][a] = MFMA16(w1r[st][0], af[st & 1][a], acc[0][a]); acc[1][a] = MFMA16(w1r[st][1], af[st & 1][a], acc[1][a]); }
          SBAR();
          if (st + 4 < 16) { if (th == 1) issue(st + 4); }
        });
        bar_lds();
#pragma unroll
        for (int i = 0; i < 2; ++i)
#pragma unroll
          for (int a = 0; a < 5; ++a) *(LAS f32x4*)(ldw + ((kq * 4 + 2 * th + i) * 5 + a) * 1024 + l16) = acc[i][a];
        bar_lds();
        STAMP(9);
        const unsigned ub = WS_U + layer * UB + ((panel4 * U_KS + (f >> 5)) * 5) * 1024 + (c16 + 16 * ((f & 31) >> 3)) * 16 + (f & 7) * 2;
        f32x4 sv[3];
#pragma unroll
        for (int i = 0; i < 3; ++i) sv[i] = sum4(lds, ea * 5 + min(ew0 + i, 4), 20, lane);
#pragma unroll
        for (int i = 0; i < 3; ++i) {
          if (i < ewn) {
            const f32x4 s = sv[i];
            u32x2 w;
            w[0] = pack_bf16(gelu_tanh(s[0] + bf_lo(bias[0])), gelu_tanh(s[1] + bf_hi(bias[0])));
            w[1] = pack_bf16(gelu_tanh(s[2] + bf_lo(bias[1])), gelu_tanh(s[3] + bf_hi(bias[1])));
            st8_dev(rws, ub + (ew0 + i) * 1024, w);
          }
        }
        publish(ws, fl + FL_U + panel4 * 512 + ft * 4, 4, 128, tag, wave, lane);
      }
    }
    STAMP(10);
    {
      FRESH();
      const int tok = tb * 64 + ea * 16 + c16;
      const float bias = __uint_as_float((unsigned)b2[tok] << 16);
      if (wave >= 4) {
        const int r = lane >> 3, pos = lane & 7;
        const unsigned pe = (((pos >> 1) ^ ((r >> 1) & 1)) << 1 | (pos & 1)) * 16;
        const unsigned v0 = r * (S_ * 2) + pe + 4096, v1 = (r + 8) * (S_ * 2) + (pe ^ 64) + 3072, vstep = 16 * (S_ * 2) - 2048;
        const char* wsrc = (const char*)(w2 + tb * 64) - 4096 + (long)kq * 32 * (S_ * 2);
#pragma unroll 1
        for (int st = 4; st < 9; ++st) glds4<false>(wsrc + (long)st * 128 * (S_ * 2), lds0 + (st - 4) * 16384 + kq * 4096, v0, v1, v0 + vstep, v1 + vstep);
        wait_vbar<32>();
        static_for<0, 13>([&](auto ST) {
          constexpr int st = decltype(ST)::v;
          constexpr int last = (st + 8 > 13) ? 13 : st + 8;
          wait_vbar<4 * (last - st - 1)>();
          if (st + 9 < 14) {
            const long o = (st + 9 == 13 && kq >= 2) ? (long)(2 * 13 - 1) * 64 * (S_ * 2) : (long)(st + 9) * 128 * (S_ * 2);
            glds4<false>(wsrc + o, lds0 + w2_slot(st + 9) + kq * 4096, v0, v1, v0 + vstep, v1 + vstep);
          }
        });
        asm volatile("s_barrier" ::: "memory");
      } else {
        if (wave == 0) poll_flags(ws, fl + FL_U + j * 512 + (tb & 3) * 128, 27, tag, lane, dead);
        asm volatile("s_barrier" ::: "memory");
        STAMP(11);
        const unsigned abase = WS_U + layer * UB + j * (U_KS * 5 * 1024) + kq * 5120 + lane * 16;
        const int q = c16 >> 2, p = lane & 3;
        const unsigned fsw = (((q >> 1) & 1) | ((g & 1) << 1)) * 32;
        const lds_cptr wrow = lds + (32 * kq + 8 * g + q) * 128 + 8 * p;
        u32x4 af[3][5];
#pragma unroll
        for (int st = 0; st < 3; ++st)
#pragma unroll
          for (int a = 0; a < 5; ++a) af[st][a] = ld16_dev_s(rws, abase + (a & 3) * 1024, st * 20480 + (a >> 2) * 4096);
        f32x4 acc[4][5];
#pragma unroll
        for (int i = 0; i < 4; ++i)
#pragma unroll
          for (int a = 0; a < 5; ++a) acc[i][a] = f32x4{0.f, 0.f, 0.f, 0.f};
        bf16x8 wf[2][4];
#pragma unroll
        for (int i = 0; i < 4; ++i) wf[0][i] = lds_tr8(wrow + w2_slot(0) + ((i * 32) ^ fsw), 512);
        static_for<0, 14>([&](auto ST) {
          constexpr int st = decltype(ST)::v;
          if constexpr (st < 13) {
            bar_lds();
#pragma unroll
            for (int i = 0; i < 4; ++i) wf[(st + 1) & 1][i] = lds_tr8(wrow + w2_slot(st + 1) + ((i * 32) ^ fsw), 512);
          }
          if (st < 13 || kq < 2) {
            SBAR();
#pragma unroll
            for (int a = 0; a < 5; ++a) {
              const bf16x8 x = __builtin_bit_cast(bf16x8, af[st % 3][a]);
#pragma unroll
              for (int i = 0; i < 4; ++i) acc[i][a] = MFMA16(x, wf[st & 1][i], acc[i][a]);
              SBAR();
              if (st + 3 < 14) af[st % 3][a] = ld16_dev_s(rws, abase + (a & 3) * 1024, (st + 3) * 20480 + (a >> 2) * 4096);
              SBAR();
            }
          }
        });
        bar_lds();
#pragma unroll
        for (int i = 0; i < 4; ++i)
#pragma unroll
          for (int a = 0; a < 5; ++a) *(LAS f32x4*)(ldw + ((kq * 4 + i) * 5 + a) * 1024 + lane * 16) = acc[i][a];
      }
      bar_lds();
      STAMP(12);
      f32x4 sv[3]; u32x2 rv[3];
#pragma unroll
      for (int i = 0; i < 3; ++i) { const int at = min(ew0 + i, 4); sv[i] = sum4(lds, ea * 5 + at, 20, lane); rv[i] = *(const LAS u32x2*)(lds + res_off + at * 32); }
#pragma unroll
      for (int i = 0; i < 3; ++i) {
        if (i < ewn) {
          const int at = ew0 + i;
          const f32x4 s = sv[i];
          const u32x2 r = rv[i];
          u32x2 w;
          w[0] = pack_bf16(bf_lo(r[0]) + (s[0] + bias), bf_hi(r[0]) + (s[1] + bias));
          w[1] = pack_bf16(bf_lo(r[1]) + (s[2] + bias), bf_hi(r[1]) + (s[3] + bias));
          const int d = j * 80 + at * 16 + 4 * g;
          if (layer == 0) {
            *(LAS u32x2*)(ldw + res_off + at * 32) = w;
            st8_dev(rws, WS_X + tb * 81920 + (((d >> 5) * 4 + ea) * 64 + c16 + 16 * ((d & 31) >> 3)) * 16 + (d & 7) * 2, w);
          } else {
            *(u32x2*)(out + (long)tok * D_ + d) = w;
          }
        }
      }
      if (layer == 0) publish(ws, WS_FLAGS + FL_X + tb * 128 + j * 4, 1, 0, tag, wave, lane);
      STAMP(13);
    }
  }
  if (blockIdx.x == 0 && tid == 0) __hip_atomic_store((gu32*)(ws + WS_EPOCH), tag, RLX_AGENT);
}

extern "C" void kernel_launch(void* const* d_in, const int* in_sizes, int n_in, void* d_out, int out_size, void* d_ws,
                              size_t ws_size, hipStream_t stream) {
  (void)hipFuncSetAttribute((const void*)mega_kernel, hipFuncAttributeMaxDynamicSharedMemorySize, LDS_BYTES);
  mega_kernel<<<256, 512, LDS_BYTES, stream>>>((const u16*)d_in[0], (const u16*)d_in[1], (const u16*)d_in[2], (const u16*)d_in[3],
                                               (const u16*)d_in[4], (const u16*)d_in[6], (const u16*)d_in[7], (const u16*)d_in[8],
                                               (const u16*)d_in[9], (u16*)d_out, (char*)d_ws);
}
```

```cpp
#include <hip/hip_runtime.h>
#include <hip/hip_bf16.h>
#include <stdint.h>

typedef __attribute__((ext_vector_type(8))) short bf16x8;
typedef __attribute__((ext_vector_type(4))) short bf16x4;
typedef __attribute__((ext_vector_type(16))) float f32x16;
typedef __attribute__((ext_vector_type(4))) float f32x4;
typedef __attribute__((ext_vector_type(2))) float f32x2;
typedef __attribute__((ext_vector_type(2))) __bf16 bf2_t;
typedef __attribute__((ext_vector_type(4))) unsigned u32x4;
typedef __attribute__((ext_vector_type(2))) unsigned u32x2;
typedef unsigned short u16;
#define LAS __attribute__((address_space(3)))
#define GAS __attribute__((address_space(1)))
typedef LAS const char* lds_cptr;
typedef LAS char* lds_ptr;
typedef GAS unsigned gu32;
typedef GAS unsigned long long gu64;
#define DI __device__ __forceinline__
#define RLX_AGENT __ATOMIC_RELAXED, __HIP_MEMORY_SCOPE_AGENT

constexpr int S_ = 2048, D_ = 640, DH_ = 20, F_ = 1728;
constexpr float LN_EPS = 1e-5f;
constexpr float QSCALE = 0.22360679774997896f * 1.4426950408889634f;

constexpr int KV_TILE = 2688, KV_KT = 1024, KV_V = 1280, KV_VS = 704;

constexpr unsigned WS_EPOCH = 0;
constexpr unsigned WS_FLAGS = 4096, FL_LAYER = 65536;
constexpr unsigned FL_KV = 0;
constexpr unsigned FL_O = 32768;
constexpr unsigned FL_X = 36864;
constexpr unsigned FL_H = 40960;
constexpr unsigned FL_U = 45056;
constexpr unsigned WS_DIAG = 262144;
constexpr unsigned WS_STAT = 524288, ST_LAYER = 524288;
constexpr unsigned WS_KV = WS_STAT + 2 * ST_LAYER, KV_LAYER = 196608;
constexpr unsigned XB = S_ * D_ * 2;
constexpr unsigned WS_O = WS_KV + 2 * KV_LAYER;
constexpr unsigned WS_H = WS_O + 2 * XB;
constexpr int U_KS = 56;
constexpr unsigned UB = 8 * U_KS * 5 * 1024;
constexpr unsigned WS_U = WS_H + 2 * XB;
constexpr unsigned WS_X = WS_U + 2 * UB;
constexpr unsigned WS_END = WS_X + XB;

constexpr int L_W = 0;
constexpr int L_WT = 81920;
constexpr int L_RING = 81920;
constexpr int L_ATT = 102400;
constexpr int L_QT = 81920;
constexpr int L_KVP = 128064;
constexpr int L_STG = 134656;
constexpr int L_RES = 151040;
constexpr int L_DUMP = 161280;
constexpr int LDS_BYTES = 163840;
constexpr unsigned SPIN_CAP = 1u << 21;

DI unsigned pack_bf16(float lo, float hi) {
  f32x2 v = {lo, hi};
  bf2_t b = __builtin_convertvector(v, bf2_t);
  return __builtin_bit_cast(unsigned, b);
}
DI float bf_lo(unsigned u) { return __uint_as_float(u << 16); }
DI float bf_hi(unsigned u) { return __uint_as_float(u & 0xffff0000u); }
DI float rnd1(float v) { return bf_lo(pack_bf16(v, 0.f)); }
DI float ln_rsd(float var) { return __builtin_amdgcn_rcpf(rnd1(__builtin_amdgcn_sqrtf(rnd1(rnd1(var) + LN_EPS)))); }

#define GLDS_HEAD "s_mov_b32 m0, %1\n\ts_nop 4\n\t"
template <bool DEV> DI void glds1(const void* sbase, unsigned m0v, unsigned v0) {
  if (DEV) asm volatile(GLDS_HEAD "global_load_lds_dwordx4 %2, %0 sc1" ::"s"(sbase), "s"(m0v), "v"(v0) : "memory");
  else asm volatile(GLDS_HEAD "global_load_lds_dwordx4 %2, %0" ::"s"(sbase), "s"(m0v), "v"(v0) : "memory");
}
template <bool DEV> DI void glds4(const void* sbase, unsigned m0v, unsigned v0, unsigned v1, unsigned v2, unsigned v3) {
  if (DEV)
    asm volatile(GLDS_HEAD "global_load_lds_dwordx4 %2, %0 sc1\n\tglobal_load_lds_dwordx4 %3, %0 offset:1024 sc1\n\t"
                           "global_load_lds_dwordx4 %4, %0 offset:2048 sc1\n\tglobal_load_lds_dwordx4 %5, %0 offset:3072 sc1"
                 ::"s"(sbase), "s"(m0v), "v"(v0), "v"(v1), "v"(v2), "v"(v3) : "memory");
  else
    asm volatile(GLDS_HEAD "global_load_lds_dwordx4 %2, %0\n\tglobal_load_lds_dwordx4 %3, %0 offset:1024\n\t"
                           "global_load_lds_dwordx4 %4, %0 offset:2048\n\tglobal_load_lds_dwordx4 %5, %0 offset:3072"
                 ::"s"(sbase), "s"(m0v), "v"(v0), "v"(v1), "v"(v2), "v"(v3) : "memory");
}
template <int N, bool DEV> DI void glds_lin(const void* sbase, unsigned m0v, unsigned v) {
  if (N == 1) glds1<DEV>(sbase, m0v, v);
  else if (N == 4) glds4<DEV>(sbase, m0v, v, v, v, v);
  else if (N == 2) {
    if (DEV) asm volatile(GLDS_HEAD "global_load_lds_dwordx4 %2, %0 sc1\n\tglobal_load_lds_dwordx4 %2, %0 offset:1024 sc1" ::"s"(sbase), "s"(m0v), "v"(v) : "memory");
    else asm volatile(GLDS_HEAD "global_load_lds_dwordx4 %2, %0\n\tglobal_load_lds_dwordx4 %2, %0 offset:1024" ::"s"(sbase), "s"(m0v), "v"(v) : "memory");
  } else {
    if (DEV) asm volatile(GLDS_HEAD "global_load_lds_dwordx4 %2, %0 sc1\n\tglobal_load_lds_dwordx4 %2, %0 offset:1024 sc1\n\tglobal_load_lds_dwordx4 %2, %0 offset:2048 sc1" ::"s"(sbase), "s"(m0v), "v"(v) : "memory");
    else asm volatile(GLDS_HEAD "global_load_lds_dwordx4 %2, %0\n\tglobal_load_lds_dwordx4 %2, %0 offset:1024\n\tglobal_load_lds_dwordx4 %2, %0 offset:2048" ::"s"(sbase), "s"(m0v), "v"(v) : "memory");
  }
}
DI void glds2(const void* sbase, unsigned m0v, unsigned v0, unsigned v1) {
  asm volatile(GLDS_HEAD "global_load_lds_dwordx4 %2, %0\n\tglobal_load_lds_dwordx4 %3, %0 offset:1024" ::"s"(sbase), "s"(m0v), "v"(v0), "v"(v1) : "memory");
}
#define UNI(x) __builtin_amdgcn_readfirstlane(x)
template <int N> DI void wait_bar() { asm volatile("s_waitcnt vmcnt(%0) lgkmcnt(0)\n\ts_barrier" ::"i"(N) : "memory"); }
template <int N> DI void wait_vbar() { asm volatile("s_waitcnt vmcnt(%0)\n\ts_barrier" ::"i"(N) : "memory"); }
DI void bar_lds() { asm volatile("s_waitcnt lgkmcnt(0)\n\ts_barrier" ::: "memory"); }
typedef short v4i16_t __attribute__((ext_vector_type(4)));
DI bf16x4 lds_tr(lds_cptr p) {
  return __builtin_bit_cast(bf16x4, __builtin_amdgcn_ds_read_tr16_b64_v4i16((LAS v4i16_t*)p));
}
DI bf16x8 lds_tr8(lds_cptr p, int step) {
  const bf16x4 lo = lds_tr(p), hi = lds_tr(p + step);
  return __builtin_shufflevector(lo, hi, 0, 1, 2, 3, 4, 5, 6, 7);
}
DI bf16x8 lds_b128(lds_cptr p) { return *(const LAS bf16x8*)p; }
#define MFMA16(a, b, c) __builtin_amdgcn_mfma_f32_16x16x32_bf16((a), (b), (c), 0, 0, 0)
#define MFMA32(a, b, c) __builtin_amdgcn_mfma_f32_32x32x16_bf16((a), (b), (c), 0, 0, 0)
#define MFMA32K8(a, b, c) __builtin_amdgcn_mfma_f32_32x32x8bf16_1k((a), (b), (c), 0, 0, 0)
#define SBAR() __builtin_amdgcn_sched_barrier(0)
#define PIN(x) asm volatile("" : "+v"(x))
typedef __amdgpu_buffer_rsrc_t rsrc_t;
DI void st8_dev(rsrc_t r, unsigned off, u32x2 v) { __builtin_amdgcn_raw_buffer_store_b64(v, r, off, 0, 16); }
DI u32x4 ld16_dev(rsrc_t r, unsigned off) { return __builtin_amdgcn_raw_buffer_load_b128(r, off, 0, 16); }
DI u32x4 ld16_dev_s(rsrc_t r, unsigned off, unsigned soff) { return __builtin_amdgcn_raw_buffer_load_b128(r, off, soff, 16); }

DI void poll_flags(const char* ws, unsigned off, int n, unsigned tag, int lane, bool& dead) {
  gu32* f = (gu32*)(ws + off);
  unsigned spins = 0;
  while (!dead) {
    unsigned v = tag;
    if (lane < n) v = __hip_atomic_load(f + lane, RLX_AGENT);
    if (__all(v == tag)) break;
    if (++spins > SPIN_CAP) dead = true;
    __builtin_amdgcn_s_sleep(1);
  }
  asm volatile("" ::: "memory");
}
DI void publish(const char* ws, unsigned off, int nrep, unsigned rstride, unsigned tag, int wave, int lane) {
  wait_bar<0>();
  if (wave == 0 && lane < nrep) __hip_atomic_store((gu32*)(ws + off + lane * rstride), tag, RLX_AGENT);
}

DI float fmax3(float a, float b, float c) { return __builtin_fmaxf(__builtin_fmaxf(a, b), c); }
DI float rowmax16(const f32x16& s) {
  float a = fmax3(s[0], s[1], s[2]), b = fmax3(s[3], s[4], s[5]);
  a = fmax3(a, s[6], s[7]); b = fmax3(b, s[8], s[9]); a = fmax3(a, s[10], s[11]); b = fmax3(b, s[12], s[13]);
  a = fmax3(a, s[14], s[15]); a = __builtin_fmaxf(a, b);
  return __builtin_fmaxf(a, __shfl_xor(a, 32));
}
DI void pack_p(const f32x16& p, bf16x8& lo, bf16x8& hi) {
  u32x4 a, b;
#pragma unroll
  for (int i = 0; i < 4; ++i) { a[i] = pack_bf16(p[2 * i], p[2 * i + 1]); b[i] = pack_bf16(p[8 + 2 * i], p[9 + 2 * i]); }
  lo = __builtin_bit_cast(bf16x8, a); hi = __builtin_bit_cast(bf16x8, b);
}
DI float gelu_tanh(float u) {
  const float z = 0.7978845608028654f * (u + 0.044715f * u * u * u);
  const float e = __builtin_amdgcn_exp2f(-2.885390081777927f * z);
  return u * __builtin_amdgcn_rcpf(1.f + e);
}

DI void wslice_block(const u16* w, unsigned lds0, int lw, int lane, int blk) {
  const char* src = (const char*)w + lw * (160 * 1280);
  if (blk < 5) {
    const int r = lane >> 3, pos = lane & 7;
    const unsigned pe = (((pos >> 1) ^ ((r >> 1) & 1)) << 1 | (pos & 1)) * 16;
    const unsigned v0 = r * 1280 + pe, v1 = r * 1280 + (pe ^ 64) + 9216;
    glds4<false>(src + blk * (32 * 1280), lds0 + L_W + lw * 20480 + blk * 4096, v0, v1, v0 + 18432, v1 + 18432);
  } else {
    const unsigned t0 = (lane >> 1) * 1280 + 128 + (lane & 1) * 16;
    if (blk == 5) glds4<false>(src, lds0 + L_WT + lw * 5120, t0, t0 + 39936, t0 + 2 * 39936, t0 + 3 * 39936);
    else glds1<false>(src + 128 * 1280, lds0 + L_WT + lw * 5120 + 4096, t0);
  }
}

DI void touch(const void* sbase, unsigned voff, unsigned& sink) {
  asm volatile("s_nop 4\n\tglobal_load_dword %0, %1, %2" : "+v"(sink) : "v"(voff), "s"(sbase) : "memory");
}
DI float xsum16(float x) {
  auto r = __builtin_amdgcn_permlane16_swap(__float_as_uint(x), __float_as_uint(x), false, false);
  return __uint_as_float(r[0]) + __uint_as_float(r[1]);
}
DI float xsum32(float x) {
  auto r = __builtin_amdgcn_permlane32_swap(__float_as_uint(x), __float_as_uint(x), false, false);
  return __uint_as_float(r[0]) + __uint_as_float(r[1]);
}
DI f32x4 ldf4(lds_cptr p) { return *(const LAS f32x4*)p; }

DI f32x4 sum4(lds_cptr lds, int t, int nt, int lane) {
  f32x4 s = *(const LAS f32x4*)(lds + t * 1024 + lane * 16);
#pragma unroll
  for (int k = 1; k < 4; ++k) s += *(const LAS f32x4*)(lds + (k * nt + t) * 1024 + lane * 16);
  return s;
}
DI void ln_finish(float s1, float s2, float& mu, float& var) { mu = s1 * (1.f / D_); var = fmaxf(s2 * (1.f / D_) - mu * mu, 0.f); }

constexpr int ATT_CH = 4;
constexpr int ATT_CHB = ATT_CH * KV_TILE;
constexpr int ATT_SLOT = 10752;
constexpr int ATT_NCH = S_ / 32 / ATT_CH;
constexpr int w2_slot(int st) { return (st % 9) < 4 ? L_RING + (st % 9) * 16384 : ((st % 9) - 4) * 16384; }
template <int N> struct IC { static constexpr int v = N; };
template <int I, int N, class F> DI void static_for(F&& f) { if constexpr (I < N) { f(IC<I>{}); static_for<I + 1, N>(f); } }

#define STAMP(i) do { } while (0)

__global__ __launch_bounds__(512, 2) void mega_kernel(const u16* __restrict__ x0, const u16* __restrict__ wq, const u16* __restrict__ wk,
                                                      const u16* __restrict__ wv, const u16* __restrict__ wo, const u16* __restrict__ w1,
                                                      const u16* __restrict__ b1, const u16* __restrict__ w2, const u16* __restrict__ b2,
                                                      u16* __restrict__ out, char* ws) {
  extern __shared__ __attribute__((aligned(16))) char smem[];
  const int tid = threadIdx.x;
  const unsigned lds0 = (unsigned)(uintptr_t)smem;
  const lds_cptr lds = (lds_cptr)smem;
  const lds_ptr ldw = (lds_ptr)smem;
#define FRESH() \
  int lane = tid & 63; asm volatile("" : "+v"(lane)); \
  int wave = UNI(tid >> 6); asm volatile("" : "+s"(wave)); \
  int bid = blockIdx.x; asm volatile("" : "+s"(bid)); \
  const int xcd = bid & 7, idx = bid >> 3, tb = xcd * 4 + (idx >> 3), j = idx & 7; \
  const int g = lane >> 4, c16 = lane & 15, kq = wave & 3, th = wave >> 2; \
  const int ea = wave & 3, ew0 = th ? 3 : 0, ewn = th ? 2 : 3; \
  const unsigned res_off = L_RES + (ea * 16 + c16) * 160 + 8 * g; \
  const int panel4 = idx & 7, ft = (xcd < 3 ? 4 * xcd : 3 + 3 * xcd) + (idx >> 3); \
  const bool has4 = (idx >> 3) < (xcd < 3 ? 4 : 3); \
  (void)xcd; (void)g; (void)c16; (void)kq; (void)th; (void)ea; (void)ew0; (void)ewn; (void)res_off; (void)tb; (void)j; (void)panel4; (void)has4
  const rsrc_t rws = __builtin_amdgcn_make_buffer_rsrc(ws, 0, WS_END, 0x00020000);
  unsigned tag = 0;
  bool dead = false;

#pragma unroll 1
  for (int layer = 0; layer < 2; ++layer) {
    const unsigned fl = WS_FLAGS + layer * FL_LAYER;
    const unsigned kvoff = WS_KV + layer * KV_LAYER;
    unsigned sink = 0;
    {
      FRESH();
      STAMP(0);
      auto load_kvw = [&]() {
        const char* xs = (const char*)((j & 1) ? wv : wk);
        if (wave < 3) {
          glds_lin<4, false>(xs + wave * 8192, lds0 + L_ATT + wave * 8192, lane * 16);
          glds_lin<4, false>(xs + wave * 8192 + 4096, lds0 + L_ATT + wave * 8192 + 4096, lane * 16);
        } else glds1<false>(xs + 24576, lds0 + L_ATT + 24576, lane * 16);
      };
      u32x4 af[5][2];
      if (layer == 0) {
        const rsrc_t ra = __builtin_amdgcn_make_buffer_rsrc((void*)x0, 0, XB, 0x00020000);
        const unsigned abase = (tb * 64 + th * 32 + c16) * (D_ * 2) + kq * 64 + g * 16;
#pragma unroll
        for (int s = 0; s < 5; ++s)
#pragma unroll
          for (int t = 0; t < 2; ++t) af[s][t] = __builtin_amdgcn_raw_buffer_load_b128(ra, abase + s * 256, t * (16 * D_ * 2), 0);
        const unsigned epoch = __hip_atomic_load((gu32*)(ws + WS_EPOCH), RLX_AGENT);
        if (wave < 4) load_kvw();
        else touch(wq, min((idx * 4 + kq) * 50 + lane, 6399) * 128, sink);
        STAMP(1);
        wait_vbar<0>();
        tag = UNI(epoch) + 1u;
        if (wave >= 4) {
#pragma unroll 1
          for (int blk = 0; blk < 7; ++blk) wslice_block(wq + j * 80, lds0, kq, lane, blk);
        } else {
#pragma unroll 1
          for (int pc = wave; pc < 10; pc += 4) {
            const int n = pc * 64 + lane, row = n / 10, ch = n - row * 10;
            glds1<false>(x0 + (long)tb * 64 * D_ + j * 80, lds0 + L_RES + pc * 1024, row * 1280 + ch * 16);
          }
        }
      } else {
        if (wave >= 4) {
#pragma unroll 1
          for (int blk = 0; blk < 7; ++blk) wslice_block(wq + j * 80, lds0, kq, lane, blk);
        } else load_kvw();
        if (wave == 0) poll_flags(ws, WS_FLAGS + FL_X + tb * 128, 8, tag, lane, dead);
        wait_vbar<0>();
        STAMP(1);
        const unsigned abase = WS_X + tb * 81920 + (kq * 4 + th * 2) * 1024 + lane * 16;
#pragma unroll
        for (int s = 0; s < 5; ++s)
#pragma unroll
          for (int t = 0; t < 2; ++t) af[s][t] = ld16_dev_s(rws, abase + t * 1024, s * 16384);
      }
      bf16x8 ones;
#pragma unroll
      for (int i = 0; i < 8; ++i) ones[i] = (short)0x3f80;
      const f32x4 z4 = {0.f, 0.f, 0.f, 0.f};
      const int q = c16 >> 2, p = lane & 3;
      const int row = 32 * kq + 8 * g + q;
      const unsigned fsw = (((q >> 1) & 1) | ((g & 1) << 1)) * 32;
      const unsigned woff = L_W + row * 128 + 8 * p, toff = L_WT + row * 32 + 8 * p, xoff = L_ATT + row * 40 + 8 * p;
      LAS float* stat = (LAS float*)(ldw + L_DUMP);
      LAS float* csum = (LAS float*)(ldw + L_KVP + 20480);
      const int tk = j >> 1;
      const bool isv = j & 1;
      {
        f32x4 a1s[2] = {z4, z4}, a2s[2] = {z4, z4}, akv[2] = {z4, z4}, aok[2] = {z4, z4};
#pragma unroll
        for (int s = 0; s < 5; ++s) {
          const bf16x8 a0 = __builtin_bit_cast(bf16x8, af[s][0]), a1 = __builtin_bit_cast(bf16x8, af[s][1]);
          if (th == (tk >> 1)) {
            const bf16x8 a = (tk & 1) ? a1 : a0;
            const bf16x8 x0f = lds_tr8(lds + xoff + s * 5120, 160), x1f = lds_tr8(lds + xoff + s * 5120 + 32, 160);
            if (!isv) {
              akv[0] = MFMA16(x0f, a, akv[0]); akv[1] = MFMA16(x1f, a, akv[1]);
              aok[0] = MFMA16(x0f, ones, aok[0]); aok[1] = MFMA16(x1f, ones, aok[1]);
            } else {
              akv[0] = MFMA16(a, x0f, akv[0]); akv[1] = MFMA16(a, x1f, akv[1]);
              aok[0] = MFMA16(ones, x0f, aok[0]); aok[1] = MFMA16(ones, x1f, aok[1]);
            }
          }
          a1s[0] = MFMA16(ones, a0, a1s[0]); a1s[1] = MFMA16(ones, a1, a1s[1]);
          a2s[0] = MFMA16(a0, a0, a2s[0]); a2s[1] = MFMA16(a1, a1, a2s[1]);
        }
        if (g == q) {
#pragma unroll
          for (int t = 0; t < 2; ++t) {
            const float d = (p & 2) ? ((p & 1) ? a2s[t][3] : a2s[t][2]) : ((p & 1) ? a2s[t][1] : a2s[t][0]);
            *(LAS f32x2*)(stat + (kq * 64 + th * 32 + t * 16 + c16) * 2) = f32x2{a1s[t][0], d};
          }
        }
        if (th == (tk >> 1)) {
          const bool l2 = isv ? (c16 < 4) : (g == 0);
          const int i2 = isv ? g * 4 + c16 : c16;
          const int o = L_KVP + kq * 2560;
          *(LAS f32x4*)(ldw + o + lane * 16) = akv[0];
          *(LAS f32x4*)(ldw + o + 1280 + lane * 16) = aok[0];
          if (l2) { *(LAS f32x4*)(ldw + o + 1024 + i2 * 16) = akv[1]; *(LAS f32x4*)(ldw + o + 2304 + i2 * 16) = aok[1]; }
        }
      }
      wait_bar<0>();
      float mu, rs;
      {
        f32x2 sv[4];
#pragma unroll
        for (int k = 0; k < 4; ++k) sv[k] = *(const LAS f32x2*)(stat + (k * 64 + ea * 16 + c16) * 2);
        float var; ln_finish((sv[0][0] + sv[1][0]) + (sv[2][0] + sv[3][0]), (sv[0][1] + sv[1][1]) + (sv[2][1] + sv[3][1]), mu, var);
        mu = rnd1(mu); rs = ln_rsd(var);
      }
      if (ea == tk) {
        const int li = th == 0 ? lane : 64 + (isv ? g * 4 + (c16 & 3) : c16);
        f32x4 s = z4, cs = z4;
#pragma unroll
        for (int k = 0; k < 4; ++k) { s += ldf4(lds + L_KVP + k * 2560 + li * 16); cs += ldf4(lds + L_KVP + k * 2560 + 1280 + li * 16); }
        const unsigned tile = kvoff + (tb * 2 + (ea >> 1)) * KV_TILE;
        if (!isv) {
          u32x2 w; w[0] = pack_bf16((s[0] - mu * cs[0]) * rs, (s[1] - mu * cs[1]) * rs); w[1] = pack_bf16((s[2] - mu * cs[2]) * rs, (s[3] - mu * cs[3]) * rs);
          const int r32 = (ea & 1) * 16 + c16;
          if (th == 0) st8_dev(rws, tile + ((g >> 1) * 32 + r32) * 16 + (g & 1) * 8, w);
          else if (g == 0) st8_dev(rws, tile + KV_KT + r32 * 8, w);
        } else {
          float o[4];
#pragma unroll
          for (int e = 0; e < 4; ++e) o[e] = (s[e] - __shfl(mu, 4 * g + e) * cs[e]) * __shfl(rs, 4 * g + e);
          const int d = th * 16 + c16;
          u32x2 w; w[0] = pack_bf16(o[0], o[1]); w[1] = pack_bf16(o[2], o[3]);
          if (d == DH_) { w[0] = 0x3f803f80u; w[1] = 0x3f803f80u; }
          if (d == DH_ + 1) { w[0] = 0u; w[1] = 0u; }
          if (d <= DH_ + 1) st8_dev(rws, tile + KV_V + (ea & 1) * KV_VS + ((g & 1) * 22 + d) * 16 + (g >> 1) * 8, w);
        }
      }
      f32x4 acc[5][2], aon[3] = {z4, z4, z4};
#pragma unroll
      for (int i = 0; i < 5; ++i) { acc[i][0] = z4; acc[i][1] = z4; }
      bf16x8 wq2[2][5];
#pragma unroll
      for (int i = 0; i < 5; ++i) wq2[0][i] = i < 4 ? lds_tr8(lds + woff + ((i * 32) ^ fsw), 512) : lds_tr8(lds + toff, 128);
#pragma unroll
      for (int s = 0; s < 5; ++s) {
        const bf16x8 a0 = __builtin_bit_cast(bf16x8, af[s][0]), a1 = __builtin_bit_cast(bf16x8, af[s][1]);
        const bf16x8* wf = wq2[s & 1];
#pragma unroll
        for (int i = 0; i < 5; ++i) {
          if (s < 4) wq2[(s + 1) & 1][i] = i < 4 ? lds_tr8(lds + woff + (s + 1) * 16384 + ((i * 32) ^ fsw), 512) : lds_tr8(lds + toff + (s + 1) * 4096, 128);
          acc[i][0] = MFMA16(wf[i], a0, acc[i][0]); acc[i][1] = MFMA16(wf[i], a1, acc[i][1]);
          SBAR();
        }
        if (th == 0) { aon[0] = MFMA16(wf[0], ones, aon[0]); aon[1] = MFMA16(wf[1], ones, aon[1]); aon[2] = MFMA16(wf[2], ones, aon[2]); }
        else { aon[0] = MFMA16(wf[3], ones, aon[0]); aon[1] = MFMA16(wf[4], ones, aon[1]); }
        if (s == 1) {
          publish(ws, fl + FL_KV + (tb * 8 + j) * 4, 32, 1024, tag, wave, lane);
          if (wave >= 4) {
            const char* w1w = (const char*)(w1 + (has4 ? ft : 0) * 64) + (long)((idx & 7) * 256 + kq * 64) * (F_ * 2);
            touch(w1w, lane * (F_ * 2), sink);
            touch((const char*)(w2 + tb * 64) + (long)(j * 216 + kq * 54) * (S_ * 2), (lane < 54 ? lane : 53) * (S_ * 2), sink);
          }
        }
      }
      bar_lds();
#pragma unroll
      for (int i = 0; i < 5; ++i)
#pragma unroll
        for (int t = 0; t < 2; ++t) *(LAS f32x4*)(ldw + ((kq * 5 + i) * 4 + th * 2 + t) * 1024 + lane * 16) = acc[i][t];
      if (c16 == 0) {
#pragma unroll
        for (int i = 0; i < 3; ++i) if (i < ewn) *(LAS f32x4*)(csum + (kq * 7 + ew0 + i) * 16 + 4 * g) = aon[i];
      }
      bar_lds();
      {
        const float r = rs * QSCALE;
        f32x4 s[3], cs[3];
#pragma unroll
        for (int i = 0; i < 3; ++i) {
          const int wt = min(ew0 + i, 4);
          s[i] = sum4(lds, wt * 4 + ea, 20, lane);
          cs[i] = z4;
#pragma unroll
          for (int k = 0; k < 4; ++k) cs[i] += *(const LAS f32x4*)(csum + (k * 7 + wt) * 16 + 4 * g);
        }
#pragma unroll
        for (int i = 0; i < 3; ++i) {
          if (i < ewn) {
            u32x2 w;
            w[0] = pack_bf16((s[i][0] - mu * cs[i][0]) * r, (s[i][1] - mu * cs[i][1]) * r);
            w[1] = pack_bf16((s[i][2] - mu * cs[i][2]) * r, (s[i][3] - mu * cs[i][3]) * r);
            *(LAS u32x2*)(ldw + L_QT + (ea * 16 + c16) * 160 + (ew0 + i) * 32 + 8 * g) = w;
          }
        }
      }
      bar_lds();
    }
    STAMP(2);
    bf16x8 w1r[16][2];
    {
      FRESH();
      const int r32 = lane & 31, hi = lane >> 5;
      const int hh = wave & 3, trow = th * 32 + r32;
      bf16x8 q16; bf16x4 q4;
      {
        const lds_cptr qp = lds + L_QT + trow * 160 + hh * 40;
        const u32x2 a = *(const LAS u32x2*)(qp + hi * 16), b = *(const LAS u32x2*)(qp + hi * 16 + 8), c = *(const LAS u32x2*)(qp + 32);
        u32x4 v = {a[0], a[1], b[0], b[1]};
        q16 = __builtin_bit_cast(bf16x8, v);
        u32x2 t = {hi ? 0u : c[0], hi ? 0u : c[1]};
        q4 = __builtin_bit_cast(bf16x4, t);
        PIN(q16); PIN(q4);
      }
      if (wave == 0) {
        unsigned spins = 0;
        while (!dead) {
          const u32x4 v = ld16_dev(rws, fl + FL_KV + (bid & 31) * 1024 + lane * 16);
          if (__all(v[0] == tag && v[1] == tag && v[2] == tag && v[3] == tag)) break;
          if (++spins > SPIN_CAP) dead = true;
          __builtin_amdgcn_s_sleep(1);
          asm volatile("" ::: "memory");
        }
      }
      bar_lds();
      STAMP(3);
      const unsigned koff = lane * 16;
      const char* KV = ws + kvoff + wave * 3072;
      auto issue_kv = [&](int ch) {
        const unsigned dst = lds0 + L_ATT + (ch % 3) * ATT_SLOT + wave * 3072;
        if (wave < 3) glds_lin<3, true>(KV + ch * ATT_CHB, dst, koff);
        else { glds1<true>(KV + ch * ATT_CHB, dst, koff); if (lane < 32) glds1<true>(KV + ch * ATT_CHB + 1024, dst + 1024, koff); }
      };
      const char* w1s = (const char*)(w1 + (has4 ? ft : 0) * 64) + (long)wave * 16 * (F_ * 2);
      unsigned sv0, sv1;
      {
        const int r = lane >> 3, pos = lane & 7;
        const unsigned pe = (((pos >> 1) ^ ((r >> 1) & 1)) << 1 | (pos & 1)) * 16;
        sv0 = r * (F_ * 2) + pe; sv1 = (r + 8) * (F_ * 2) + (pe ^ 64) - 1024;
      }
      const unsigned fsw = (((c16 >> 3) & 1) | ((g & 1) << 1)) * 32;
      const unsigned srow = (32 * kq + 8 * g + (c16 >> 2)) * 128 + 8 * (lane & 3);
      const unsigned sr0 = srow + ((64 * th) ^ fsw), sr1 = srow + ((64 * th + 32) ^ fsw);
      if (wave < 4) { issue_kv(0); issue_kv(1); }
      glds2(w1s, lds0 + L_STG + wave * 2048, sv0, sv1);
      const unsigned ktoff = KV_KT + r32 * 8, voff = KV_V + (hi * 22 + (r32 < 21 ? r32 : 21)) * 16;
      f32x16 o = {}, negm, sA, sB;
      bf16x8 k16, v0, v1; bf16x4 k4; u32x4 p0, p1;
      if (wave < 3) wait_bar<5>(); else if (wave == 3) wait_bar<4>(); else bar_lds();
      const lds_cptr ring = lds + L_ATT;
      {
        k16 = lds_b128(ring + koff);
        k4 = *(const LAS bf16x4*)(ring + ktoff);
        f32x16 z = {};
        z = MFMA32(k16, q16, z); z = MFMA32K8(k4, q4, z);
        k16 = lds_b128(ring + KV_TILE + koff);
        k4 = *(const LAS bf16x4*)(ring + KV_TILE + ktoff);
        v1 = lds_b128(ring + voff);
        const float m = rowmax16(z);
#pragma unroll
        for (int i = 0; i < 16; ++i) { negm[i] = -m; sA[i] = z[i] - m; }
        p1 = u32x4{0u, 0u, 0u, 0u};
      }
#define EX(x) __builtin_amdgcn_exp2f(x)
#define EXP4(X, i) do { X[i] = EX(X[i]); X[i + 1] = EX(X[i + 1]); X[i + 2] = EX(X[i + 2]); X[i + 3] = EX(X[i + 3]); } while (0)
#define BF8(x) __builtin_bit_cast(bf16x8, x)
#define PK4(P, X, i) do { P[0] = pack_bf16(X[i], X[i + 1]); P[1] = pack_bf16(X[i + 2], X[i + 3]); P[2] = pack_bf16(X[i + 4], X[i + 5]); P[3] = pack_bf16(X[i + 6], X[i + 7]); } while (0)
#define STEP(C, N, kn, vc) do { \
    N = MFMA32(k16, q16, negm); SBAR(); \
    v0 = lds_b128((vc) + voff); EXP4(C, 0); PIN(C); SBAR(); \
    o = MFMA32(v1, BF8(p1), o); SBAR(); \
    k16 = lds_b128((kn) + koff); EXP4(C, 4); PIN(C); SBAR(); PK4(p0, C, 0); PIN(p0); SBAR(); \
    N = MFMA32K8(k4, q4, N); SBAR(); \
    v1 = lds_b128((vc) + voff + KV_VS); EXP4(C, 8); PIN(C); SBAR(); \
    o = MFMA32(v0, BF8(p0), o); SBAR(); \
    k4 = *(const LAS bf16x4*)((kn) + ktoff); EXP4(C, 12); PIN(C); SBAR(); PK4(p1, C, 8); PIN(p1); SBAR(); \
  } while (0)
      static_for<0, ATT_NCH>([&](auto CH) {
        constexpr int ch = decltype(CH)::v;
        const lds_cptr cb = ring + (ch % 3) * ATT_SLOT;
        const lds_cptr nb = (ch + 1 < ATT_NCH) ? ring + ((ch + 1) % 3) * ATT_SLOT : cb;
        __builtin_amdgcn_s_setprio(1);
        STEP(sA, sB, cb + 2 * KV_TILE, cb);
        __builtin_amdgcn_s_setprio(0);
        STEP(sB, sA, cb + 3 * KV_TILE, cb + KV_TILE);
        if (wave < 4) wait_vbar<(ch >= 1 && ch <= 5) ? 4 : 0>(); else wait_vbar<0>();
        if (ch + 2 < ATT_NCH) { if (wave < 4) issue_kv(ch + 2); }
        if (ch + 1 < 16) glds2(w1s + (long)(ch + 1) * 128 * (F_ * 2), lds0 + ((ch & 1) ? L_STG : L_WT) + wave * 2048, sv0, sv1);
        if (ch < 5) { if (wave < 4) wslice_block(wo + j * 80, lds0, wave, lane, ch); }
        w1r[ch][0] = lds_tr8(lds + ((ch & 1) ? L_WT : L_STG) + sr0, 512); w1r[ch][1] = lds_tr8(lds + ((ch & 1) ? L_WT : L_STG) + sr1, 512);
        __builtin_amdgcn_s_setprio(3);
        SBAR();
        STEP(sA, sB, nb, cb + 2 * KV_TILE);
        PIN(w1r[ch][0]); PIN(w1r[ch][1]);
        __builtin_amdgcn_s_setprio(2);
        SBAR();
        STEP(sB, sA, nb + KV_TILE, cb + 3 * KV_TILE);
      });
      __builtin_amdgcn_s_setprio(0);
      o = MFMA32(v1, BF8(p1), o);
      float l = xsum32(hi ? o[8] : 0.f);
      bool bad = !(l < 1.8446744e19f);
#pragma unroll
      for (int i = 0; i < 12; ++i) bad |= !(__builtin_fabsf(o[i]) < 3.0e38f);
      if (__builtin_expect(__any(bad), 0)) {
        float m = -INFINITY;
        o = f32x16{};
#pragma unroll 1
        for (int t = 0; t < S_ / 32; ++t) {
          const unsigned tp = kvoff + t * KV_TILE;
          const bf16x8 a16 = BF8(ld16_dev(rws, tp + koff));
          const u32x2 a4 = __builtin_amdgcn_raw_buffer_load_b64(rws, tp + ktoff, 0, 16);
          const bf16x8 w0 = BF8(ld16_dev(rws, tp + voff)), w1f = BF8(ld16_dev(rws, tp + voff + KV_VS));
          f32x16 z = {};
          z = MFMA32(a16, q16, z); z = MFMA32K8(__builtin_bit_cast(bf16x4, a4), q4, z);
          const float mn = __builtin_fmaxf(m, rowmax16(z));
          const float alpha = __builtin_amdgcn_exp2f(m - mn);
          m = mn;
#pragma unroll
          for (int i = 0; i < 16; ++i) { z[i] = __builtin_amdgcn_exp2f(z[i] - m); o[i] *= alpha; }
          bf16x8 a0, a1; pack_p(z, a0, a1);
          o = MFMA32(w0, a0, o); o = MFMA32(w1f, a1, o);
        }
        l = xsum32(hi ? o[8] : 0.f);
      }
      const float rl = __builtin_amdgcn_rcpf(l);
      const unsigned ob = WS_O + layer * XB + tb * 81920 + ((trow >> 4) * 64 + (trow & 15)) * 16;
#pragma unroll
      for (int gq = 0; gq < 3; ++gq) {
        const int d0 = 8 * gq + 4 * hi;
        if (d0 < DH_) {
          const int k = (j * 4 + hh) * DH_ + d0;
          u32x2 w; w[0] = pack_bf16(o[4 * gq] * rl, o[4 * gq + 1] * rl); w[1] = pack_bf16(o[4 * gq + 2] * rl, o[4 * gq + 3] * rl);
          st8_dev(rws, ob + (k >> 5) * 4096 + ((k & 31) >> 3) * 256 + (k & 7) * 2, w);
        }
      }
      STAMP(4);
      publish(ws, fl + FL_O + tb * 128 + j * 4, 1, 0, tag, wave, lane);
      asm volatile("" :: "v"(sink));
      if (wave >= 4) { wslice_block(wo + j * 80, lds0, kq, lane, 5); wslice_block(wo + j * 80, lds0, kq, lane, 6); }
    }
    {
      FRESH();
      if (wave == 0) poll_flags(ws, fl + FL_O + tb * 128, 8, tag, lane, dead);
      wait_vbar<0>();
      STAMP(5);
      const unsigned abase = WS_O + layer * XB + tb * 81920 + (kq * 4 + th * 2) * 1024 + lane * 16;
      u32x4 af[5][2];
#pragma unroll
      for (int s = 0; s < 5; ++s)
#pragma unroll
        for (int t = 0; t < 2; ++t) af[s][t] = ld16_dev_s(rws, abase + t * 1024, s * 16384);
      f32x4 acc[5][2];
#pragma unroll
      for (int i = 0; i < 5; ++i) { acc[i][0] = f32x4{0.f, 0.f, 0.f, 0.f}; acc[i][1] = acc[i][0]; }
      const int q = c16 >> 2, p = lane & 3;
      const int row = 32 * kq + 8 * g + q;
      const unsigned fsw = (((q >> 1) & 1) | ((g & 1) << 1)) * 32;
      const unsigned woff = L_W + row * 128 + 8 * p, toff = L_WT + row * 32 + 8 * p;
      bf16x8 wf[2][5];
#pragma unroll
      for (int i = 0; i < 5; ++i) wf[0][i] = i < 4 ? lds_tr8(lds + woff + ((i * 32) ^ fsw), 512) : lds_tr8(lds + toff, 128);
#pragma unroll
      for (int s = 0; s < 5; ++s) {
        const bf16x8 a0 = __builtin_bit_cast(bf16x8, af[s][0]), a1 = __builtin_bit_cast(bf16x8, af[s][1]);
#pragma unroll
        for (int i = 0; i < 5; ++i) {
          if (s < 4) wf[(s + 1) & 1][i] = i < 4 ? lds_tr8(lds + woff + (s + 1) * 16384 + ((i * 32) ^ fsw), 512) : lds_tr8(lds + toff + (s + 1) * 4096, 128);
          acc[i][0] = MFMA16(wf[s & 1][i], a0, acc[i][0]); acc[i][1] = MFMA16(wf[s & 1][i], a1, acc[i][1]);
          SBAR();
        }
      }
      bar_lds();
#pragma unroll
      for (int i = 0; i < 5; ++i)
#pragma unroll
        for (int t = 0; t < 2; ++t) *(LAS f32x4*)(ldw + ((kq * 5 + i) * 4 + th * 2 + t) * 1024 + lane * 16) = acc[i][t];
      bar_lds();
      gu64* gr = (gu64*)(ws + WS_STAT + layer * ST_LAYER + (tb * 4 + ea) * 4096);
      {
        f32x4 s[3]; u32x2 r[3];
#pragma unroll
        for (int i = 0; i < 3; ++i) {
          const int wt = min(ew0 + i, 4);
          s[i] = sum4(lds, wt * 4 + ea, 20, lane);
          r[i] = *(const LAS u32x2*)(lds + res_off + wt * 32);
        }
        float s1 = 0.f, s2 = 0.f;
#pragma unroll
        for (int i = 0; i < 3; ++i) {
          if (i < ewn) {
            u32x2 w;
            w[0] = pack_bf16(bf_lo(r[i][0]) + s[i][0], bf_hi(r[i][0]) + s[i][1]);
            w[1] = pack_bf16(bf_lo(r[i][1]) + s[i][2], bf_hi(r[i][1]) + s[i][3]);
            *(LAS u32x2*)(ldw + res_off + (ew0 + i) * 32) = w;
            const float a = bf_lo(w[0]), b = bf_hi(w[0]), c = bf_lo(w[1]), d = bf_hi(w[1]);
            s1 += (a + b) + (c + d);
            s2 += (a * a + b * b) + (c * c + d * d);
          }
        }
        s1 = xsum32(xsum16(s1)); s2 = xsum32(xsum16(s2));
        if (lane < 32) __hip_atomic_store(gr + (2 * j + th) * 32 + lane, ((unsigned long long)tag << 32) | __float_as_uint(g ? s2 : s1), RLX_AGENT);
      }
      bar_lds();
      if (wave >= 4) {
        const int r = lane >> 3, pos = lane & 7;
        const unsigned pe = (((pos >> 1) ^ ((r >> 1) & 1)) << 1 | (pos & 1)) * 16;
        const unsigned v0 = r * (S_ * 2) + pe + 4096, v1 = (r + 8) * (S_ * 2) + (pe ^ 64) + 3072, vstep = 16 * (S_ * 2) - 2048;
        const char* wsrc = (const char*)(w2 + tb * 64) - 4096 + (long)kq * 32 * (S_ * 2);
#pragma unroll 1
        for (int st = 0; st < 4; ++st) glds4<false>(wsrc + (long)st * 128 * (S_ * 2), lds0 + L_RING + st * 16384 + kq * 4096, v0, v1, v0 + vstep, v1 + vstep);
        asm volatile("s_barrier" ::: "memory");
      } else {
        u32x2 xv[5];
#pragma unroll
        for (int wt = 0; wt < 5; ++wt) xv[wt] = __builtin_bit_cast(u32x2, lds_tr(lds + L_RES + (ea * 16 + 4 * g + q) * 160 + wt * 32 + 8 * p));
        float tot;
        {
          unsigned spins = 0;
          for (;;) {
            bool ok = true; tot = 0.f;
#pragma unroll
            for (int i = 0; i < 8; ++i) {
              const unsigned long long v = __hip_atomic_load(gr + i * 64 + lane, RLX_AGENT);
              tot += __uint_as_float((unsigned)v); ok &= (unsigned)(v >> 32) == tag;
            }
            if (__all(ok) || dead) break;
            if (++spins > SPIN_CAP) dead = true;
            __builtin_amdgcn_s_sleep(1);
          }
        }
        STAMP(6);
        tot = xsum32(tot);
        float mu4[4], rr4[4], m24[4], r24[4];
#pragma unroll
        for (int e = 0; e < 4; ++e) { mu4[e] = __shfl(tot, 4 * g + e); rr4[e] = __shfl(tot, 16 + 4 * g + e); }
#pragma unroll
        for (int e = 0; e < 4; ++e) {
          float mu, var; ln_finish(mu4[e], rr4[e], mu, var);
          mu4[e] = rnd1(mu); rr4[e] = ln_rsd(var);
          m24[e] = rnd1((mu - mu4[e]) * rr4[e]); r24[e] = ln_rsd(var * rr4[e] * rr4[e]);
        }
        const unsigned hb = WS_H + layer * XB + j * 327680 + ((tb * 2 + (ea >> 1)) * 5) * 1024 + (c16 + 16 * ((ea & 1) * 2 + (g >> 1))) * 16 + (g & 1) * 8;
#pragma unroll
        for (int wt = 0; wt < 5; ++wt) {
          const u32x2 v = xv[wt];
          u32x2 w;
          w[0] = pack_bf16(bf_lo(v[0]) - mu4[0], bf_hi(v[0]) - mu4[1]); w[1] = pack_bf16(bf_lo(v[1]) - mu4[2], bf_hi(v[1]) - mu4[3]);
          w[0] = pack_bf16(bf_lo(w[0]) * rr4[0], bf_hi(w[0]) * rr4[1]); w[1] = pack_bf16(bf_lo(w[1]) * rr4[2], bf_hi(w[1]) * rr4[3]);
          w[0] = pack_bf16(bf_lo(w[0]) - m24[0], bf_hi(w[0]) - m24[1]); w[1] = pack_bf16(bf_lo(w[1]) - m24[2], bf_hi(w[1]) - m24[3]);
          w[0] = pack_bf16(bf_lo(w[0]) * r24[0], bf_hi(w[0]) * r24[1]); w[1] = pack_bf16(bf_lo(w[1]) * r24[2], bf_hi(w[1]) * r24[3]);
          st8_dev(rws, hb + wt * 1024, w);
        }
        publish(ws, fl + FL_H + j * 512 + tb * 4, 4, 128, tag, wave, lane);
      }
      STAMP(7);
    }
    {
      FRESH();
      if (has4) {
        const int f = ft * 64 + ea * 16 + 4 * g;
        const u32x2 bias = *(const u32x2*)(b1 + f);
        if (wave == 0) poll_flags(ws, fl + FL_H + panel4 * 512 + (xcd & 3) * 128, 32, tag, lane, dead);
        asm volatile("s_barrier" ::: "memory");
        STAMP(8);
        const unsigned l16 = lane * 16;
        const int po = (th ? 12 + 2 * kq : 3 * kq) * 1024;
        const char* hsrc = ws + WS_H + layer * XB + panel4 * 327680 + po;
        auto issue = [&](int st) {
          if (th == 0) glds_lin<3, true>(hsrc + st * 20480, lds0 + (st & 3) * 20480 + po, l16);
          else glds_lin<2, true>(hsrc + st * 20480, lds0 + (st & 3) * 20480 + po, l16);
        };
        issue(0); issue(1); issue(2); issue(3);
        f32x4 acc[2][5];
#pragma unroll
        for (int i = 0; i < 2; ++i)
#pragma unroll
          for (int a = 0; a < 5; ++a) acc[i][a] = f32x4{0.f, 0.f, 0.f, 0.f};
        const lds_cptr ab = lds + kq * 5120 + l16;
        bf16x8 af[2][5];
        if (th == 0) wait_vbar<9>(); else wait_vbar<6>();
#pragma unroll
        for (int a = 0; a < 5; ++a) af[0][a] = lds_b128(ab + a * 1024);
        static_for<0, 16>([&](auto ST) {
          constexpr int st = decltype(ST)::v;
          if constexpr (st < 15) {
            constexpr int ahead = (14 - st) < 2 ? (14 - st) : 2;
            if (th == 0) wait_bar<3 * ahead>(); else wait_bar<2 * ahead>();
#pragma unroll
            for (int a = 0; a < 5; ++a) af[(st + 1) & 1][a] = lds_b128(ab + ((st + 1) & 3) * 20480 + a * 1024);
          }
          if (st + 4 < 16) { if (th == 0) issue(st + 4); }
          SBAR();
#pragma unroll
          for (int a = 0; a < 5; ++a) { acc[0][a] = MFMA16(w1r[st][0], af[st & 1][a], acc[0][a]); acc[1][a] = MFMA16(w1r[st][1], af[st & 1][a], acc[1][a]); }
          SBAR();
          if (st + 4 < 16) { if (th == 1) issue(st + 4); }
        });
        bar_lds();
#pragma unroll
        for (int i = 0; i < 2; ++i)
#pragma unroll
          for (int a = 0; a < 5; ++a) *(LAS f32x4*)(ldw + ((kq * 4 + 2 * th + i) * 5 + a) * 1024 + l16) = acc[i][a];
        bar_lds();
        STAMP(9);
        const unsigned ub = WS_U + layer * UB + ((panel4 * U_KS + (f >> 5)) * 5) * 1024 + (c16 + 16 * ((f & 31) >> 3)) * 16 + (f & 7) * 2;
        f32x4 sv[3];
#pragma unroll
        for (int i = 0; i < 3; ++i) sv[i] = sum4(lds, ea * 5 + min(ew0 + i, 4), 20, lane);
#pragma unroll
        for (int i = 0; i < 3; ++i) {
          if (i < ewn) {
            const f32x4 s = sv[i];
            u32x2 w;
            w[0] = pack_bf16(gelu_tanh(s[0] + bf_lo(bias[0])), gelu_tanh(s[1] + bf_hi(bias[0])));
            w[1] = pack_bf16(gelu_tanh(s[2] + bf_lo(bias[1])), gelu_tanh(s[3] + bf_hi(bias[1])));
            st8_dev(rws, ub + (ew0 + i) * 1024, w);
          }
        }
        publish(ws, fl + FL_U + panel4 * 512 + ft * 4, 4, 128, tag, wave, lane);
      }
    }
    STAMP(10);
    {
      FRESH();
      const int tok = tb * 64 + ea * 16 + c16;
      const float bias = __uint_as_float((unsigned)b2[tok] << 16);
      if (wave >= 4) {
        const int r = lane >> 3, pos = lane & 7;
        const unsigned pe = (((pos >> 1) ^ ((r >> 1) & 1)) << 1 | (pos & 1)) * 16;
        const unsigned v0 = r * (S_ * 2) + pe + 4096, v1 = (r + 8) * (S_ * 2) + (pe ^ 64) + 3072, vstep = 16 * (S_ * 2) - 2048;
        const char* wsrc = (const char*)(w2 + tb * 64) - 4096 + (long)kq * 32 * (S_ * 2);
#pragma unroll 1
        for (int st = 4; st < 9; ++st) glds4<false>(wsrc + (long)st * 128 * (S_ * 2), lds0 + (st - 4) * 16384 + kq * 4096, v0, v1, v0 + vstep, v1 + vstep);
        wait_vbar<32>();
        static_for<0, 13>([&](auto ST) {
          constexpr int st = decltype(ST)::v;
          constexpr int last = (st + 8 > 13) ? 13 : st + 8;
          wait_vbar<4 * (last - st - 1)>();
          if (st + 9 < 14) {
            const long o = (st + 9 == 13 && kq >= 2) ? (long)(2 * 13 - 1) * 64 * (S_ * 2) : (long)(st + 9) * 128 * (S_ * 2);
            glds4<false>(wsrc + o, lds0 + w2_slot(st + 9) + kq * 4096, v0, v1, v0 + vstep, v1 + vstep);
          }
        });
        asm volatile("s_barrier" ::: "memory");
      } else {
        if (wave == 0) poll_flags(ws, fl + FL_U + j * 512 + (tb & 3) * 128, 27, tag, lane, dead);
        asm volatile("s_barrier" ::: "memory");
        STAMP(11);
        const unsigned abase = WS_U + layer * UB + j * (U_KS * 5 * 1024) + kq * 5120 + lane * 16;
        const int q = c16 >> 2, p = lane & 3;
        const unsigned fsw = (((q >> 1) & 1) | ((g & 1) << 1)) * 32;
        const lds_cptr wrow = lds + (32 * kq + 8 * g + q) * 128 + 8 * p;
        u32x4 af[3][5];
#pragma unroll
        for (int st = 0; st < 3; ++st)
#pragma unroll
          for (int a = 0; a < 5; ++a) af[st][a] = ld16_dev_s(rws, abase + (a & 3) * 1024, st * 20480 + (a >> 2) * 4096);
        f32x4 acc[4][5];
#pragma unroll
        for (int i = 0; i < 4; ++i)
#pragma unroll
          for (int a = 0; a < 5; ++a) acc[i][a] = f32x4{0.f, 0.f, 0.f, 0.f};
        bf16x8 wf[2][4];
#pragma unroll
        for (int i = 0; i < 4; ++i) wf[0][i] = lds_tr8(wrow + w2_slot(0) + ((i * 32) ^ fsw), 512);
        static_for<0, 14>([&](auto ST) {
          constexpr int st = decltype(ST)::v;
          if constexpr (st < 13) {
            bar_lds();
#pragma unroll
            for (int i = 0; i < 4; ++i) wf[(st + 1) & 1][i] = lds_tr8(wrow + w2_slot(st + 1) + ((i * 32) ^ fsw), 512);
          }
          if (st < 13 || kq < 2) {
            SBAR();
#pragma unroll
            for (int a = 0; a < 5; ++a) {
              const bf16x8 x = __builtin_bit_cast(bf16x8, af[st % 3][a]);
#pragma unroll
              for (int i = 0; i < 4; ++i) acc[i][a] = MFMA16(x, wf[st & 1][i], acc[i][a]);
              SBAR();
              if (st + 3 < 14) af[st % 3][a] = ld16_dev_s(rws, abase + (a & 3) * 1024, (st + 3) * 20480 + (a >> 2) * 4096);
              SBAR();
            }
          }
        });
        bar_lds();
#pragma unroll
        for (int i = 0; i < 4; ++i)
#pragma unroll
          for (int a = 0; a < 5; ++a) *(LAS f32x4*)(ldw + ((kq * 4 + i) * 5 + a) * 1024 + lane * 16) = acc[i][a];
      }
      bar_lds();
      STAMP(12);
      f32x4 sv[3]; u32x2 rv[3];
#pragma unroll
      for (int i = 0; i < 3; ++i) { const int at = min(ew0 + i, 4); sv[i] = sum4(lds, ea * 5 + at, 20, lane); rv[i] = *(const LAS u32x2*)(lds + res_off + at * 32); }
#pragma unroll
      for (int i = 0; i < 3; ++i) {
        if (i < ewn) {
          const int at = ew0 + i;
          const f32x4 s = sv[i];
          const u32x2 r = rv[i];
          u32x2 w;
          w[0] = pack_bf16(bf_lo(r[0]) + (s[0] + bias), bf_hi(r[0]) + (s[1] + bias));
          w[1] = pack_bf16(bf_lo(r[1]) + (s[2] + bias), bf_hi(r[1]) + (s[3] + bias));
          const int d = j * 80 + at * 16 + 4 * g;
          if (layer == 0) {
            *(LAS u32x2*)(ldw + res_off + at * 32) = w;
            st8_dev(rws, WS_X + tb * 81920 + (((d >> 5) * 4 + ea) * 64 + c16 + 16 * ((d & 31) >> 3)) * 16 + (d & 7) * 2, w);
          } else {
            *(u32x2*)(out + (long)tok * D_ + d) = w;
          }
        }
      }
      if (layer == 0) publish(ws, WS_FLAGS + FL_X + tb * 128 + j * 4, 1, 0, tag, wave, lane);
      STAMP(13);
    }
  }
  if (blockIdx.x == 0 && tid == 0) __hip_atomic_store((gu32*)(ws + WS_EPOCH), tag, RLX_AGENT);
}

extern "C" void kernel_launch(void* const* d_in, const int* in_sizes, int n_in, void* d_out, int out_size, void* d_ws,
                              size_t ws_size, hipStream_t stream) {
  (void)hipFuncSetAttribute((const void*)mega_kernel, hipFuncAttributeMaxDynamicSharedMemorySize, LDS_BYTES);
  mega_kernel<<<256, 512, LDS_BYTES, stream>>>((const u16*)d_in[0], (const u16*)d_in[1], (const u16*)d_in[2], (const u16*)d_in[3],
                                               (const u16*)d_in[4], (const u16*)d_in[6], (const u16*)d_in[7], (const u16*)d_in[8],
                                               (const u16*)d_in[9], (u16*)d_out, (char*)d_ws);
}
```

```cpp
#include <hip/hip_runtime.h>
#include <hip/hip_bf16.h>
#include <stdint.h>

typedef __attribute__((ext_vector_type(8))) short bf16x8;
typedef __attribute__((ext_vector_type(4))) short bf16x4;
typedef __attribute__((ext_vector_type(16))) float f32x16;
typedef __attribute__((ext_vector_type(4))) float f32x4;
typedef __attribute__((ext_vector_type(2))) float f32x2;
typedef __attribute__((ext_vector_type(2))) __bf16 bf2_t;
typedef __attribute__((ext_vector_type(4))) unsigned u32x4;
typedef __attribute__((ext_vector_type(2))) unsigned u32x2;
typedef unsigned short u16;
#define LAS __attribute__((address_space(3)))
#define GAS __attribute__((address_space(1)))
typedef LAS const char* lds_cptr;
typedef LAS char* lds_ptr;
typedef GAS unsigned gu32;
typedef GAS unsigned long long gu64;
#define DI __device__ __forceinline__
#define RLX_AGENT __ATOMIC_RELAXED, __HIP_MEMORY_SCOPE_AGENT

constexpr int S_ = 2048, D_ = 640, DH_ = 20, F_ = 1728;
constexpr float LN_EPS = 1e-5f;
constexpr float QSCALE = 0.22360679774997896f * 1.4426950408889634f;

constexpr int KV_TILE = 2688, KV_KT = 1024, KV_V = 1280, KV_VS = 704;

constexpr unsigned WS_EPOCH = 0;
constexpr unsigned WS_FLAGS = 4096, FL_LAYER = 65536;
constexpr unsigned FL_KV = 0;
constexpr unsigned FL_O = 32768;
constexpr unsigned FL_X = 36864;
constexpr unsigned FL_H = 40960;
constexpr unsigned FL_U = 45056;
constexpr unsigned WS_STAT = 524288, ST_LAYER = 524288;
constexpr unsigned WS_KV = WS_STAT + 2 * ST_LAYER, KV_LAYER = 196608;
constexpr unsigned XB = S_ * D_ * 2;
constexpr unsigned WS_O = WS_KV + 2 * KV_LAYER;
constexpr unsigned WS_H = WS_O + 2 * XB;
constexpr int U_KS = 56;
constexpr unsigned UB = 8 * U_KS * 5 * 1024;
constexpr unsigned WS_U = WS_H + 2 * XB;
constexpr unsigned WS_X = WS_U + 2 * UB;
constexpr unsigned WS_END = WS_X + XB;

constexpr int L_W = 0;
constexpr int L_WT = 81920;
constexpr int L_RING = 81920;
constexpr int L_ATT = 102400;
constexpr int L_QT = 81920;
constexpr int L_KVP = 128064;
constexpr int L_STG = 134656;
constexpr int L_RES = 151040;
constexpr int L_DUMP = 161280;
constexpr int LDS_BYTES = 163840;
constexpr unsigned SPIN_CAP = 1u << 21;

DI unsigned pack_bf16(float lo, float hi) {
  f32x2 v = {lo, hi};
  bf2_t b = __builtin_convertvector(v, bf2_t);
  return __builtin_bit_cast(unsigned, b);
}
DI float bf_lo(unsigned u) { return __uint_as_float(u << 16); }
DI float bf_hi(unsigned u) { return __uint_as_float(u & 0xffff0000u); }
DI float rnd1(float v) { return bf_lo(pack_bf16(v, 0.f)); }
DI float ln_rsd(float var) { return __builtin_amdgcn_rcpf(rnd1(__builtin_amdgcn_sqrtf(rnd1(rnd1(var) + LN_EPS)))); }

#define GLDS_HEAD "s_mov_b32 m0, %1\n\ts_nop 4\n\t"
template <bool DEV> DI void glds1(const void* sbase, unsigned m0v, unsigned v0) {
  if (DEV) asm volatile(GLDS_HEAD "global_load_lds_dwordx4 %2, %0 sc1" ::"s"(sbase), "s"(m0v), "v"(v0) : "memory");
  else asm volatile(GLDS_HEAD "global_load_lds_dwordx4 %2, %0" ::"s"(sbase), "s"(m0v), "v"(v0) : "memory");
}
template <bool DEV> DI void glds4(const void* sbase, unsigned m0v, unsigned v0, unsigned v1, unsigned v2, unsigned v3) {
  if (DEV)
    asm volatile(GLDS_HEAD "global_load_lds_dwordx4 %2, %0 sc1\n\tglobal_load_lds_dwordx4 %3, %0 offset:1024 sc1\n\t"
                           "global_load_lds_dwordx4 %4, %0 offset:2048 sc1\n\tglobal_load_lds_dwordx4 %5, %0 offset:3072 sc1"
                 ::"s"(sbase), "s"(m0v), "v"(v0), "v"(v1), "v"(v2), "v"(v3) : "memory");
  else
    asm volatile(GLDS_HEAD "global_load_lds_dwordx4 %2, %0\n\tglobal_load_lds_dwordx4 %3, %0 offset:1024\n\t"
                           "global_load_lds_dwordx4 %4, %0 offset:2048\n\tglobal_load_lds_dwordx4 %5, %0 offset:3072"
                 ::"s"(sbase), "s"(m0v), "v"(v0), "v"(v1), "v"(v2), "v"(v3) : "memory");
}
template <int N, bool DEV> DI void glds_lin(const void* sbase, unsigned m0v, unsigned v) {
  if (N == 1) glds1<DEV>(sbase, m0v, v);
  else if (N == 4) glds4<DEV>(sbase, m0v, v, v, v, v);
  else if (N == 2) {
    if (DEV) asm volatile(GLDS_HEAD "global_load_lds_dwordx4 %2, %0 sc1\n\tglobal_load_lds_dwordx4 %2, %0 offset:1024 sc1" ::"s"(sbase), "s"(m0v), "v"(v) : "memory");
    else asm volatile(GLDS_HEAD "global_load_lds_dwordx4 %2, %0\n\tglobal_load_lds_dwordx4 %2, %0 offset:1024" ::"s"(sbase), "s"(m0v), "v"(v) : "memory");
  } else {
    if (DEV) asm volatile(GLDS_HEAD "global_load_lds_dwordx4 %2, %0 sc1\n\tglobal_load_lds_dwordx4 %2, %0 offset:1024 sc1\n\tglobal_load_lds_dwordx4 %2, %0 offset:2048 sc1" ::"s"(sbase), "s"(m0v), "v"(v) : "memory");
    else asm volatile(GLDS_HEAD "global_load_lds_dwordx4 %2, %0\n\tglobal_load_lds_dwordx4 %2, %0 offset:1024\n\tglobal_load_lds_dwordx4 %2, %0 offset:2048" ::"s"(sbase), "s"(m0v), "v"(v) : "memory");
  }
}
DI void glds2(const void* sbase, unsigned m0v, unsigned v0, unsigned v1) {
  asm volatile(GLDS_HEAD "global_load_lds_dwordx4 %2, %0\n\tglobal_load_lds_dwordx4 %3, %0 offset:1024" ::"s"(sbase), "s"(m0v), "v"(v0), "v"(v1) : "memory");
}
#define UNI(x) __builtin_amdgcn_readfirstlane(x)
template <int N> DI void wait_bar() { asm volatile("s_waitcnt vmcnt(%0) lgkmcnt(0)\n\ts_barrier" ::"i"(N) : "memory"); }
template <int N> DI void wait_vbar() { asm volatile("s_waitcnt vmcnt(%0)\n\ts_barrier" ::"i"(N) : "memory"); }
DI void bar_lds() { asm volatile("s_waitcnt lgkmcnt(0)\n\ts_barrier" ::: "memory"); }
typedef short v4i16_t __attribute__((ext_vector_type(4)));
DI bf16x4 lds_tr(lds_cptr p) {
  return __builtin_bit_cast(bf16x4, __builtin_amdgcn_ds_read_tr16_b64_v4i16((LAS v4i16_t*)p));
}
DI bf16x8 lds_tr8(lds_cptr p, int step) {
  const bf16x4 lo = lds_tr(p), hi = lds_tr(p + step);
  return __builtin_shufflevector(lo, hi, 0, 1, 2, 3, 4, 5, 6, 7);
}
DI bf16x8 lds_b128(lds_cptr p) { return *(const LAS bf16x8*)p; }
#define MFMA16(a, b, c) __builtin_amdgcn_mfma_f32_16x16x32_bf16((a), (b), (c), 0, 0, 0)
#define MFMA32(a, b, c) __builtin_amdgcn_mfma_f32_32x32x16_bf16((a), (b), (c), 0, 0, 0)
#define MFMA32K8(a, b, c) __builtin_amdgcn_mfma_f32_32x32x8bf16_1k((a), (b), (c), 0, 0, 0)
#define SBAR() __builtin_amdgcn_sched_barrier(0)
#define PIN(x) asm volatile("" : "+v"(x))
typedef __amdgpu_buffer_rsrc_t rsrc_t;
DI void st8_dev(rsrc_t r, unsigned off, u32x2 v) { __builtin_amdgcn_raw_buffer_store_b64(v, r, off, 0, 16); }
DI u32x4 ld16_dev(rsrc_t r, unsigned off) { return __builtin_amdgcn_raw_buffer_load_b128(r, off, 0, 16); }
DI u32x4 ld16_dev_s(rsrc_t r, unsigned off, unsigned soff) { return __builtin_amdgcn_raw_buffer_load_b128(r, off, soff, 16); }

DI void poll_flags(const char* ws, unsigned off, int n, unsigned tag, int lane, bool& dead) {
  gu32* f = (gu32*)(ws + off);
  unsigned spins = 0;
  while (!dead) {
    unsigned v = tag;
    if (lane < n) v = __hip_atomic_load(f + lane, RLX_AGENT);
    if (__all(v == tag)) break;
    if (++spins > SPIN_CAP) dead = true;
    __builtin_amdgcn_s_sleep(1);
  }
  asm volatile("" ::: "memory");
}
DI void publish(const char* ws, unsigned off, int nrep, unsigned rstride, unsigned tag, int wave, int lane) {
  wait_bar<0>();
  if (wave == 0 && lane < nrep) __hip_atomic_store((gu32*)(ws + off + lane * rstride), tag, RLX_AGENT);
}

DI float fmax3(float a, float b, float c) { return __builtin_fmaxf(__builtin_fmaxf(a, b), c); }
DI float rowmax16(const f32x16& s) {
  float a = fmax3(s[0], s[1], s[2]), b = fmax3(s[3], s[4], s[5]);
  a = fmax3(a, s[6], s[7]); b = fmax3(b, s[8], s[9]); a = fmax3(a, s[10], s[11]); b = fmax3(b, s[12], s[13]);
  a = fmax3(a, s[14], s[15]); a = __builtin_fmaxf(a, b);
  return __builtin_fmaxf(a, __shfl_xor(a, 32));
}
DI void pack_p(const f32x16& p, bf16x8& lo, bf16x8& hi) {
  u32x4 a, b;
#pragma unroll
  for (int i = 0; i < 4; ++i) { a[i] = pack_bf16(p[2 * i], p[2 * i + 1]); b[i] = pack_bf16(p[8 + 2 * i], p[9 + 2 * i]); }
  lo = __builtin_bit_cast(bf16x8, a); hi = __builtin_bit_cast(bf16x8, b);
}
DI float gelu_tanh(float u) {
  const float z = 0.7978845608028654f * (u + 0.044715f * u * u * u);
  const float e = __builtin_amdgcn_exp2f(-2.885390081777927f * z);
  return u * __builtin_amdgcn_rcpf(1.f + e);
}

DI void wslice_block(const u16* w, unsigned lds0, int lw, int lane, int blk) {
  const char* src = (const char*)w + lw * (160 * 1280);
  if (blk < 5) {
    const int r = lane >> 3, pos = lane & 7;
    const unsigned pe = (((pos >> 1) ^ ((r >> 1) & 1)) << 1 | (pos & 1)) * 16;
    const unsigned v0 = r * 1280 + pe, v1 = r * 1280 + (pe ^ 64) + 9216;
    glds4<false>(src + blk * (32 * 1280), lds0 + L_W + lw * 20480 + blk * 4096, v0, v1, v0 + 18432, v1 + 18432);
  } else {
    const unsigned t0 = (lane >> 1) * 1280 + 128 + (lane & 1) * 16;
    if (blk == 5) glds4<false>(src, lds0 + L_WT + lw * 5120, t0, t0 + 39936, t0 + 2 * 39936, t0 + 3 * 39936);
    else glds1<false>(src + 128 * 1280, lds0 + L_WT + lw * 5120 + 4096, t0);
  }
}

DI void touch(const void* sbase, unsigned voff, unsigned& sink) {
  asm volatile("s_nop 4\n\tglobal_load_dword %0, %1, %2" : "+v"(sink) : "v"(voff), "s"(sbase) : "memory");
}
DI float xsum16(float x) {
  auto r = __builtin_amdgcn_permlane16_swap(__float_as_uint(x), __float_as_uint(x), false, false);
  return __uint_as_float(r[0]) + __uint_as_float(r[1]);
}
DI float xsum32(float x) {
  auto r = __builtin_amdgcn_permlane32_swap(__float_as_uint(x), __float_as_uint(x), false, false);
  return __uint_as_float(r[0]) + __uint_as_float(r[1]);
}
DI f32x4 ldf4(lds_cptr p) { return *(const LAS f32x4*)p; }

DI f32x4 sum4(lds_cptr lds, int t, int nt, int lane) {
  f32x4 s = *(const LAS f32x4*)(lds + t * 1024 + lane * 16);
#pragma unroll
  for (int k = 1; k < 4; ++k) s += *(const LAS f32x4*)(lds + (k * nt + t) * 1024 + lane * 16);
  return s;
}
DI void ln_finish(float s1, float s2, float& mu, float& var) { mu = s1 * (1.f / D_); var = fmaxf(s2 * (1.f / D_) - mu * mu, 0.f); }

constexpr int ATT_CH = 4;
constexpr int ATT_CHB = ATT_CH * KV_TILE;
constexpr int ATT_SLOT = 10752;
constexpr int ATT_NCH = S_ / 32 / ATT_CH;
constexpr int w2_slot(int st) { return (st % 9) < 4 ? L_RING + (st % 9) * 16384 : ((st % 9) - 4) * 16384; }
template <int N> struct IC { static constexpr int v = N; };
template <int I, int N, class F> DI void static_for(F&& f) { if constexpr (I < N) { f(IC<I>{}); static_for<I + 1, N>(f); } }


__global__ __launch_bounds__(512, 2) void mega_kernel(const u16* __restrict__ x0, const u16* __restrict__ wq, const u16* __restrict__ wk,
                                                      const u16* __restrict__ wv, const u16* __restrict__ wo, const u16* __restrict__ w1,
                                                      const u16* __restrict__ b1, const u16* __restrict__ w2, const u16* __restrict__ b2,
                                                      u16* __restrict__ out, char* ws) {
  extern __shared__ __attribute__((aligned(16))) char smem[];
  const int tid = threadIdx.x;
  const unsigned lds0 = (unsigned)(uintptr_t)smem;
  const lds_cptr lds = (lds_cptr)smem;
  const lds_ptr ldw = (lds_ptr)smem;
#define FRESH() \
  int lane = tid & 63; asm volatile("" : "+v"(lane)); \
  int wave = UNI(tid >> 6); asm volatile("" : "+s"(wave)); \
  int bid = blockIdx.x; asm volatile("" : "+s"(bid)); \
  const int xcd = bid & 7, idx = bid >> 3, tb = xcd * 4 + (idx >> 3), j = idx & 7; \
  const int g = lane >> 4, c16 = lane & 15, kq = wave & 3, th = wave >> 2; \
  const int ea = wave & 3, ew0 = th ? 3 : 0, ewn = th ? 2 : 3; \
  const unsigned res_off = L_RES + (ea * 16 + c16) * 160 + 8 * g; \
  const int panel4 = (xcd & 1) * 4 + (idx & 3), ft = (xcd >> 1) * 7 + (idx >> 2); \
  const bool has4 = idx < 28 && ft < 27; \
  (void)xcd; (void)g; (void)c16; (void)kq; (void)th; (void)ea; (void)ew0; (void)ewn; (void)res_off; (void)tb; (void)j; (void)panel4; (void)has4
  const rsrc_t rws = __builtin_amdgcn_make_buffer_rsrc(ws, 0, WS_END, 0x00020000);
  unsigned tag = 0;
  bool dead = false;

#pragma unroll 1
  for (int layer = 0; layer < 2; ++layer) {
    const unsigned fl = WS_FLAGS + layer * FL_LAYER;
    const unsigned kvoff = WS_KV + layer * KV_LAYER;
    unsigned sink = 0;
    {
      FRESH();
      auto load_kvw = [&]() {
        const char* xs = (const char*)((j & 1) ? wv : wk);
        if (wave < 3) {
          glds_lin<4, false>(xs + wave * 8192, lds0 + L_ATT + wave * 8192, lane * 16);
          glds_lin<4, false>(xs + wave * 8192 + 4096, lds0 + L_ATT + wave * 8192 + 4096, lane * 16);
        } else glds1<false>(xs + 24576, lds0 + L_ATT + 24576, lane * 16);
      };
      u32x4 af[5][2];
      if (layer == 0) {
        const rsrc_t ra = __builtin_amdgcn_make_buffer_rsrc((void*)x0, 0, XB, 0x00020000);
        const unsigned abase = (tb * 64 + th * 32 + c16) * (D_ * 2) + kq * 64 + g * 16;
#pragma unroll
        for (int s = 0; s < 5; ++s)
#pragma unroll
          for (int t = 0; t < 2; ++t) af[s][t] = __builtin_amdgcn_raw_buffer_load_b128(ra, abase + s * 256, t * (16 * D_ * 2), 0);
        const unsigned epoch = __hip_atomic_load((gu32*)(ws + WS_EPOCH), RLX_AGENT);
        if (wave < 4) load_kvw();
        else touch(wq, min((idx * 4 + kq) * 50 + lane, 6399) * 128, sink);
        wait_vbar<0>();
        tag = UNI(epoch) + 1u;
        if (wave >= 4) {
#pragma unroll 1
          for (int blk = 0; blk < 7; ++blk) wslice_block(wq + j * 80, lds0, kq, lane, blk);
        } else {
#pragma unroll 1
          for (int pc = wave; pc < 10; pc += 4) {
            const int n = pc * 64 + lane, row = n / 10, ch = n - row * 10;
            glds1<false>(x0 + (long)tb * 64 * D_ + j * 80, lds0 + L_RES + pc * 1024, row * 1280 + ch * 16);
          }
        }
      } else {
        if (wave >= 4) {
#pragma unroll 1
          for (int blk = 0; blk < 7; ++blk) wslice_block(wq + j * 80, lds0, kq, lane, blk);
        } else load_kvw();
        if (wave == 0) poll_flags(ws, WS_FLAGS + FL_X + tb * 128, 8, tag, lane, dead);
        wait_vbar<0>();
        const unsigned abase = WS_X + tb * 81920 + (kq * 4 + th * 2) * 1024 + lane * 16;
#pragma unroll
        for (int s = 0; s < 5; ++s)
#pragma unroll
          for (int t = 0; t < 2; ++t) af[s][t] = ld16_dev_s(rws, abase + t * 1024, s * 16384);
      }
      bf16x8 ones;
#pragma unroll
      for (int i = 0; i < 8; ++i) ones[i] = (short)0x3f80;
      const f32x4 z4 = {0.f, 0.f, 0.f, 0.f};
      const int q = c16 >> 2, p = lane & 3;
      const int row = 32 * kq + 8 * g + q;
      const unsigned fsw = (((q >> 1) & 1) | ((g & 1) << 1)) * 32;
      const unsigned woff = L_W + row * 128 + 8 * p, toff = L_WT + row * 32 + 8 * p, xoff = L_ATT + row * 40 + 8 * p;
      LAS float* stat = (LAS float*)(ldw + L_DUMP);
      LAS float* csum = (LAS float*)(ldw + L_KVP + 20480);
      const int tk = j >> 1;
      const bool isv = j & 1;
      {
        f32x4 a1s[2] = {z4, z4}, a2s[2] = {z4, z4}, akv[2] = {z4, z4}, aok[2] = {z4, z4};
#pragma unroll
        for (int s = 0; s < 5; ++s) {
          const bf16x8 a0 = __builtin_bit_cast(bf16x8, af[s][0]), a1 = __builtin_bit_cast(bf16x8, af[s][1]);
          if (th == (tk >> 1)) {
            const bf16x8 a = (tk & 1) ? a1 : a0;
            const bf16x8 x0f = lds_tr8(lds + xoff + s * 5120, 160), x1f = lds_tr8(lds + xoff + s * 5120 + 32, 160);
            if (!isv) {
              akv[0] = MFMA16(x0f, a, akv[0]); akv[1] = MFMA16(x1f, a, akv[1]);
              aok[0] = MFMA16(x0f, ones, aok[0]); aok[1] = MFMA16(x1f, ones, aok[1]);
            } else {
              akv[0] = MFMA16(a, x0f, akv[0]); akv[1] = MFMA16(a, x1f, akv[1]);
              aok[0] = MFMA16(ones, x0f, aok[0]); aok[1] = MFMA16(ones, x1f, aok[1]);
            }
          }
          a1s[0] = MFMA16(ones, a0, a1s[0]); a1s[1] = MFMA16(ones, a1, a1s[1]);
          a2s[0] = MFMA16(a0, a0, a2s[0]); a2s[1] = MFMA16(a1, a1, a2s[1]);
        }
        if (g == q) {
#pragma unroll
          for (int t = 0; t < 2; ++t) {
            const float d = (p & 2) ? ((p & 1) ? a2s[t][3] : a2s[t][2]) : ((p & 1) ? a2s[t][1] : a2s[t][0]);
            *(LAS f32x2*)(stat + (kq * 64 + th * 32 + t * 16 + c16) * 2) = f32x2{a1s[t][0], d};
          }
        }
        if (th == (tk >> 1)) {
          const bool l2 = isv ? (c16 < 4) : (g == 0);
          const int i2 = isv ? g * 4 + c16 : c16;
          const int o = L_KVP + kq * 2560;
          *(LAS f32x4*)(ldw + o + lane * 16) = akv[0];
          *(LAS f32x4*)(ldw + o + 1280 + lane * 16) = aok[0];
          if (l2) { *(LAS f32x4*)(ldw + o + 1024 + i2 * 16) = akv[1]; *(LAS f32x4*)(ldw + o + 2304 + i2 * 16) = aok[1]; }
        }
      }
      wait_bar<0>();
      float mu, rs;
      {
        f32x2 sv[4];
#pragma unroll
        for (int k = 0; k < 4; ++k) sv[k] = *(const LAS f32x2*)(stat + (k * 64 + ea * 16 + c16) * 2);
        float var; ln_finish((sv[0][0] + sv[1][0]) + (sv[2][0] + sv[3][0]), (sv[0][1] + sv[1][1]) + (sv[2][1] + sv[3][1]), mu, var);
        mu = rnd1(mu); rs = ln_rsd(var);
      }
      if (ea == tk) {
        const int li = th == 0 ? lane : 64 + (isv ? g * 4 + (c16 & 3) : c16);
        f32x4 s = z4, cs = z4;
#pragma unroll
        for (int k = 0; k < 4; ++k) { s += ldf4(lds + L_KVP + k * 2560 + li * 16); cs += ldf4(lds + L_KVP + k * 2560 + 1280 + li * 16); }
        const unsigned tile = kvoff + (tb * 2 + (ea >> 1)) * KV_TILE;
        if (!isv) {
          u32x2 w; w[0] = pack_bf16((s[0] - mu * cs[0]) * rs, (s[1] - mu * cs[1]) * rs); w[1] = pack_bf16((s[2] - mu * cs[2]) * rs, (s[3] - mu * cs[3]) * rs);
          const int r32 = (ea & 1) * 16 + c16;
          if (th == 0) st8_dev(rws, tile + ((g >> 1) * 32 + r32) * 16 + (g & 1) * 8, w);
          else if (g == 0) st8_dev(rws, tile + KV_KT + r32 * 8, w);
        } else {
          float o[4];
#pragma unroll
          for (int e = 0; e < 4; ++e) o[e] = (s[e] - __shfl(mu, 4 * g + e) * cs[e]) * __shfl(rs, 4 * g + e);
          const int d = th * 16 + c16;
          u32x2 w; w[0] = pack_bf16(o[0], o[1]); w[1] = pack_bf16(o[2], o[3]);
          if (d == DH_) { w[0] = 0x3f803f80u; w[1] = 0x3f803f80u; }
          if (d == DH_ + 1) { w[0] = 0u; w[1] = 0u; }
          if (d <= DH_ + 1) st8_dev(rws, tile + KV_V + (ea & 1) * KV_VS + ((g & 1) * 22 + d) * 16 + (g >> 1) * 8, w);
        }
      }
      f32x4 acc[5][2], aon[3] = {z4, z4, z4};
#pragma unroll
      for (int i = 0; i < 5; ++i) { acc[i][0] = z4; acc[i][1] = z4; }
      bf16x8 wq2[2][5];
#pragma unroll
      for (int i = 0; i < 5; ++i) wq2[0][i] = i < 4 ? lds_tr8(lds + woff + ((i * 32) ^ fsw), 512) : lds_tr8(lds + toff, 128);
#pragma unroll
      for (int s = 0; s < 5; ++s) {
        const bf16x8 a0 = __builtin_bit_cast(bf16x8, af[s][0]), a1 = __builtin_bit_cast(bf16x8, af[s][1]);
        const bf16x8* wf = wq2[s & 1];
#pragma unroll
        for (int i = 0; i < 5; ++i) {
          if (s < 4) wq2[(s + 1) & 1][i] = i < 4 ? lds_tr8(lds + woff + (s + 1) * 16384 + ((i * 32) ^ fsw), 512) : lds_tr8(lds + toff + (s + 1) * 4096, 128);
          acc[i][0] = MFMA16(wf[i], a0, acc[i][0]); acc[i][1] = MFMA16(wf[i], a1, acc[i][1]);
          SBAR();
        }
        if (th == 0) { aon[0] = MFMA16(wf[0], ones, aon[0]); aon[1] = MFMA16(wf[1], ones, aon[1]); aon[2] = MFMA16(wf[2], ones, aon[2]); }
        else { aon[0] = MFMA16(wf[3], ones, aon[0]); aon[1] = MFMA16(wf[4], ones, aon[1]); }
        if (s == 1) {
          publish(ws, fl + FL_KV + (tb * 8 + j) * 4, 32, 1024, tag, wave, lane);
          if (wave >= 4) {
            touch((const char*)(w2 + tb * 64) + (long)(j * 216 + kq * 54) * (S_ * 2), (lane < 54 ? lane : 53) * (S_ * 2), sink);
          }
        }
      }
      bar_lds();
#pragma unroll
      for (int i = 0; i < 5; ++i)
#pragma unroll
        for (int t = 0; t < 2; ++t) *(LAS f32x4*)(ldw + ((kq * 5 + i) * 4 + th * 2 + t) * 1024 + lane * 16) = acc[i][t];
      if (c16 == 0) {
#pragma unroll
        for (int i = 0; i < 3; ++i) if (i < ewn) *(LAS f32x4*)(csum + (kq * 7 + ew0 + i) * 16 + 4 * g) = aon[i];
      }
      bar_lds();
      {
        const float r = rs * QSCALE;
        f32x4 s[3], cs[3];
#pragma unroll
        for (int i = 0; i < 3; ++i) {
          const int wt = min(ew0 + i, 4);
          s[i] = sum4(lds, wt * 4 + ea, 20, lane);
          cs[i] = z4;
#pragma unroll
          for (int k = 0; k < 4; ++k) cs[i] += *(const LAS f32x4*)(csum + (k * 7 + wt) * 16 + 4 * g);
        }
#pragma unroll
        for (int i = 0; i < 3; ++i) {
          if (i < ewn) {
            u32x2 w;
            w[0] = pack_bf16((s[i][0] - mu * cs[i][0]) * r, (s[i][1] - mu * cs[i][1]) * r);
            w[1] = pack_bf16((s[i][2] - mu * cs[i][2]) * r, (s[i][3] - mu * cs[i][3]) * r);
            *(LAS u32x2*)(ldw + L_QT + (ea * 16 + c16) * 160 + (ew0 + i) * 32 + 8 * g) = w;
          }
        }
      }
      bar_lds();
    }
    bf16x8 w1r[16][2];
    {
      FRESH();
      const int r32 = lane & 31, hi = lane >> 5;
      const int hh = wave & 3, trow = th * 32 + r32;
      bf16x8 q16; bf16x4 q4;
      {
        const lds_cptr qp = lds + L_QT + trow * 160 + hh * 40;
        const u32x2 a = *(const LAS u32x2*)(qp + hi * 16), b = *(const LAS u32x2*)(qp + hi * 16 + 8), c = *(const LAS u32x2*)(qp + 32);
        u32x4 v = {a[0], a[1], b[0], b[1]};
        q16 = __builtin_bit_cast(bf16x8, v);
        u32x2 t = {hi ? 0u : c[0], hi ? 0u : c[1]};
        q4 = __builtin_bit_cast(bf16x4, t);
        PIN(q16); PIN(q4);
      }
      if (wave == 0) {
        unsigned spins = 0;
        while (!dead) {
          const u32x4 v = ld16_dev(rws, fl + FL_KV + (bid & 31) * 1024 + lane * 16);
          if (__all(v[0] == tag && v[1] == tag && v[2] == tag && v[3] == tag)) break;
          if (++spins > SPIN_CAP) dead = true;
          __builtin_amdgcn_s_sleep(1);
          asm volatile("" ::: "memory");
        }
      }
      bar_lds();
      const unsigned koff = lane * 16;
      const char* KV = ws + kvoff + wave * 3072;
      auto issue_kv = [&](int ch) {
        const unsigned dst = lds0 + L_ATT + (ch % 3) * ATT_SLOT + wave * 3072;
        if (wave < 3) glds_lin<3, true>(KV + ch * ATT_CHB, dst, koff);
        else { glds1<true>(KV + ch * ATT_CHB, dst, koff); if (lane < 32) glds1<true>(KV + ch * ATT_CHB + 1024, dst + 1024, koff); }
      };
      const char* w1s = (const char*)(w1 + (has4 ? ft : 0) * 64) + (long)wave * 16 * (F_ * 2);
      unsigned sv0, sv1;
      {
        const int r = lane >> 3, pos = lane & 7;
        const unsigned pe = (((pos >> 1) ^ ((r >> 1) & 1)) << 1 | (pos & 1)) * 16;
        sv0 = r * (F_ * 2) + pe; sv1 = (r + 8) * (F_ * 2) + (pe ^ 64) - 1024;
      }
      const unsigned fsw = (((c16 >> 3) & 1) | ((g & 1) << 1)) * 32;
      const unsigned srow = (32 * kq + 8 * g + (c16 >> 2)) * 128 + 8 * (lane & 3);
      const unsigned sr0 = srow + ((64 * th) ^ fsw), sr1 = srow + ((64 * th + 32) ^ fsw);
      if (wave < 4) { issue_kv(0); issue_kv(1); }
      glds2(w1s, lds0 + L_STG + wave * 2048, sv0, sv1);
      const unsigned ktoff = KV_KT + r32 * 8, voff = KV_V + (hi * 22 + (r32 < 21 ? r32 : 21)) * 16;
      f32x16 o = {}, negm, sA, sB;
      bf16x8 k16, v0, v1; bf16x4 k4; u32x4 p0, p1;
      if (wave < 3) wait_bar<5>(); else if (wave == 3) wait_bar<4>(); else bar_lds();
      const lds_cptr ring = lds + L_ATT;
      {
        k16 = lds_b128(ring + koff);
        k4 = *(const LAS bf16x4*)(ring + ktoff);
        f32x16 z = {};
        z = MFMA32(k16, q16, z); z = MFMA32K8(k4, q4, z);
        k16 = lds_b128(ring + KV_TILE + koff);
        k4 = *(const LAS bf16x4*)(ring + KV_TILE + ktoff);
        v1 = lds_b128(ring + voff);
        const float m = rowmax16(z);
#pragma unroll
        for (int i = 0; i < 16; ++i) { negm[i] = -m; sA[i] = z[i] - m; }
        p1 = u32x4{0u, 0u, 0u, 0u};
      }
#define EX(x) __builtin_amdgcn_exp2f(x)
#define EXP4(X, i) do { X[i] = EX(X[i]); X[i + 1] = EX(X[i + 1]); X[i + 2] = EX(X[i + 2]); X[i + 3] = EX(X[i + 3]); } while (0)
#define BF8(x) __builtin_bit_cast(bf16x8, x)
#define PK4(P, X, i) do { P[0] = pack_bf16(X[i], X[i + 1]); P[1] = pack_bf16(X[i + 2], X[i + 3]); P[2] = pack_bf16(X[i + 4], X[i + 5]); P[3] = pack_bf16(X[i + 6], X[i + 7]); } while (0)
#define STEP(C, N, kn, vc) do { \
    N = MFMA32(k16, q16, negm); SBAR(); \
    v0 = lds_b128((vc) + voff); EXP4(C, 0); PIN(C); SBAR(); \
    o = MFMA32(v1, BF8(p1), o); SBAR(); \
    k16 = lds_b128((kn) + koff); EXP4(C, 4); PIN(C); SBAR(); PK4(p0, C, 0); PIN(p0); SBAR(); \
    N = MFMA32K8(k4, q4, N); SBAR(); \
    v1 = lds_b128((vc) + voff + KV_VS); EXP4(C, 8); PIN(C); SBAR(); \
    o = MFMA32(v0, BF8(p0), o); SBAR(); \
    k4 = *(const LAS bf16x4*)((kn) + ktoff); EXP4(C, 12); PIN(C); SBAR(); PK4(p1, C, 8); PIN(p1); SBAR(); \
  } while (0)
      static_for<0, ATT_NCH>([&](auto CH) {
        constexpr int ch = decltype(CH)::v;
        const lds_cptr cb = ring + (ch % 3) * ATT_SLOT;
        const lds_cptr nb = (ch + 1 < ATT_NCH) ? ring + ((ch + 1) % 3) * ATT_SLOT : cb;
        __builtin_amdgcn_s_setprio(1);
        STEP(sA, sB, cb + 2 * KV_TILE, cb);
        __builtin_amdgcn_s_setprio(0);
        STEP(sB, sA, cb + 3 * KV_TILE, cb + KV_TILE);
        if (wave < 4) wait_vbar<(ch >= 1 && ch <= 5) ? 4 : 0>(); else wait_vbar<0>();
        if (ch + 2 < ATT_NCH) { if (wave < 4) issue_kv(ch + 2); }
        if (ch + 1 < 16) glds2(w1s + (long)(ch + 1) * 128 * (F_ * 2), lds0 + ((ch & 1) ? L_STG : L_WT) + wave * 2048, sv0, sv1);
        if (ch < 5) { if (wave < 4) wslice_block(wo + j * 80, lds0, wave, lane, ch); }
        w1r[ch][0] = lds_tr8(lds + ((ch & 1) ? L_WT : L_STG) + sr0, 512); w1r[ch][1] = lds_tr8(lds + ((ch & 1) ? L_WT : L_STG) + sr1, 512);
        __builtin_amdgcn_s_setprio(3);
        SBAR();
        STEP(sA, sB, nb, cb + 2 * KV_TILE);
        PIN(w1r[ch][0]); PIN(w1r[ch][1]);
        __builtin_amdgcn_s_setprio(2);
        SBAR();
        STEP(sB, sA, nb + KV_TILE, cb + 3 * KV_TILE);
      });
      __builtin_amdgcn_s_setprio(0);
      o = MFMA32(v1, BF8(p1), o);
      float l = xsum32(hi ? o[8] : 0.f);
      bool bad = !(l < 1.8446744e19f);
#pragma unroll
      for (int i = 0; i < 12; ++i) bad |= !(__builtin_fabsf(o[i]) < 3.0e38f);
      if (__builtin_expect(__any(bad), 0)) {
        float m = -INFINITY;
        o = f32x16{};
#pragma unroll 1
        for (int t = 0; t < S_ / 32; ++t) {
          const unsigned tp = kvoff + t * KV_TILE;
          const bf16x8 a16 = BF8(ld16_dev(rws, tp + koff));
          const u32x2 a4 = __builtin_amdgcn_raw_buffer_load_b64(rws, tp + ktoff, 0, 16);
          const bf16x8 w0 = BF8(ld16_dev(rws, tp + voff)), w1f = BF8(ld16_dev(rws, tp + voff + KV_VS));
          f32x16 z = {};
          z = MFMA32(a16, q16, z); z = MFMA32K8(__builtin_bit_cast(bf16x4, a4), q4, z);
          const float mn = __builtin_fmaxf(m, rowmax16(z));
          const float alpha = __builtin_amdgcn_exp2f(m - mn);
          m = mn;
#pragma unroll
          for (int i = 0; i < 16; ++i) { z[i] = __builtin_amdgcn_exp2f(z[i] - m); o[i] *= alpha; }
          bf16x8 a0, a1; pack_p(z, a0, a1);
          o = MFMA32(w0, a0, o); o = MFMA32(w1f, a1, o);
        }
        l = xsum32(hi ? o[8] : 0.f);
      }
      const float rl = __builtin_amdgcn_rcpf(l);
      const unsigned ob = WS_O + layer * XB + tb * 81920 + ((trow >> 4) * 64 + (trow & 15)) * 16;
#pragma unroll
      for (int gq = 0; gq < 3; ++gq) {
        const int d0 = 8 * gq + 4 * hi;
        if (d0 < DH_) {
          const int k = (j * 4 + hh) * DH_ + d0;
          u32x2 w; w[0] = pack_bf16(o[4 * gq] * rl, o[4 * gq + 1] * rl); w[1] = pack_bf16(o[4 * gq + 2] * rl, o[4 * gq + 3] * rl);
          st8_dev(rws, ob + (k >> 5) * 4096 + ((k & 31) >> 3) * 256 + (k & 7) * 2, w);
        }
      }
      publish(ws, fl + FL_O + tb * 128 + j * 4, 1, 0, tag, wave, lane);
      asm volatile("" :: "v"(sink));
      if (wave >= 4) { wslice_block(wo + j * 80, lds0, kq, lane, 5); wslice_block(wo + j * 80, lds0, kq, lane, 6); }
    }
    {
      FRESH();
      if (wave == 0) poll_flags(ws, fl + FL_O + tb * 128, 8, tag, lane, dead);
      wait_vbar<0>();
      const unsigned abase = WS_O + layer * XB + tb * 81920 + (kq * 4 + th * 2) * 1024 + lane * 16;
      u32x4 af[5][2];
#pragma unroll
      for (int s = 0; s < 5; ++s)
#pragma unroll
        for (int t = 0; t < 2; ++t) af[s][t] = ld16_dev_s(rws, abase + t * 1024, s * 16384);
      f32x4 acc[5][2];
#pragma unroll
      for (int i = 0; i < 5; ++i) { acc[i][0] = f32x4{0.f, 0.f, 0.f, 0.f}; acc[i][1] = acc[i][0]; }
      const int q = c16 >> 2, p = lane & 3;
      const int row = 32 * kq + 8 * g + q;
      const unsigned fsw = (((q >> 1) & 1) | ((g & 1) << 1)) * 32;
      const unsigned woff = L_W + row * 128 + 8 * p, toff = L_WT + row * 32 + 8 * p;
      bf16x8 wf[2][5];
#pragma unroll
      for (int i = 0; i < 5; ++i) wf[0][i] = i < 4 ? lds_tr8(lds + woff + ((i * 32) ^ fsw), 512) : lds_tr8(lds + toff, 128);
#pragma unroll
      for (int s = 0; s < 5; ++s) {
        const bf16x8 a0 = __builtin_bit_cast(bf16x8, af[s][0]), a1 = __builtin_bit_cast(bf16x8, af[s][1]);
#pragma unroll
        for (int i = 0; i < 5; ++i) {
          if (s < 4) wf[(s + 1) & 1][i] = i < 4 ? lds_tr8(lds + woff + (s + 1) * 16384 + ((i * 32) ^ fsw), 512) : lds_tr8(lds + toff + (s + 1) * 4096, 128);
          acc[i][0] = MFMA16(wf[s & 1][i], a0, acc[i][0]); acc[i][1] = MFMA16(wf[s & 1][i], a1, acc[i][1]);
          SBAR();
        }
      }
      bar_lds();
#pragma unroll
      for (int i = 0; i < 5; ++i)
#pragma unroll
        for (int t = 0; t < 2; ++t) *(LAS f32x4*)(ldw + ((kq * 5 + i) * 4 + th * 2 + t) * 1024 + lane * 16) = acc[i][t];
      bar_lds();
      gu64* gr = (gu64*)(ws + WS_STAT + layer * ST_LAYER + (tb * 4 + ea) * 4096);
      {
        f32x4 s[3]; u32x2 r[3];
#pragma unroll
        for (int i = 0; i < 3; ++i) {
          const int wt = min(ew0 + i, 4);
          s[i] = sum4(lds, wt * 4 + ea, 20, lane);
          r[i] = *(const LAS u32x2*)(lds + res_off + wt * 32);
        }
        float s1 = 0.f, s2 = 0.f;
#pragma unroll
        for (int i = 0; i < 3; ++i) {
          if (i < ewn) {
            u32x2 w;
            w[0] = pack_bf16(bf_lo(r[i][0]) + s[i][0], bf_hi(r[i][0]) + s[i][1]);
            w[1] = pack_bf16(bf_lo(r[i][1]) + s[i][2], bf_hi(r[i][1]) + s[i][3]);
            *(LAS u32x2*)(ldw + res_off + (ew0 + i) * 32) = w;
            const float a = bf_lo(w[0]), b = bf_hi(w[0]), c = bf_lo(w[1]), d = bf_hi(w[1]);
            s1 += (a + b) + (c + d);
            s2 += (a * a + b * b) + (c * c + d * d);
          }
        }
        s1 = xsum32(xsum16(s1)); s2 = xsum32(xsum16(s2));
        if (lane < 32) __hip_atomic_store(gr + (2 * j + th) * 32 + lane, ((unsigned long long)tag << 32) | __float_as_uint(g ? s2 : s1), RLX_AGENT);
      }
      bar_lds();
      if (wave >= 4) {
        const int r = lane >> 3, pos = lane & 7;
        const unsigned pe = (((pos >> 1) ^ ((r >> 1) & 1)) << 1 | (pos & 1)) * 16;
        const unsigned v0 = r * (S_ * 2) + pe + 4096, v1 = (r + 8) * (S_ * 2) + (pe ^ 64) + 3072, vstep = 16 * (S_ * 2) - 2048;
        const char* wsrc = (const char*)(w2 + tb * 64) - 4096 + (long)kq * 32 * (S_ * 2);
#pragma unroll 1
        for (int st = 0; st < 4; ++st) glds4<false>(wsrc + (long)st * 128 * (S_ * 2), lds0 + L_RING + st * 16384 + kq * 4096, v0, v1, v0 + vstep, v1 + vstep);
        asm volatile("s_barrier" ::: "memory");
      } else {
        u32x2 xv[5];
#pragma unroll
        for (int wt = 0; wt < 5; ++wt) xv[wt] = __builtin_bit_cast(u32x2, lds_tr(lds + L_RES + (ea * 16 + 4 * g + q) * 160 + wt * 32 + 8 * p));
        float tot;
        {
          unsigned spins = 0;
          for (;;) {
            bool ok = true; tot = 0.f;
#pragma unroll
            for (int i = 0; i < 8; ++i) {
              const unsigned long long v = __hip_atomic_load(gr + i * 64 + lane, RLX_AGENT);
              tot += __uint_as_float((unsigned)v); ok &= (unsigned)(v >> 32) == tag;
            }
            if (__all(ok) || dead) break;
            if (++spins > SPIN_CAP) dead = true;
            __builtin_amdgcn_s_sleep(1);
          }
        }
        tot = xsum32(tot);
        float mu4[4], rr4[4], m24[4], r24[4];
#pragma unroll
        for (int e = 0; e < 4; ++e) { mu4[e] = __shfl(tot, 4 * g + e); rr4[e] = __shfl(tot, 16 + 4 * g + e); }
#pragma unroll
        for (int e = 0; e < 4; ++e) {
          float mu, var; ln_finish(mu4[e], rr4[e], mu, var);
          mu4[e] = rnd1(mu); rr4[e] = ln_rsd(var);
          m24[e] = rnd1((mu - mu4[e]) * rr4[e]); r24[e] = ln_rsd(var * rr4[e] * rr4[e]);
        }
        const unsigned hb = WS_H + layer * XB + j * 327680 + ((tb * 2 + (ea >> 1)) * 5) * 1024 + (c16 + 16 * ((ea & 1) * 2 + (g >> 1))) * 16 + (g & 1) * 8;
#pragma unroll
        for (int wt = 0; wt < 5; ++wt) {
          const u32x2 v = xv[wt];
          u32x2 w;
          w[0] = pack_bf16(bf_lo(v[0]) - mu4[0], bf_hi(v[0]) - mu4[1]); w[1] = pack_bf16(bf_lo(v[1]) - mu4[2], bf_hi(v[1]) - mu4[3]);
          w[0] = pack_bf16(bf_lo(w[0]) * rr4[0], bf_hi(w[0]) * rr4[1]); w[1] = pack_bf16(bf_lo(w[1]) * rr4[2], bf_hi(w[1]) * rr4[3]);
          w[0] = pack_bf16(bf_lo(w[0]) - m24[0], bf_hi(w[0]) - m24[1]); w[1] = pack_bf16(bf_lo(w[1]) - m24[2], bf_hi(w[1]) - m24[3]);
          w[0] = pack_bf16(bf_lo(w[0]) * r24[0], bf_hi(w[0]) * r24[1]); w[1] = pack_bf16(bf_lo(w[1]) * r24[2], bf_hi(w[1]) * r24[3]);
          st8_dev(rws, hb + wt * 1024, w);
        }
        publish(ws, fl + FL_H + j * 512 + tb * 4, 4, 128, tag, wave, lane);
      }
    }
    {
      FRESH();
      if (has4) {
        const int f = ft * 64 + ea * 16 + 4 * g;
        const u32x2 bias = *(const u32x2*)(b1 + f);
        if (wave == 0) poll_flags(ws, fl + FL_H + panel4 * 512 + ((idx >> 2) & 3) * 128, 32, tag, lane, dead);
        asm volatile("s_barrier" ::: "memory");
        const unsigned l16 = lane * 16;
        const int po = (th ? 12 + 2 * kq : 3 * kq) * 1024;
        const char* hsrc = ws + WS_H + layer * XB + panel4 * 327680 + po;
        auto issue = [&](int st) {
          if (th == 0) glds_lin<3, true>(hsrc + st * 20480, lds0 + (st & 3) * 20480 + po, l16);
          else glds_lin<2, true>(hsrc + st * 20480, lds0 + (st & 3) * 20480 + po, l16);
        };
        issue(0); issue(1); issue(2); issue(3);
        f32x4 acc[2][5];
#pragma unroll
        for (int i = 0; i < 2; ++i)
#pragma unroll
          for (int a = 0; a < 5; ++a) acc[i][a] = f32x4{0.f, 0.f, 0.f, 0.f};
        const lds_cptr ab = lds + kq * 5120 + l16;
        bf16x8 af[2][5];
        if (th == 0) wait_vbar<9>(); else wait_vbar<6>();
#pragma unroll
        for (int a = 0; a < 5; ++a) af[0][a] = lds_b128(ab + a * 1024);
        static_for<0, 16>([&](auto ST) {
          constexpr int st = decltype(ST)::v;
          if constexpr (st < 15) {
            constexpr int ahead = (14 - st) < 2 ? (14 - st) : 2;
            if (th == 0) wait_bar<3 * ahead>(); else wait_bar<2 * ahead>();
#pragma unroll
            for (int a = 0; a < 5; ++a) af[(st + 1) & 1][a] = lds_b128(ab + ((st + 1) & 3) * 20480 + a * 1024);
          }
          if (st + 4 < 16) { if (th == 0) issue(st + 4); }
          SBAR();
#pragma unroll
          for (int a = 0; a < 5; ++a) { acc[0][a] = MFMA16(w1r[st][0], af[st & 1][a], acc[0][a]); acc[1][a] = MFMA16(w1r[st][1], af[st & 1][a], acc[1][a]); }
          SBAR();
          if (st + 4 < 16) { if (th == 1) issue(st + 4); }
        });
        bar_lds();
#pragma unroll
        for (int i = 0; i < 2; ++i)
#pragma unroll
          for (int a = 0; a < 5; ++a) *(LAS f32x4*)(ldw + ((kq * 4 + 2 * th + i) * 5 + a) * 1024 + l16) = acc[i][a];
        bar_lds();
        const unsigned ub = WS_U + layer * UB + ((panel4 * U_KS + (f >> 5)) * 5) * 1024 + (c16 + 16 * ((f & 31) >> 3)) * 16 + (f & 7) * 2;
        f32x4 sv[3];
#pragma unroll
        for (int i = 0; i < 3; ++i) sv[i] = sum4(lds, ea * 5 + min(ew0 + i, 4), 20, lane);
#pragma unroll
        for (int i = 0; i < 3; ++i) {
          if (i < ewn) {
            const f32x4 s = sv[i];
            u32x2 w;
            w[0] = pack_bf16(gelu_tanh(s[0] + bf_lo(bias[0])), gelu_tanh(s[1] + bf_hi(bias[0])));
            w[1] = pack_bf16(gelu_tanh(s[2] + bf_lo(bias[1])), gelu_tanh(s[3] + bf_hi(bias[1])));
            st8_dev(rws, ub + (ew0 + i) * 1024, w);
          }
        }
        publish(ws, fl + FL_U + panel4 * 512 + ft * 4, 4, 128, tag, wave, lane);
      }
    }
    {
      FRESH();
      const int tok = tb * 64 + ea * 16 + c16;
      const float bias = __uint_as_float((unsigned)b2[tok] << 16);
      if (wave >= 4) {
        const int r = lane >> 3, pos = lane & 7;
        const unsigned pe = (((pos >> 1) ^ ((r >> 1) & 1)) << 1 | (pos & 1)) * 16;
        const unsigned v0 = r * (S_ * 2) + pe + 4096, v1 = (r + 8) * (S_ * 2) + (pe ^ 64) + 3072, vstep = 16 * (S_ * 2) - 2048;
        const char* wsrc = (const char*)(w2 + tb * 64) - 4096 + (long)kq * 32 * (S_ * 2);
#pragma unroll 1
        for (int st = 4; st < 9; ++st) glds4<false>(wsrc + (long)st * 128 * (S_ * 2), lds0 + (st - 4) * 16384 + kq * 4096, v0, v1, v0 + vstep, v1 + vstep);
        wait_vbar<32>();
        static_for<0, 13>([&](auto ST) {
          constexpr int st = decltype(ST)::v;
          constexpr int last = (st + 8 > 13) ? 13 : st + 8;
          wait_vbar<4 * (last - st - 1)>();
          if (st + 9 < 14) {
            const long o = (st + 9 == 13 && kq >= 2) ? (long)(2 * 13 - 1) * 64 * (S_ * 2) : (long)(st + 9) * 128 * (S_ * 2);
            glds4<false>(wsrc + o, lds0 + w2_slot(st + 9) + kq * 4096, v0, v1, v0 + vstep, v1 + vstep);
          }
        });
        asm volatile("s_barrier" ::: "memory");
      } else {
        if (wave == 0) poll_flags(ws, fl + FL_U + j * 512 + (tb & 3) * 128, 27, tag, lane, dead);
        asm volatile("s_barrier" ::: "memory");
        const unsigned abase = WS_U + layer * UB + j * (U_KS * 5 * 1024) + kq * 5120 + lane * 16;
        const int q = c16 >> 2, p = lane & 3;
        const unsigned fsw = (((q >> 1) & 1) | ((g & 1) << 1)) * 32;
        const lds_cptr wrow = lds + (32 * kq + 8 * g + q) * 128 + 8 * p;
        u32x4 af[3][5];
#pragma unroll
        for (int st = 0; st < 3; ++st)
#pragma unroll
          for (int a = 0; a < 5; ++a) af[st][a] = ld16_dev_s(rws, abase + (a & 3) * 1024, st * 20480 + (a >> 2) * 4096);
        f32x4 acc[4][5];
#pragma unroll
        for (int i = 0; i < 4; ++i)
#pragma unroll
          for (int a = 0; a < 5; ++a) acc[i][a] = f32x4{0.f, 0.f, 0.f, 0.f};
        bf16x8 wf[2][4];
#pragma unroll
        for (int i = 0; i < 4; ++i) wf[0][i] = lds_tr8(wrow + w2_slot(0) + ((i * 32) ^ fsw), 512);
        static_for<0, 14>([&](auto ST) {
          constexpr int st = decltype(ST)::v;
          if constexpr (st < 13) {
            bar_lds();
#pragma unroll
            for (int i = 0; i < 4; ++i) wf[(st + 1) & 1][i] = lds_tr8(wrow + w2_slot(st + 1) + ((i * 32) ^ fsw), 512);
          }
          if (st < 13 || kq < 2) {
            SBAR();
#pragma unroll
            for (int a = 0; a < 5; ++a) {
              const bf16x8 x = __builtin_bit_cast(bf16x8, af[st % 3][a]);
#pragma unroll
              for (int i = 0; i < 4; ++i) acc[i][a] = MFMA16(x, wf[st & 1][i], acc[i][a]);
              SBAR();
              if (st + 3 < 14) af[st % 3][a] = ld16_dev_s(rws, abase + (a & 3) * 1024, (st + 3) * 20480 + (a >> 2) * 4096);
              SBAR();
            }
          }
        });
        bar_lds();
#pragma unroll
        for (int i = 0; i < 4; ++i)
#pragma unroll
          for (int a = 0; a < 5; ++a) *(LAS f32x4*)(ldw + ((kq * 4 + i) * 5 + a) * 1024 + lane * 16) = acc[i][a];
      }
      bar_lds();
      f32x4 sv[3]; u32x2 rv[3];
#pragma unroll
      for (int i = 0; i < 3; ++i) { const int at = min(ew0 + i, 4); sv[i] = sum4(lds, ea * 5 + at, 20, lane); rv[i] = *(const LAS u32x2*)(lds + res_off + at * 32); }
#pragma unroll
      for (int i = 0; i < 3; ++i) {
        if (i < ewn) {
          const int at = ew0 + i;
          const f32x4 s = sv[i];
          const u32x2 r = rv[i];
          u32x2 w;
          w[0] = pack_bf16(bf_lo(r[0]) + (s[0] + bias), bf_hi(r[0]) + (s[1] + bias));
          w[1] = pack_bf16(bf_lo(r[1]) + (s[2] + bias), bf_hi(r[1]) + (s[3] + bias));
          const int d = j * 80 + at * 16 + 4 * g;
          if (layer == 0) {
            *(LAS u32x2*)(ldw + res_off + at * 32) = w;
            st8_dev(rws, WS_X + tb * 81920 + (((d >> 5) * 4 + ea) * 64 + c16 + 16 * ((d & 31) >> 3)) * 16 + (d & 7) * 2, w);
          } else {
            *(u32x2*)(out + (long)tok * D_ + d) = w;
          }
        }
      }
      if (layer == 0) publish(ws, WS_FLAGS + FL_X + tb * 128 + j * 4, 1, 0, tag, wave, lane);
    }
  }
  if (blockIdx.x == 0 && tid == 0) __hip_atomic_store((gu32*)(ws + WS_EPOCH), tag, RLX_AGENT);
}

extern "C" void kernel_launch(void* const* d_in, const int* in_sizes, int n_in, void* d_out, int out_size, void* d_ws,
                              size_t ws_size, hipStream_t stream) {
  (void)hipFuncSetAttribute((const void*)mega_kernel, hipFuncAttributeMaxDynamicSharedMemorySize, LDS_BYTES);
  mega_kernel<<<256, 512, LDS_BYTES, stream>>>((const u16*)d_in[0], (const u16*)d_in[1], (const u16*)d_in[2], (const u16*)d_in[3],
                                               (const u16*)d_in[4], (const u16*)d_in[6], (const u16*)d_in[7], (const u16*)d_in[8],
                                               (const u16*)d_in[9], (u16*)d_out, (char*)d_ws);
}
```

```cpp
#include <hip/hip_runtime.h>
#include <hip/hip_bf16.h>
#include <stdint.h>

typedef __attribute__((ext_vector_type(8))) short bf16x8;
typedef __attribute__((ext_vector_type(4))) short bf16x4;
typedef __attribute__((ext_vector_type(16))) float f32x16;
typedef __attribute__((ext_vector_type(4))) float f32x4;
typedef __attribute__((ext_vector_type(2))) float f32x2;
typedef __attribute__((ext_vector_type(2))) __bf16 bf2_t;
typedef __attribute__((ext_vector_type(4))) unsigned u32x4;
typedef __attribute__((ext_vector_type(2))) unsigned u32x2;
typedef unsigned short u16;
#define LAS __attribute__((address_space(3)))
#define GAS __attribute__((address_space(1)))
typedef LAS const char* lds_cptr;
typedef LAS char* lds_ptr;
typedef GAS unsigned gu32;
typedef GAS unsigned long long gu64;
#define DI __device__ __forceinline__
#define RLX_AGENT __ATOMIC_RELAXED, __HIP_MEMORY_SCOPE_AGENT

constexpr int S_ = 2048, D_ = 640, DH_ = 20, F_ = 1728;
constexpr float LN_EPS = 1e-5f;
constexpr float QSCALE = 0.22360679774997896f * 1.4426950408889634f;

constexpr int KV_TILE = 2688, KV_KT = 1024, KV_V = 1280, KV_VS = 704;

constexpr unsigned WS_EPOCH = 0;
constexpr unsigned WS_FLAGS = 4096, FL_LAYER = 65536;
constexpr unsigned FL_KV = 0;
constexpr unsigned FL_O = 32768;
constexpr unsigned FL_X = 36864;
constexpr unsigned FL_H = 40960;
constexpr unsigned FL_U = 45056;
constexpr unsigned WS_STAT = 524288, ST_LAYER = 524288;
constexpr unsigned WS_KV = WS_STAT + 2 * ST_LAYER, KV_LAYER = 196608;
constexpr unsigned XB = S_ * D_ * 2;
constexpr unsigned WS_O = WS_KV + 2 * KV_LAYER;
constexpr unsigned WS_H = WS_O + 2 * XB;
constexpr int U_KS = 56;
constexpr unsigned UB = 8 * U_KS * 5 * 1024;
constexpr unsigned WS_U = WS_H + 2 * XB;
constexpr unsigned WS_X = WS_U + 2 * UB;
constexpr unsigned WS_END = WS_X + XB;

constexpr int L_W = 0;
constexpr int L_WT = 81920;
constexpr int L_RING = 81920;
constexpr int L_ATT = 102400;
constexpr int L_QT = 81920;
constexpr int L_KVP = 128064;
constexpr int L_STG = 134656;
constexpr int L_RES = 151040;
constexpr int L_DUMP = 161280;
constexpr int LDS_BYTES = 163840;
constexpr unsigned SPIN_CAP = 1u << 21;

DI unsigned pack_bf16(float lo, float hi) {
  f32x2 v = {lo, hi};
  bf2_t b = __builtin_convertvector(v, bf2_t);
  return __builtin_bit_cast(unsigned, b);
}
DI float bf_lo(unsigned u) { return __uint_as_float(u << 16); }
DI float bf_hi(unsigned u) { return __uint_as_float(u & 0xffff0000u); }
DI float rnd1(float v) { return bf_lo(pack_bf16(v, 0.f)); }
DI float ln_rsd(float var) { return __builtin_amdgcn_rcpf(rnd1(__builtin_amdgcn_sqrtf(rnd1(rnd1(var) + LN_EPS)))); }

#define GLDS_HEAD "s_mov_b32 m0, %1\n\ts_nop 4\n\t"
template <bool DEV> DI void glds1(const void* sbase, unsigned m0v, unsigned v0) {
  if (DEV) asm volatile(GLDS_HEAD "global_load_lds_dwordx4 %2, %0 sc1" ::"s"(sbase), "s"(m0v), "v"(v0) : "memory");
  else asm volatile(GLDS_HEAD "global_load_lds_dwordx4 %2, %0" ::"s"(sbase), "s"(m0v), "v"(v0) : "memory");
}
template <bool DEV> DI void glds4(const void* sbase, unsigned m0v, unsigned v0, unsigned v1, unsigned v2, unsigned v3) {
  if (DEV)
    asm volatile(GLDS_HEAD "global_load_lds_dwordx4 %2, %0 sc1\n\tglobal_load_lds_dwordx4 %3, %0 offset:1024 sc1\n\t"
                           "global_load_lds_dwordx4 %4, %0 offset:2048 sc1\n\tglobal_load_lds_dwordx4 %5, %0 offset:3072 sc1"
                 ::"s"(sbase), "s"(m0v), "v"(v0), "v"(v1), "v"(v2), "v"(v3) : "memory");
  else
    asm volatile(GLDS_HEAD "global_load_lds_dwordx4 %2, %0\n\tglobal_load_lds_dwordx4 %3, %0 offset:1024\n\t"
                           "global_load_lds_dwordx4 %4, %0 offset:2048\n\tglobal_load_lds_dwordx4 %5, %0 offset:3072"
                 ::"s"(sbase), "s"(m0v), "v"(v0), "v"(v1), "v"(v2), "v"(v3) : "memory");
}
template <int N, bool DEV> DI void glds_lin(const void* sbase, unsigned m0v, unsigned v) {
  if (N == 1) glds1<DEV>(sbase, m0v, v);
  else if (N == 4) glds4<DEV>(sbase, m0v, v, v, v, v);
  else if (N == 2) {
    if (DEV) asm volatile(GLDS_HEAD "global_load_lds_dwordx4 %2, %0 sc1\n\tglobal_load_lds_dwordx4 %2, %0 offset:1024 sc1" ::"s"(sbase), "s"(m0v), "v"(v) : "memory");
    else asm volatile(GLDS_HEAD "global_load_lds_dwordx4 %2, %0\n\tglobal_load_lds_dwordx4 %2, %0 offset:1024" ::"s"(sbase), "s"(m0v), "v"(v) : "memory");
  } else {
    if (DEV) asm volatile(GLDS_HEAD "global_load_lds_dwordx4 %2, %0 sc1\n\tglobal_load_lds_dwordx4 %2, %0 offset:1024 sc1\n\tglobal_load_lds_dwordx4 %2, %0 offset:2048 sc1" ::"s"(sbase), "s"(m0v), "v"(v) : "memory");
    else asm volatile(GLDS_HEAD "global_load_lds_dwordx4 %2, %0\n\tglobal_load_lds_dwordx4 %2, %0 offset:1024\n\tglobal_load_lds_dwordx4 %2, %0 offset:2048" ::"s"(sbase), "s"(m0v), "v"(v) : "memory");
  }
}
DI void glds2(const void* sbase, unsigned m0v, unsigned v0, unsigned v1) {
  asm volatile(GLDS_HEAD "global_load_lds_dwordx4 %2, %0\n\tglobal_load_lds_dwordx4 %3, %0 offset:1024" ::"s"(sbase), "s"(m0v), "v"(v0), "v"(v1) : "memory");
}
#define UNI(x) __builtin_amdgcn_readfirstlane(x)
template <int N> DI void wait_bar() { asm volatile("s_waitcnt vmcnt(%0) lgkmcnt(0)\n\ts_barrier" ::"i"(N) : "memory"); }
template <int N> DI void wait_vbar() { asm volatile("s_waitcnt vmcnt(%0)\n\ts_barrier" ::"i"(N) : "memory"); }
DI void bar_lds() { asm volatile("s_waitcnt lgkmcnt(0)\n\ts_barrier" ::: "memory"); }
typedef short v4i16_t __attribute__((ext_vector_type(4)));
DI bf16x4 lds_tr(lds_cptr p) {
  return __builtin_bit_cast(bf16x4, __builtin_amdgcn_ds_read_tr16_b64_v4i16((LAS v4i16_t*)p));
}
DI bf16x8 lds_tr8(lds_cptr p, int step) {
  const bf16x4 lo = lds_tr(p), hi = lds_tr(p + step);
  return __builtin_shufflevector(lo, hi, 0, 1, 2, 3, 4, 5, 6, 7);
}
DI bf16x8 lds_b128(lds_cptr p) { return *(const LAS bf16x8*)p; }
#define MFMA16(a, b, c) __builtin_amdgcn_mfma_f32_16x16x32_bf16((a), (b), (c), 0, 0, 0)
#define MFMA32(a, b, c) __builtin_amdgcn_mfma_f32_32x32x16_bf16((a), (b), (c), 0, 0, 0)
#define MFMA32K8(a, b, c) __builtin_amdgcn_mfma_f32_32x32x8bf16_1k((a), (b), (c), 0, 0, 0)
#define SBAR() __builtin_amdgcn_sched_barrier(0)
#define PIN(x) asm volatile("" : "+v"(x))
typedef __amdgpu_buffer_rsrc_t rsrc_t;
DI void st8_dev(rsrc_t r, unsigned off, u32x2 v) { __builtin_amdgcn_raw_buffer_store_b64(v, r, off, 0, 16); }
DI u32x4 ld16_dev(rsrc_t r, unsigned off) { return __builtin_amdgcn_raw_buffer_load_b128(r, off, 0, 16); }
DI u32x4 ld16_dev_s(rsrc_t r, unsigned off, unsigned soff) { return __builtin_amdgcn_raw_buffer_load_b128(r, off, soff, 16); }

DI void poll_flags(const char* ws, unsigned off, int n, unsigned tag, int lane, bool& dead) {
  gu32* f = (gu32*)(ws + off);
  unsigned spins = 0;
  while (!dead) {
    unsigned v = tag;
    if (lane < n) v = __hip_atomic_load(f + lane, RLX_AGENT);
    if (__all(v == tag)) break;
    if (++spins > SPIN_CAP) dead = true;
    __builtin_amdgcn_s_sleep(1);
  }
  asm volatile("" ::: "memory");
}
DI void publish(const char* ws, unsigned off, int nrep, unsigned rstride, unsigned tag, int wave, int lane) {
  wait_bar<0>();
  if (wave == 0 && lane < nrep) __hip_atomic_store((gu32*)(ws + off + lane * rstride), tag, RLX_AGENT);
}

DI float fmax3(float a, float b, float c) { return __builtin_fmaxf(__builtin_fmaxf(a, b), c); }
DI float rowmax16(const f32x16& s) {
  float a = fmax3(s[0], s[1], s[2]), b = fmax3(s[3], s[4], s[5]);
  a = fmax3(a, s[6], s[7]); b = fmax3(b, s[8], s[9]); a = fmax3(a, s[10], s[11]); b = fmax3(b, s[12], s[13]);
  a = fmax3(a, s[14], s[15]); a = __builtin_fmaxf(a, b);
  return __builtin_fmaxf(a, __shfl_xor(a, 32));
}
DI void pack_p(const f32x16& p, bf16x8& lo, bf16x8& hi) {
  u32x4 a, b;
#pragma unroll
  for (int i = 0; i < 4; ++i) { a[i] = pack_bf16(p[2 * i], p[2 * i + 1]); b[i] = pack_bf16(p[8 + 2 * i], p[9 + 2 * i]); }
  lo = __builtin_bit_cast(bf16x8, a); hi = __builtin_bit_cast(bf16x8, b);
}
DI float gelu_tanh(float u) {
  const float z = 0.7978845608028654f * (u + 0.044715f * u * u * u);
  const float e = __builtin_amdgcn_exp2f(-2.885390081777927f * z);
  return u * __builtin_amdgcn_rcpf(1.f + e);
}

DI void wslice_block(const u16* w, unsigned lds0, int lw, int lane, int blk) {
  const char* src = (const char*)w + lw * (160 * 1280);
  if (blk < 5) {
    const int r = lane >> 3, pos = lane & 7;
    const unsigned pe = (((pos >> 1) ^ ((r >> 1) & 1)) << 1 | (pos & 1)) * 16;
    const unsigned v0 = r * 1280 + pe, v1 = r * 1280 + (pe ^ 64) + 9216;
    glds4<false>(src + blk * (32 * 1280), lds0 + L_W + lw * 20480 + blk * 4096, v0, v1, v0 + 18432, v1 + 18432);
  } else {
    const unsigned t0 = (lane >> 1) * 1280 + 128 + (lane & 1) * 16;
    if (blk == 5) glds4<false>(src, lds0 + L_WT + lw * 5120, t0, t0 + 39936, t0 + 2 * 39936, t0 + 3 * 39936);
    else glds1<false>(src + 128 * 1280, lds0 + L_WT + lw * 5120 + 4096, t0);
  }
}

DI void touch(const void* sbase, unsigned voff, unsigned& sink) {
  asm volatile("s_nop 4\n\tglobal_load_dword %0, %1, %2" : "+v"(sink) : "v"(voff), "s"(sbase) : "memory");
}
DI float xsum16(float x) {
  auto r = __builtin_amdgcn_permlane16_swap(__float_as_uint(x), __float_as_uint(x), false, false);
  return __uint_as_float(r[0]) + __uint_as_float(r[1]);
}
DI float xsum32(float x) {
  auto r = __builtin_amdgcn_permlane32_swap(__float_as_uint(x), __float_as_uint(x), false, false);
  return __uint_as_float(r[0]) + __uint_as_float(r[1]);
}
DI f32x4 ldf4(lds_cptr p) { return *(const LAS f32x4*)p; }

DI f32x4 sum4(lds_cptr lds, int t, int nt, int lane) {
  f32x4 s = *(const LAS f32x4*)(lds + t * 1024 + lane * 16);
#pragma unroll
  for (int k = 1; k < 4; ++k) s += *(const LAS f32x4*)(lds + (k * nt + t) * 1024 + lane * 16);
  return s;
}
DI void ln_finish(float s1, float s2, float& mu, float& var) { mu = s1 * (1.f / D_); var = fmaxf(s2 * (1.f / D_) - mu * mu, 0.f); }

constexpr int ATT_CH = 4;
constexpr int ATT_CHB = ATT_CH * KV_TILE;
constexpr int ATT_SLOT = 10752;
constexpr int ATT_NCH = S_ / 32 / ATT_CH;
constexpr int w2_slot(int st) { return (st % 9) < 4 ? L_RING + (st % 9) * 16384 : ((st % 9) - 4) * 16384; }
template <int N> struct IC { static constexpr int v = N; };
template <int I, int N, class F> DI void static_for(F&& f) { if constexpr (I < N) { f(IC<I>{}); static_for<I + 1, N>(f); } }


__global__ __launch_bounds__(512, 2) void mega_kernel(const u16* __restrict__ x0, const u16* __restrict__ wq, const u16* __restrict__ wk,
                                                      const u16* __restrict__ wv, const u16* __restrict__ wo, const u16* __restrict__ w1,
                                                      const u16* __restrict__ b1, const u16* __restrict__ w2, const u16* __restrict__ b2,
                                                      u16* __restrict__ out, char* ws) {
  extern __shared__ __attribute__((aligned(16))) char smem[];
  const int wave0 = UNI(threadIdx.x >> 6);
  const unsigned lds0 = (unsigned)(uintptr_t)smem;
  const lds_cptr lds = (lds_cptr)smem;
  const lds_ptr ldw = (lds_ptr)smem;
#define FRESH() \
  unsigned ones_ = ~0u; asm volatile("" : "+s"(ones_)); \
  const int lane = __builtin_amdgcn_mbcnt_hi(ones_, __builtin_amdgcn_mbcnt_lo(ones_, 0u)); \
  int wave = wave0; asm volatile("" : "+s"(wave)); \
  int bid = blockIdx.x; asm volatile("" : "+s"(bid)); \
  const int xcd = bid & 7, idx = bid >> 3, tb = xcd * 4 + (idx >> 3), j = idx & 7; \
  const int g = lane >> 4, c16 = lane & 15, kq = wave & 3, th = wave >> 2; \
  const int ea = wave & 3, ew0 = th ? 3 : 0, ewn = th ? 2 : 3; \
  const unsigned res_off = L_RES + (ea * 16 + c16) * 160 + 8 * g; \
  const int panel4 = (xcd & 1) * 4 + (idx & 3), ft = (xcd >> 1) * 7 + (idx >> 2); \
  const bool has4 = idx < 28 && ft < 27; \
  (void)xcd; (void)g; (void)c16; (void)kq; (void)th; (void)ea; (void)ew0; (void)ewn; (void)res_off; (void)tb; (void)j; (void)panel4; (void)has4
  const rsrc_t rws = __builtin_amdgcn_make_buffer_rsrc(ws, 0, WS_END, 0x00020000);
  unsigned tag = 0;
  bool dead = false;

#pragma unroll 1
  for (int layer = 0; layer < 2; ++layer) {
    const unsigned fl = WS_FLAGS + layer * FL_LAYER;
    const unsigned kvoff = WS_KV + layer * KV_LAYER;
    unsigned sink = 0;
    {
      FRESH();
      auto load_kvw = [&]() {
        const char* xs = (const char*)((j & 1) ? wv : wk);
        if (wave < 3) {
          glds_lin<4, false>(xs + wave * 8192, lds0 + L_ATT + wave * 8192, lane * 16);
          glds_lin<4, false>(xs + wave * 8192 + 4096, lds0 + L_ATT + wave * 8192 + 4096, lane * 16);
        } else glds1<false>(xs + 24576, lds0 + L_ATT + 24576, lane * 16);
      };
      u32x4 af[5][2];
      if (layer == 0) {
        const rsrc_t ra = __builtin_amdgcn_make_buffer_rsrc((void*)x0, 0, XB, 0x00020000);
        const unsigned abase = (tb * 64 + th * 32 + c16) * (D_ * 2) + kq * 64 + g * 16;
#pragma unroll
        for (int s = 0; s < 5; ++s)
#pragma unroll
          for (int t = 0; t < 2; ++t) af[s][t] = __builtin_amdgcn_raw_buffer_load_b128(ra, abase + s * 256, t * (16 * D_ * 2), 0);
        const unsigned epoch = __hip_atomic_load((gu32*)(ws + WS_EPOCH), RLX_AGENT);
        if (wave < 4) load_kvw();
        else touch(wq, min((idx * 4 + kq) * 50 + lane, 6399) * 128, sink);
        wait_vbar<0>();
        tag = UNI(epoch) + 1u;
        if (wave >= 4) {
#pragma unroll 1
          for (int blk = 0; blk < 7; ++blk) wslice_block(wq + j * 80, lds0, kq, lane, blk);
        } else {
#pragma unroll 1
          for (int pc = wave; pc < 10; pc += 4) {
            const int n = pc * 64 + lane, row = n / 10, ch = n - row * 10;
            glds1<false>(x0 + (long)tb * 64 * D_ + j * 80, lds0 + L_RES + pc * 1024, row * 1280 + ch * 16);
          }
        }
      } else {
        if (wave >= 4) {
#pragma unroll 1
          for (int blk = 0; blk < 7; ++blk) wslice_block(wq + j * 80, lds0, kq, lane, blk);
        } else load_kvw();
        if (wave == 0) poll_flags(ws, WS_FLAGS + FL_X + tb * 128, 8, tag, lane, dead);
        wait_vbar<0>();
        const unsigned abase = WS_X + tb * 81920 + (kq * 4 + th * 2) * 1024 + lane * 16;
#pragma unroll
        for (int s = 0; s < 5; ++s)
#pragma unroll
          for (int t = 0; t < 2; ++t) af[s][t] = ld16_dev_s(rws, abase + t * 1024, s * 16384);
      }
      bf16x8 ones;
#pragma unroll
      for (int i = 0; i < 8; ++i) ones[i] = (short)0x3f80;
      const f32x4 z4 = {0.f, 0.f, 0.f, 0.f};
      const int q = c16 >> 2, p = lane & 3;
      const int row = 32 * kq + 8 * g + q;
      const unsigned fsw = (((q >> 1) & 1) | ((g & 1) << 1)) * 32;
      const unsigned woff = L_W + row * 128 + 8 * p, toff = L_WT + row * 32 + 8 * p, xoff = L_ATT + row * 40 + 8 * p;
      LAS float* stat = (LAS float*)(ldw + L_DUMP);
      LAS float* csum = (LAS float*)(ldw + L_KVP + 20480);
      const int tk = j >> 1;
      const bool isv = j & 1;
      {
        f32x4 a1s[2] = {z4, z4}, a2s[2] = {z4, z4}, akv[2] = {z4, z4}, aok[2] = {z4, z4};
#pragma unroll
        for (int s = 0; s < 5; ++s) {
          const bf16x8 a0 = __builtin_bit_cast(bf16x8, af[s][0]), a1 = __builtin_bit_cast(bf16x8, af[s][1]);
          if (th == (tk >> 1)) {
            const bf16x8 a = (tk & 1) ? a1 : a0;
            const bf16x8 x0f = lds_tr8(lds + xoff + s * 5120, 160), x1f = lds_tr8(lds + xoff + s * 5120 + 32, 160);
            if (!isv) {
              akv[0] = MFMA16(x0f, a, akv[0]); akv[1] = MFMA16(x1f, a, akv[1]);
              aok[0] = MFMA16(x0f, ones, aok[0]); aok[1] = MFMA16(x1f, ones, aok[1]);
            } else {
              akv[0] = MFMA16(a, x0f, akv[0]); akv[1] = MFMA16(a, x1f, akv[1]);
              aok[0] = MFMA16(ones, x0f, aok[0]); aok[1] = MFMA16(ones, x1f, aok[1]);
            }
          }
          a1s[0] = MFMA16(ones, a0, a1s[0]); a1s[1] = MFMA16(ones, a1, a1s[1]);
          a2s[0] = MFMA16(a0, a0, a2s[0]); a2s[1] = MFMA16(a1, a1, a2s[1]);
        }
        if (g == q) {
#pragma unroll
          for (int t = 0; t < 2; ++t) {
            const float d = (p & 2) ? ((p & 1) ? a2s[t][3] : a2s[t][2]) : ((p & 1) ? a2s[t][1] : a2s[t][0]);
            *(LAS f32x2*)(stat + (kq * 64 + th * 32 + t * 16 + c16) * 2) = f32x2{a1s[t][0], d};
          }
        }
        if (th == (tk >> 1)) {
          const bool l2 = isv ? (c16 < 4) : (g == 0);
          const int i2 = isv ? g * 4 + c16 : c16;
          const int o = L_KVP + kq * 2560;
          *(LAS f32x4*)(ldw + o + lane * 16) = akv[0];
          *(LAS f32x4*)(ldw + o + 1280 + lane * 16) = aok[0];
          if (l2) { *(LAS f32x4*)(ldw + o + 1024 + i2 * 16) = akv[1]; *(LAS f32x4*)(ldw + o + 2304 + i2 * 16) = aok[1]; }
        }
      }
      wait_bar<0>();
      float mu, rs;
      {
        f32x2 sv[4];
#pragma unroll
        for (int k = 0; k < 4; ++k) sv[k] = *(const LAS f32x2*)(stat + (k * 64 + ea * 16 + c16) * 2);
        float var; ln_finish((sv[0][0] + sv[1][0]) + (sv[2][0] + sv[3][0]), (sv[0][1] + sv[1][1]) + (sv[2][1] + sv[3][1]), mu, var);
        mu = rnd1(mu); rs = ln_rsd(var);
      }
      if (ea == tk) {
        const int li = th == 0 ? lane : 64 + (isv ? g * 4 + (c16 & 3) : c16);
        f32x4 s = z4, cs = z4;
#pragma unroll
        for (int k = 0; k < 4; ++k) { s += ldf4(lds + L_KVP + k * 2560 + li * 16); cs += ldf4(lds + L_KVP + k * 2560 + 1280 + li * 16); }
        const unsigned tile = kvoff + (tb * 2 + (ea >> 1)) * KV_TILE;
        if (!isv) {
          u32x2 w; w[0] = pack_bf16((s[0] - mu * cs[0]) * rs, (s[1] - mu * cs[1]) * rs); w[1] = pack_bf16((s[2] - mu * cs[2]) * rs, (s[3] - mu * cs[3]) * rs);
          const int r32 = (ea & 1) * 16 + c16;
          if (th == 0) st8_dev(rws, tile + ((g >> 1) * 32 + r32) * 16 + (g & 1) * 8, w);
          else if (g == 0) st8_dev(rws, tile + KV_KT + r32 * 8, w);
        } else {
          float o[4];
#pragma unroll
          for (int e = 0; e < 4; ++e) o[e] = (s[e] - __shfl(mu, 4 * g + e) * cs[e]) * __shfl(rs, 4 * g + e);
          const int d = th * 16 + c16;
          u32x2 w; w[0] = pack_bf16(o[0], o[1]); w[1] = pack_bf16(o[2], o[3]);
          if (d == DH_) { w[0] = 0x3f803f80u; w[1] = 0x3f803f80u; }
          if (d == DH_ + 1) { w[0] = 0u; w[1] = 0u; }
          if (d <= DH_ + 1) st8_dev(rws, tile + KV_V + (ea & 1) * KV_VS + ((g & 1) * 22 + d) * 16 + (g >> 1) * 8, w);
        }
      }
      f32x4 acc[5][2], aon[3] = {z4, z4, z4};
#pragma unroll
      for (int i = 0; i < 5; ++i) { acc[i][0] = z4; acc[i][1] = z4; }
      bf16x8 wq2[2][5];
#pragma unroll
      for (int i = 0; i < 5; ++i) wq2[0][i] = i < 4 ? lds_tr8(lds + woff + ((i * 32) ^ fsw), 512) : lds_tr8(lds + toff, 128);
#pragma unroll
      for (int s = 0; s < 5; ++s) {
        const bf16x8 a0 = __builtin_bit_cast(bf16x8, af[s][0]), a1 = __builtin_bit_cast(bf16x8, af[s][1]);
        const bf16x8* wf = wq2[s & 1];
#pragma unroll
        for (int i = 0; i < 5; ++i) {
          if (s < 4) wq2[(s + 1) & 1][i] = i < 4 ? lds_tr8(lds + woff + (s + 1) * 16384 + ((i * 32) ^ fsw), 512) : lds_tr8(lds + toff + (s + 1) * 4096, 128);
          acc[i][0] = MFMA16(wf[i], a0, acc[i][0]); acc[i][1] = MFMA16(wf[i], a1, acc[i][1]);
          SBAR();
        }
        if (th == 0) { aon[0] = MFMA16(wf[0], ones, aon[0]); aon[1] = MFMA16(wf[1], ones, aon[1]); aon[2] = MFMA16(wf[2], ones, aon[2]); }
        else { aon[0] = MFMA16(wf[3], ones, aon[0]); aon[1] = MFMA16(wf[4], ones, aon[1]); }
        if (s == 1) {
          publish(ws, fl + FL_KV + (tb * 8 + j) * 4, 32, 1024, tag, wave, lane);
          if (wave >= 4) {
            touch((const char*)(w2 + tb * 64) + (long)(j * 216 + kq * 54) * (S_ * 2), (lane < 54 ? lane : 53) * (S_ * 2), sink);
          }
        }
      }
      bar_lds();
#pragma unroll
      for (int i = 0; i < 5; ++i)
#pragma unroll
        for (int t = 0; t < 2; ++t) *(LAS f32x4*)(ldw + ((kq * 5 + i) * 4 + th * 2 + t) * 1024 + lane * 16) = acc[i][t];
      if (c16 == 0) {
#pragma unroll
        for (int i = 0; i < 3; ++i) if (i < ewn) *(LAS f32x4*)(csum + (kq * 7 + ew0 + i) * 16 + 4 * g) = aon[i];
      }
      bar_lds();
      {
        const float r = rs * QSCALE;
        f32x4 s[3], cs[3];
#pragma unroll
        for (int i = 0; i < 3; ++i) {
          const int wt = min(ew0 + i, 4);
          s[i] = sum4(lds, wt * 4 + ea, 20, lane);
          cs[i] = z4;
#pragma unroll
          for (int k = 0; k < 4; ++k) cs[i] += *(const LAS f32x4*)(csum + (k * 7 + wt) * 16 + 4 * g);
        }
#pragma unroll
        for (int i = 0; i < 3; ++i) {
          if (i < ewn) {
            u32x2 w;
            w[0] = pack_bf16((s[i][0] - mu * cs[i][0]) * r, (s[i][1] - mu * cs[i][1]) * r);
            w[1] = pack_bf16((s[i][2] - mu * cs[i][2]) * r, (s[i][3] - mu * cs[i][3]) * r);
            *(LAS u32x2*)(ldw + L_QT + (ea * 16 + c16) * 160 + (ew0 + i) * 32 + 8 * g) = w;
          }
        }
      }
      bar_lds();
    }
    bf16x8 w1r[8][4];
    {
      FRESH();
      const int r32 = lane & 31, hi = lane >> 5;
      const int hh = wave & 3, trow = th * 32 + r32;
      bf16x8 q16; bf16x4 q4;
      {
        const lds_cptr qp = lds + L_QT + trow * 160 + hh * 40;
        const u32x2 a = *(const LAS u32x2*)(qp + hi * 16), b = *(const LAS u32x2*)(qp + hi * 16 + 8), c = *(const LAS u32x2*)(qp + 32);
        u32x4 v = {a[0], a[1], b[0], b[1]};
        q16 = __builtin_bit_cast(bf16x8, v);
        u32x2 t = {hi ? 0u : c[0], hi ? 0u : c[1]};
        q4 = __builtin_bit_cast(bf16x4, t);
        PIN(q16); PIN(q4);
      }
      if (wave == 0) {
        unsigned spins = 0;
        while (!dead) {
          const u32x4 v = ld16_dev(rws, fl + FL_KV + (bid & 31) * 1024 + lane * 16);
          if (__all(v[0] == tag && v[1] == tag && v[2] == tag && v[3] == tag)) break;
          if (++spins > SPIN_CAP) dead = true;
          __builtin_amdgcn_s_sleep(1);
          asm volatile("" ::: "memory");
        }
      }
      bar_lds();
      const unsigned koff = lane * 16;
      const char* KV = ws + kvoff + wave * 3072;
      auto issue_kv = [&](int ch) {
        const unsigned dst = lds0 + L_ATT + (ch % 3) * ATT_SLOT + wave * 3072;
        if (wave < 3) glds_lin<3, true>(KV + ch * ATT_CHB, dst, koff);
        else { glds1<true>(KV + ch * ATT_CHB, dst, koff); if (lane < 32) glds1<true>(KV + ch * ATT_CHB + 1024, dst + 1024, koff); }
      };
      const char* w1s = (const char*)(w1 + (has4 ? ft : 0) * 64) + (long)wave * 16 * (F_ * 2);
      unsigned sv0, sv1;
      {
        const int r = lane >> 3, pos = lane & 7;
        const unsigned pe = (((pos >> 1) ^ ((r >> 1) & 1)) << 1 | (pos & 1)) * 16;
        sv0 = r * (F_ * 2) + pe; sv1 = (r + 8) * (F_ * 2) + (pe ^ 64) - 1024;
      }
      const unsigned fsw = (((c16 >> 3) & 1) | ((g & 1) << 1)) * 32;
      const unsigned srow = (th ? L_WT : L_STG) + (32 * kq + 8 * g + (c16 >> 2)) * 128 + 8 * (lane & 3);
      if (wave < 4) { issue_kv(0); issue_kv(1); }
      glds2(w1s, lds0 + L_STG + wave * 2048, sv0, sv1);
      glds2(w1s + 128L * (F_ * 2), lds0 + L_WT + wave * 2048, sv0, sv1);
      const unsigned ktoff = KV_KT + r32 * 8, voff = KV_V + (hi * 22 + (r32 < 21 ? r32 : 21)) * 16;
      f32x16 o = {}, negm, sA, sB;
      bf16x8 k16, v0, v1; bf16x4 k4; u32x4 p0, p1;
      if (wave < 3) wait_bar<7>(); else if (wave == 3) wait_bar<6>(); else bar_lds();
      const lds_cptr ring = lds + L_ATT;
      {
        k16 = lds_b128(ring + koff);
        k4 = *(const LAS bf16x4*)(ring + ktoff);
        f32x16 z = {};
        z = MFMA32(k16, q16, z); z = MFMA32K8(k4, q4, z);
        k16 = lds_b128(ring + KV_TILE + koff);
        k4 = *(const LAS bf16x4*)(ring + KV_TILE + ktoff);
        v1 = lds_b128(ring + voff);
        const float m = rowmax16(z);
#pragma unroll
        for (int i = 0; i < 16; ++i) { negm[i] = -m; sA[i] = z[i] - m; }
        p1 = u32x4{0u, 0u, 0u, 0u};
      }
#define EX(x) __builtin_amdgcn_exp2f(x)
#define EXP4(X, i) do { X[i] = EX(X[i]); X[i + 1] = EX(X[i + 1]); X[i + 2] = EX(X[i + 2]); X[i + 3] = EX(X[i + 3]); } while (0)
#define BF8(x) __builtin_bit_cast(bf16x8, x)
#define PK4(P, X, i) do { P[0] = pack_bf16(X[i], X[i + 1]); P[1] = pack_bf16(X[i + 2], X[i + 3]); P[2] = pack_bf16(X[i + 4], X[i + 5]); P[3] = pack_bf16(X[i + 6], X[i + 7]); } while (0)
#define STEP(C, N, kn, vc) do { \
    N = MFMA32(k16, q16, negm); SBAR(); \
    v0 = lds_b128((vc) + voff); EXP4(C, 0); PIN(C); SBAR(); \
    o = MFMA32(v1, BF8(p1), o); SBAR(); \
    k16 = lds_b128((kn) + koff); EXP4(C, 4); PIN(C); SBAR(); PK4(p0, C, 0); PIN(p0); SBAR(); \
    N = MFMA32K8(k4, q4, N); SBAR(); \
    v1 = lds_b128((vc) + voff + KV_VS); EXP4(C, 8); PIN(C); SBAR(); \
    o = MFMA32(v0, BF8(p0), o); SBAR(); \
    k4 = *(const LAS bf16x4*)((kn) + ktoff); EXP4(C, 12); PIN(C); SBAR(); PK4(p1, C, 8); PIN(p1); SBAR(); \
  } while (0)
      static_for<0, ATT_NCH>([&](auto CH) {
        constexpr int ch = decltype(CH)::v;
        const lds_cptr cb = ring + (ch % 3) * ATT_SLOT;
        const lds_cptr nb = (ch + 1 < ATT_NCH) ? ring + ((ch + 1) % 3) * ATT_SLOT : cb;
        __builtin_amdgcn_s_setprio(1);
        STEP(sA, sB, cb + 2 * KV_TILE, cb);
        __builtin_amdgcn_s_setprio(0);
        STEP(sB, sA, cb + 3 * KV_TILE, cb + KV_TILE);
        if (wave < 4) wait_vbar<(ch >= 1 && ch <= 5) ? 4 : 0>(); else wait_vbar<0>();
        if (ch + 2 < ATT_NCH) { if (wave < 4) issue_kv(ch + 2); }
        if ((ch & 1) && ch + 1 < 16) {
          glds2(w1s + (long)(ch + 1) * 128 * (F_ * 2), lds0 + L_STG + wave * 2048, sv0, sv1);
          glds2(w1s + (long)(ch + 2) * 128 * (F_ * 2), lds0 + L_WT + wave * 2048, sv0, sv1);
        }
        if (ch < 5) { if (wave < 4) wslice_block(wo + j * 80, lds0, wave, lane, ch); }
        if constexpr ((ch & 1) == 0) {
#pragma unroll
          for (int i = 0; i < 4; ++i) w1r[ch >> 1][i] = lds_tr8(lds + srow + ((32 * i) ^ fsw), 512);
        }
        __builtin_amdgcn_s_setprio(3);
        SBAR();
        STEP(sA, sB, nb, cb + 2 * KV_TILE);
        if constexpr ((ch & 1) == 0) { PIN(w1r[ch >> 1][0]); PIN(w1r[ch >> 1][1]); PIN(w1r[ch >> 1][2]); PIN(w1r[ch >> 1][3]); }
        __builtin_amdgcn_s_setprio(2);
        SBAR();
        STEP(sB, sA, nb + KV_TILE, cb + 3 * KV_TILE);
      });
      __builtin_amdgcn_s_setprio(0);
      o = MFMA32(v1, BF8(p1), o);
      float l = xsum32(hi ? o[8] : 0.f);
      bool bad = !(l < 1.8446744e19f);
#pragma unroll
      for (int i = 0; i < 12; ++i) bad |= !(__builtin_fabsf(o[i]) < 3.0e38f);
      if (__builtin_expect(__any(bad), 0)) {
        float m = -INFINITY;
        o = f32x16{};
#pragma unroll 1
        for (int t = 0; t < S_ / 32; ++t) {
          const unsigned tp = kvoff + t * KV_TILE;
          const bf16x8 a16 = BF8(ld16_dev(rws, tp + koff));
          const u32x2 a4 = __builtin_amdgcn_raw_buffer_load_b64(rws, tp + ktoff, 0, 16);
          const bf16x8 w0 = BF8(ld16_dev(rws, tp + voff)), w1f = BF8(ld16_dev(rws, tp + voff + KV_VS));
          f32x16 z = {};
          z = MFMA32(a16, q16, z); z = MFMA32K8(__builtin_bit_cast(bf16x4, a4), q4, z);
          const float mn = __builtin_fmaxf(m, rowmax16(z));
          const float alpha = __builtin_amdgcn_exp2f(m - mn);
          m = mn;
#pragma unroll
          for (int i = 0; i < 16; ++i) { z[i] = __builtin_amdgcn_exp2f(z[i] - m); o[i] *= alpha; }
          bf16x8 a0, a1; pack_p(z, a0, a1);
          o = MFMA32(w0, a0, o); o = MFMA32(w1f, a1, o);
        }
        l = xsum32(hi ? o[8] : 0.f);
      }
      const float rl = __builtin_amdgcn_rcpf(l);
      const unsigned ob = WS_O + layer * XB + tb * 81920 + ((trow >> 4) * 64 + (trow & 15)) * 16;
#pragma unroll
      for (int gq = 0; gq < 3; ++gq) {
        const int d0 = 8 * gq + 4 * hi;
        if (d0 < DH_) {
          const int k = (j * 4 + hh) * DH_ + d0;
          u32x2 w; w[0] = pack_bf16(o[4 * gq] * rl, o[4 * gq + 1] * rl); w[1] = pack_bf16(o[4 * gq + 2] * rl, o[4 * gq + 3] * rl);
          st8_dev(rws, ob + (k >> 5) * 4096 + ((k & 31) >> 3) * 256 + (k & 7) * 2, w);
        }
      }
      publish(ws, fl + FL_O + tb * 128 + j * 4, 1, 0, tag, wave, lane);
      asm volatile("" :: "v"(sink));
      if (wave >= 4) { wslice_block(wo + j * 80, lds0, kq, lane, 5); wslice_block(wo + j * 80, lds0, kq, lane, 6); }
    }
    {
      FRESH();
      if (wave == 0) poll_flags(ws, fl + FL_O + tb * 128, 8, tag, lane, dead);
      wait_vbar<0>();
      const unsigned abase = WS_O + layer * XB + tb * 81920 + (kq * 4 + th * 2) * 1024 + lane * 16;
      u32x4 af[5][2];
#pragma unroll
      for (int s = 0; s < 5; ++s)
#pragma unroll
        for (int t = 0; t < 2; ++t) af[s][t] = ld16_dev_s(rws, abase + t * 1024, s * 16384);
      f32x4 acc[5][2];
#pragma unroll
      for (int i = 0; i < 5; ++i) { acc[i][0] = f32x4{0.f, 0.f, 0.f, 0.f}; acc[i][1] = acc[i][0]; }
      const int q = c16 >> 2, p = lane & 3;
      const int row = 32 * kq + 8 * g + q;
      const unsigned fsw = (((q >> 1) & 1) | ((g & 1) << 1)) * 32;
      const unsigned woff = L_W + row * 128 + 8 * p, toff = L_WT + row * 32 + 8 * p;
      bf16x8 wf[2][5];
#pragma unroll
      for (int i = 0; i < 5; ++i) wf[0][i] = i < 4 ? lds_tr8(lds + woff + ((i * 32) ^ fsw), 512) : lds_tr8(lds + toff, 128);
#pragma unroll
      for (int s = 0; s < 5; ++s) {
        const bf16x8 a0 = __builtin_bit_cast(bf16x8, af[s][0]), a1 = __builtin_bit_cast(bf16x8, af[s][1]);
#pragma unroll
        for (int i = 0; i < 5; ++i) {
          if (s < 4) wf[(s + 1) & 1][i] = i < 4 ? lds_tr8(lds + woff + (s + 1) * 16384 + ((i * 32) ^ fsw), 512) : lds_tr8(lds + toff + (s + 1) * 4096, 128);
          acc[i][0] = MFMA16(wf[s & 1][i], a0, acc[i][0]); acc[i][1] = MFMA16(wf[s & 1][i], a1, acc[i][1]);
          SBAR();
        }
      }
      bar_lds();
#pragma unroll
      for (int i = 0; i < 5; ++i)
#pragma unroll
        for (int t = 0; t < 2; ++t) *(LAS f32x4*)(ldw + ((kq * 5 + i) * 4 + th * 2 + t) * 1024 + lane * 16) = acc[i][t];
      bar_lds();
      gu64* gr = (gu64*)(ws + WS_STAT + layer * ST_LAYER + (tb * 4 + ea) * 4096);
      {
        f32x4 s[3]; u32x2 r[3];
#pragma unroll
        for (int i = 0; i < 3; ++i) {
          const int wt = min(ew0 + i, 4);
          s[i] = sum4(lds, wt * 4 + ea, 20, lane);
          r[i] = *(const LAS u32x2*)(lds + res_off + wt * 32);
        }
        float s1 = 0.f, s2 = 0.f;
#pragma unroll
        for (int i = 0; i < 3; ++i) {
          if (i < ewn) {
            u32x2 w;
            w[0] = pack_bf16(bf_lo(r[i][0]) + s[i][0], bf_hi(r[i][0]) + s[i][1]);
            w[1] = pack_bf16(bf_lo(r[i][1]) + s[i][2], bf_hi(r[i][1]) + s[i][3]);
            *(LAS u32x2*)(ldw + res_off + (ew0 + i) * 32) = w;
            const float a = bf_lo(w[0]), b = bf_hi(w[0]), c = bf_lo(w[1]), d = bf_hi(w[1]);
            s1 += (a + b) + (c + d);
            s2 += (a * a + b * b) + (c * c + d * d);
          }
        }
        s1 = xsum32(xsum16(s1)); s2 = xsum32(xsum16(s2));
        if (lane < 32) __hip_atomic_store(gr + (2 * j + th) * 32 + lane, ((unsigned long long)tag << 32) | __float_as_uint(g ? s2 : s1), RLX_AGENT);
      }
      bar_lds();
      if (wave >= 4) {
        const int r = lane >> 3, pos = lane & 7;
        const unsigned pe = (((pos >> 1) ^ ((r >> 1) & 1)) << 1 | (pos & 1)) * 16;
        const unsigned v0 = r * (S_ * 2) + pe + 4096, v1 = (r + 8) * (S_ * 2) + (pe ^ 64) + 3072, vstep = 16 * (S_ * 2) - 2048;
        const char* wsrc = (const char*)(w2 + tb * 64) - 4096 + (long)kq * 32 * (S_ * 2);
#pragma unroll 1
        for (int st = 0; st < 4; ++st) glds4<false>(wsrc + (long)st * 128 * (S_ * 2), lds0 + L_RING + st * 16384 + kq * 4096, v0, v1, v0 + vstep, v1 + vstep);
        asm volatile("s_barrier" ::: "memory");
      } else {
        u32x2 xv[5];
#pragma unroll
        for (int wt = 0; wt < 5; ++wt) xv[wt] = __builtin_bit_cast(u32x2, lds_tr(lds + L_RES + (ea * 16 + 4 * g + q) * 160 + wt * 32 + 8 * p));
        float tot;
        {
          unsigned spins = 0;
          for (;;) {
            bool ok = true; tot = 0.f;
#pragma unroll
            for (int i = 0; i < 8; ++i) {
              const unsigned long long v = __hip_atomic_load(gr + i * 64 + lane, RLX_AGENT);
              tot += __uint_as_float((unsigned)v); ok &= (unsigned)(v >> 32) == tag;
            }
            if (__all(ok) || dead) break;
            if (++spins > SPIN_CAP) dead = true;
            __builtin_amdgcn_s_sleep(1);
          }
        }
        tot = xsum32(tot);
        float mu4[4], rr4[4], m24[4], r24[4];
#pragma unroll
        for (int e = 0; e < 4; ++e) { mu4[e] = __shfl(tot, 4 * g + e); rr4[e] = __shfl(tot, 16 + 4 * g + e); }
#pragma unroll
        for (int e = 0; e < 4; ++e) {
          float mu, var; ln_finish(mu4[e], rr4[e], mu, var);
          mu4[e] = rnd1(mu); rr4[e] = ln_rsd(var);
          m24[e] = rnd1((mu - mu4[e]) * rr4[e]); r24[e] = ln_rsd(var * rr4[e] * rr4[e]);
        }
        const unsigned hb = WS_H + layer * XB + j * 327680 + ((tb * 2 + (ea >> 1)) * 5) * 1024 + (c16 + 16 * ((ea & 1) * 2 + (g >> 1))) * 16 + (g & 1) * 8;
#pragma unroll
        for (int wt = 0; wt < 5; ++wt) {
          const u32x2 v = xv[wt];
          u32x2 w;
          w[0] = pack_bf16(bf_lo(v[0]) - mu4[0], bf_hi(v[0]) - mu4[1]); w[1] = pack_bf16(bf_lo(v[1]) - mu4[2], bf_hi(v[1]) - mu4[3]);
          w[0] = pack_bf16(bf_lo(w[0]) * rr4[0], bf_hi(w[0]) * rr4[1]); w[1] = pack_bf16(bf_lo(w[1]) * rr4[2], bf_hi(w[1]) * rr4[3]);
          w[0] = pack_bf16(bf_lo(w[0]) - m24[0], bf_hi(w[0]) - m24[1]); w[1] = pack_bf16(bf_lo(w[1]) - m24[2], bf_hi(w[1]) - m24[3]);
          w[0] = pack_bf16(bf_lo(w[0]) * r24[0], bf_hi(w[0]) * r24[1]); w[1] = pack_bf16(bf_lo(w[1]) * r24[2], bf_hi(w[1]) * r24[3]);
          st8_dev(rws, hb + wt * 1024, w);
        }
        publish(ws, fl + FL_H + j * 512 + tb * 4, 4, 128, tag, wave, lane);
      }
    }
    {
      FRESH();
      if (has4) {
        const int f = ft * 64 + ea * 16 + 4 * g;
        const u32x2 bias = *(const u32x2*)(b1 + f);
        if (wave == 0) poll_flags(ws, fl + FL_H + panel4 * 512 + ((idx >> 2) & 3) * 128, 32, tag, lane, dead);
        asm volatile("s_barrier" ::: "memory");
        const unsigned l16 = lane * 16;
        const unsigned abase = WS_H + layer * XB + panel4 * 327680 + th * 20480 + kq * 5120 + l16;
        u32x4 af[2][5];
#pragma unroll
        for (int i = 0; i < 2; ++i)
#pragma unroll
          for (int a = 0; a < 5; ++a) af[i][a] = ld16_dev_s(rws, abase + (a & 3) * 1024, i * 40960 + (a >> 2) * 4096);
        f32x4 acc[4][5];
#pragma unroll
        for (int i = 0; i < 4; ++i) {
#pragma unroll
          for (int a = 0; a < 5; ++a) acc[i][a] = MFMA16(w1r[0][i], __builtin_bit_cast(bf16x8, af[0][a]), (f32x4{0.f, 0.f, 0.f, 0.f}));
          SBAR();
        }
#pragma unroll
        for (int a = 0; a < 5; ++a) af[0][a] = ld16_dev_s(rws, abase + (a & 3) * 1024, 2 * 40960 + (a >> 2) * 4096);
        SBAR();
#pragma unroll
        for (int st = 1; st < 8; ++st) {
#pragma unroll
          for (int a = 0; a < 5; ++a) {
            const bf16x8 x = __builtin_bit_cast(bf16x8, af[st & 1][a]);
#pragma unroll
            for (int i = 0; i < 4; ++i) acc[i][a] = MFMA16(w1r[st][i], x, acc[i][a]);
            SBAR();
            if (st + 2 < 8) af[st & 1][a] = ld16_dev_s(rws, abase + (a & 3) * 1024, (st + 2) * 40960 + (a >> 2) * 4096);
            SBAR();
          }
        }
        {
          const lds_ptr pb = ldw + kq * 20480 + l16;
          auto xchg = [&](auto TH) {
            constexpr int t = decltype(TH)::v;
#pragma unroll
            for (int i = 0; i < 2; ++i)
#pragma unroll
              for (int a = 0; a < 5; ++a) *(LAS f32x4*)(pb + ((2 * (1 - t) + i) * 5 + a) * 1024) = acc[2 * (1 - t) + i][a];
            bar_lds();
#pragma unroll
            for (int i = 0; i < 2; ++i)
#pragma unroll
              for (int a = 0; a < 5; ++a) acc[2 * t + i][a] += *(const LAS f32x4*)(pb + ((2 * t + i) * 5 + a) * 1024);
#pragma unroll
            for (int i = 0; i < 2; ++i)
#pragma unroll
              for (int a = 0; a < 5; ++a) *(LAS f32x4*)(pb + ((2 * t + i) * 5 + a) * 1024) = acc[2 * t + i][a];
          };
          if (th == 0) xchg(IC<0>{}); else xchg(IC<1>{});
        }
        bar_lds();
        const unsigned ub = WS_U + layer * UB + ((panel4 * U_KS + (f >> 5)) * 5) * 1024 + (c16 + 16 * ((f & 31) >> 3)) * 16 + (f & 7) * 2;
        f32x4 sv[3];
#pragma unroll
        for (int i = 0; i < 3; ++i) sv[i] = sum4(lds, ea * 5 + min(ew0 + i, 4), 20, lane);
#pragma unroll
        for (int i = 0; i < 3; ++i) {
          if (i < ewn) {
            const f32x4 s = sv[i];
            u32x2 w;
            w[0] = pack_bf16(gelu_tanh(s[0] + bf_lo(bias[0])), gelu_tanh(s[1] + bf_hi(bias[0])));
            w[1] = pack_bf16(gelu_tanh(s[2] + bf_lo(bias[1])), gelu_tanh(s[3] + bf_hi(bias[1])));
            st8_dev(rws, ub + (ew0 + i) * 1024, w);
          }
        }
        publish(ws, fl + FL_U + panel4 * 512 + ft * 4, 4, 128, tag, wave, lane);
      }
    }
    {
      FRESH();
      const int tok = tb * 64 + ea * 16 + c16;
      const float bias = __uint_as_float((unsigned)b2[tok] << 16);
      if (wave >= 4) {
        const int r = lane >> 3, pos = lane & 7;
        const unsigned pe = (((pos >> 1) ^ ((r >> 1) & 1)) << 1 | (pos & 1)) * 16;
        const unsigned v0 = r * (S_ * 2) + pe + 4096, v1 = (r + 8) * (S_ * 2) + (pe ^ 64) + 3072, vstep = 16 * (S_ * 2) - 2048;
        const char* wsrc = (const char*)(w2 + tb * 64) - 4096 + (long)kq * 32 * (S_ * 2);
#pragma unroll 1
        for (int st = 4; st < 9; ++st) glds4<false>(wsrc + (long)st * 128 * (S_ * 2), lds0 + (st - 4) * 16384 + kq * 4096, v0, v1, v0 + vstep, v1 + vstep);
        wait_vbar<32>();
        static_for<0, 13>([&](auto ST) {
          constexpr int st = decltype(ST)::v;
          constexpr int last = (st + 8 > 13) ? 13 : st + 8;
          wait_vbar<4 * (last - st - 1)>();
          if (st + 9 < 14) {
            const long o = (st + 9 == 13 && kq >= 2) ? (long)(2 * 13 - 1) * 64 * (S_ * 2) : (long)(st + 9) * 128 * (S_ * 2);
            glds4<false>(wsrc + o, lds0 + w2_slot(st + 9) + kq * 4096, v0, v1, v0 + vstep, v1 + vstep);
          }
        });
        asm volatile("s_barrier" ::: "memory");
      } else {
        if (wave == 0) poll_flags(ws, fl + FL_U + j * 512 + (tb & 3) * 128, 27, tag, lane, dead);
        asm volatile("s_barrier" ::: "memory");
        const unsigned abase = WS_U + layer * UB + j * (U_KS * 5 * 1024) + kq * 5120 + lane * 16;
        const int q = c16 >> 2, p = lane & 3;
        const unsigned fsw = (((q >> 1) & 1) | ((g & 1) << 1)) * 32;
        const lds_cptr wrow = lds + (32 * kq + 8 * g + q) * 128 + 8 * p;
        u32x4 af[3][5];
#pragma unroll
        for (int st = 0; st < 3; ++st)
#pragma unroll
          for (int a = 0; a < 5; ++a) af[st][a] = ld16_dev_s(rws, abase + (a & 3) * 1024, st * 20480 + (a >> 2) * 4096);
        f32x4 acc[4][5];
#pragma unroll
        for (int i = 0; i < 4; ++i)
#pragma unroll
          for (int a = 0; a < 5; ++a) acc[i][a] = f32x4{0.f, 0.f, 0.f, 0.f};
        bf16x8 wf[2][4];
#pragma unroll
        for (int i = 0; i < 4; ++i) wf[0][i] = lds_tr8(wrow + w2_slot(0) + ((i * 32) ^ fsw), 512);
        static_for<0, 14>([&](auto ST) {
          constexpr int st = decltype(ST)::v;
          if constexpr (st < 13) {
            bar_lds();
#pragma unroll
            for (int i = 0; i < 4; ++i) wf[(st + 1) & 1][i] = lds_tr8(wrow + w2_slot(st + 1) + ((i * 32) ^ fsw), 512);
          }
          if (st < 13 || kq < 2) {
            SBAR();
#pragma unroll
            for (int a = 0; a < 5; ++a) {
              const bf16x8 x = __builtin_bit_cast(bf16x8, af[st % 3][a]);
#pragma unroll
              for (int i = 0; i < 4; ++i) acc[i][a] = MFMA16(x, wf[st & 1][i], acc[i][a]);
              SBAR();
              if (st + 3 < 14) af[st % 3][a] = ld16_dev_s(rws, abase + (a & 3) * 1024, (st + 3) * 20480 + (a >> 2) * 4096);
              SBAR();
            }
          }
        });
        bar_lds();
#pragma unroll
        for (int i = 0; i < 4; ++i)
#pragma unroll
          for (int a = 0; a < 5; ++a) *(LAS f32x4*)(ldw + ((kq * 4 + i) * 5 + a) * 1024 + lane * 16) = acc[i][a];
      }
      bar_lds();
      f32x4 sv[3]; u32x2 rv[3];
#pragma unroll
      for (int i = 0; i < 3; ++i) { const int at = min(ew0 + i, 4); sv[i] = sum4(lds, ea * 5 + at, 20, lane); rv[i] = *(const LAS u32x2*)(lds + res_off + at * 32); }
#pragma unroll
      for (int i = 0; i < 3; ++i) {
        if (i < ewn) {
          const int at = ew0 + i;
          const f32x4 s = sv[i];
          const u32x2 r = rv[i];
          u32x2 w;
          w[0] = pack_bf16(bf_lo(r[0]) + (s[0] + bias), bf_hi(r[0]) + (s[1] + bias));
          w[1] = pack_bf16(bf_lo(r[1]) + (s[2] + bias), bf_hi(r[1]) + (s[3] + bias));
          const int d = j * 80 + at * 16 + 4 * g;
          if (layer == 0) {
            *(LAS u32x2*)(ldw + res_off + at * 32) = w;
            st8_dev(rws, WS_X + tb * 81920 + (((d >> 5) * 4 + ea) * 64 + c16 + 16 * ((d & 31) >> 3)) * 16 + (d & 7) * 2, w);
          } else {
            *(u32x2*)(out + (long)tok * D_ + d) = w;
          }
        }
      }
      if (layer == 0) publish(ws, WS_FLAGS + FL_X + tb * 128 + j * 4, 1, 0, tag, wave, lane);
    }
  }
  if (blockIdx.x == 0 && wave0 == 0 && __builtin_amdgcn_mbcnt_hi(~0u, __builtin_amdgcn_mbcnt_lo(~0u, 0u)) == 0)
    __hip_atomic_store((gu32*)(ws + WS_EPOCH), tag, RLX_AGENT);
}

extern "C" void kernel_launch(void* const* d_in, const int* in_sizes, int n_in, void* d_out, int out_size, void* d_ws,
                              size_t ws_size, hipStream_t stream) {
  (void)hipFuncSetAttribute((const void*)mega_kernel, hipFuncAttributeMaxDynamicSharedMemorySize, LDS_BYTES);
  mega_kernel<<<256, 512, LDS_BYTES, stream>>>((const u16*)d_in[0], (const u16*)d_in[1], (const u16*)d_in[2], (const u16*)d_in[3],
                                               (const u16*)d_in[4], (const u16*)d_in[6], (const u16*)d_in[7], (const u16*)d_in[8],
                                               (const u16*)d_in[9], (u16*)d_out, (char*)d_ws);
}
```

```cpp
#include <hip/hip_runtime.h>
#include <hip/hip_bf16.h>
#include <stdint.h>

typedef __attribute__((ext_vector_type(8))) short bf16x8;
typedef __attribute__((ext_vector_type(4))) short bf16x4;
typedef __attribute__((ext_vector_type(16))) float f32x16;
typedef __attribute__((ext_vector_type(4))) float f32x4;
typedef __attribute__((ext_vector_type(2))) float f32x2;
typedef __attribute__((ext_vector_type(2))) __bf16 bf2_t;
typedef __attribute__((ext_vector_type(4))) unsigned u32x4;
typedef __attribute__((ext_vector_type(2))) unsigned u32x2;
typedef unsigned short u16;
#define LAS __attribute__((address_space(3)))
#define GAS __attribute__((address_space(1)))
typedef LAS const char* lds_cptr;
typedef LAS char* lds_ptr;
typedef GAS unsigned gu32;
typedef GAS unsigned long long gu64;
#define DI __device__ __forceinline__
#define RLX_AGENT __ATOMIC_RELAXED, __HIP_MEMORY_SCOPE_AGENT

constexpr int S_ = 2048, D_ = 640, DH_ = 20, F_ = 1728;
constexpr float LN_EPS = 1e-5f;
constexpr float QSCALE = 0.22360679774997896f * 1.4426950408889634f;

constexpr int KV_TILE = 2688, KV_KT = 1024, KV_V = 1280, KV_VS = 704;

constexpr unsigned WS_EPOCH = 0;
constexpr unsigned WS_FLAGS = 4096, FL_LAYER = 65536;
constexpr unsigned FL_KV = 0;
constexpr unsigned FL_O = 32768;
constexpr unsigned FL_X = 36864;
constexpr unsigned FL_H = 40960;
constexpr unsigned FL_U = 45056;
constexpr unsigned WS_STAT = 524288, ST_LAYER = 524288;
constexpr unsigned WS_KV = WS_STAT + 2 * ST_LAYER, KV_LAYER = 196608;
constexpr unsigned XB = S_ * D_ * 2;
constexpr unsigned WS_O = WS_KV + 2 * KV_LAYER;
constexpr unsigned WS_H = WS_O + 2 * XB;
constexpr int U_KS = 56;
constexpr unsigned UB = 8 * U_KS * 5 * 1024;
constexpr unsigned WS_U = WS_H + 2 * XB;
constexpr unsigned WS_X = WS_U + 2 * UB;
constexpr unsigned WS_END = WS_X + XB;

constexpr int L_W = 0;
constexpr int L_WT = 81920;
constexpr int L_RING = 81920;
constexpr int L_ATT = 102400;
constexpr int L_QT = 81920;
constexpr int L_KVP = 128064;
constexpr int L_STG = 134656;
constexpr int L_RES = 151040;
constexpr int L_DUMP = 161280;
constexpr int LDS_BYTES = 163840;
constexpr unsigned SPIN_CAP = 1u << 21;

DI unsigned pack_bf16(float lo, float hi) {
  f32x2 v = {lo, hi};
  bf2_t b = __builtin_convertvector(v, bf2_t);
  return __builtin_bit_cast(unsigned, b);
}
DI float bf_lo(unsigned u) { return __uint_as_float(u << 16); }
DI float bf_hi(unsigned u) { return __uint_as_float(u & 0xffff0000u); }
DI float rnd1(float v) { return bf_lo(pack_bf16(v, 0.f)); }
DI float ln_rsd(float var) { return __builtin_amdgcn_rcpf(rnd1(__builtin_amdgcn_sqrtf(rnd1(rnd1(var) + LN_EPS)))); }

#define GLDS_HEAD "s_mov_b32 m0, %1\n\ts_nop 4\n\t"
template <bool DEV> DI void glds1(const void* sbase, unsigned m0v, unsigned v0) {
  if (DEV) asm volatile(GLDS_HEAD "global_load_lds_dwordx4 %2, %0 sc1" ::"s"(sbase), "s"(m0v), "v"(v0) : "memory");
  else asm volatile(GLDS_HEAD "global_load_lds_dwordx4 %2, %0" ::"s"(sbase), "s"(m0v), "v"(v0) : "memory");
}
template <bool DEV> DI void glds4(const void* sbase, unsigned m0v, unsigned v0, unsigned v1, unsigned v2, unsigned v3) {
  if (DEV)
    asm volatile(GLDS_HEAD "global_load_lds_dwordx4 %2, %0 sc1\n\tglobal_load_lds_dwordx4 %3, %0 offset:1024 sc1\n\t"
                           "global_load_lds_dwordx4 %4, %0 offset:2048 sc1\n\tglobal_load_lds_dwordx4 %5, %0 offset:3072 sc1"
                 ::"s"(sbase), "s"(m0v), "v"(v0), "v"(v1), "v"(v2), "v"(v3) : "memory");
  else
    asm volatile(GLDS_HEAD "global_load_lds_dwordx4 %2, %0\n\tglobal_load_lds_dwordx4 %3, %0 offset:1024\n\t"
                           "global_load_lds_dwordx4 %4, %0 offset:2048\n\tglobal_load_lds_dwordx4 %5, %0 offset:3072"
                 ::"s"(sbase), "s"(m0v), "v"(v0), "v"(v1), "v"(v2), "v"(v3) : "memory");
}
template <int N, bool DEV> DI void glds_lin(const void* sbase, unsigned m0v, unsigned v) {
  if (N == 1) glds1<DEV>(sbase, m0v, v);
  else if (N == 4) glds4<DEV>(sbase, m0v, v, v, v, v);
  else if (N == 2) {
    if (DEV) asm volatile(GLDS_HEAD "global_load_lds_dwordx4 %2, %0 sc1\n\tglobal_load_lds_dwordx4 %2, %0 offset:1024 sc1" ::"s"(sbase), "s"(m0v), "v"(v) : "memory");
    else asm volatile(GLDS_HEAD "global_load_lds_dwordx4 %2, %0\n\tglobal_load_lds_dwordx4 %2, %0 offset:1024" ::"s"(sbase), "s"(m0v), "v"(v) : "memory");
  } else {
    if (DEV) asm volatile(GLDS_HEAD "global_load_lds_dwordx4 %2, %0 sc1\n\tglobal_load_lds_dwordx4 %2, %0 offset:1024 sc1\n\tglobal_load_lds_dwordx4 %2, %0 offset:2048 sc1" ::"s"(sbase), "s"(m0v), "v"(v) : "memory");
    else asm volatile(GLDS_HEAD "global_load_lds_dwordx4 %2, %0\n\tglobal_load_lds_dwordx4 %2, %0 offset:1024\n\tglobal_load_lds_dwordx4 %2, %0 offset:2048" ::"s"(sbase), "s"(m0v), "v"(v) : "memory");
  }
}
DI void glds2(const void* sbase, unsigned m0v, unsigned v0, unsigned v1) {
  asm volatile(GLDS_HEAD "global_load_lds_dwordx4 %2, %0\n\tglobal_load_lds_dwordx4 %3, %0 offset:1024" ::"s"(sbase), "s"(m0v), "v"(v0), "v"(v1) : "memory");
}
#define UNI(x) __builtin_amdgcn_readfirstlane(x)
template <int N> DI void wait_bar() { asm volatile("s_waitcnt vmcnt(%0) lgkmcnt(0)\n\ts_barrier" ::"i"(N) : "memory"); }
template <int N> DI void wait_vbar() { asm volatile("s_waitcnt vmcnt(%0)\n\ts_barrier" ::"i"(N) : "memory"); }
DI void bar_lds() { asm volatile("s_waitcnt lgkmcnt(0)\n\ts_barrier" ::: "memory"); }
typedef short v4i16_t __attribute__((ext_vector_type(4)));
DI bf16x4 lds_tr(lds_cptr p) {
  return __builtin_bit_cast(bf16x4, __builtin_amdgcn_ds_read_tr16_b64_v4i16((LAS v4i16_t*)p));
}
DI bf16x8 lds_tr8(lds_cptr p, int step) {
  const bf16x4 lo = lds_tr(p), hi = lds_tr(p + step);
  return __builtin_shufflevector(lo, hi, 0, 1, 2, 3, 4, 5, 6, 7);
}
DI bf16x8 lds_b128(lds_cptr p) { return *(const LAS bf16x8*)p; }
#define MFMA16(a, b, c) __builtin_amdgcn_mfma_f32_16x16x32_bf16((a), (b), (c), 0, 0, 0)
#define MFMA32(a, b, c) __builtin_amdgcn_mfma_f32_32x32x16_bf16((a), (b), (c), 0, 0, 0)
#define MFMA32K8(a, b, c) __builtin_amdgcn_mfma_f32_32x32x8bf16_1k((a), (b), (c), 0, 0, 0)
#define SBAR() __builtin_amdgcn_sched_barrier(0)
#define PIN(x) asm volatile("" : "+v"(x))
typedef __amdgpu_buffer_rsrc_t rsrc_t;
DI void st8_dev(rsrc_t r, unsigned off, u32x2 v) { __builtin_amdgcn_raw_buffer_store_b64(v, r, off, 0, 16); }
DI u32x4 ld16_dev(rsrc_t r, unsigned off) { return __builtin_amdgcn_raw_buffer_load_b128(r, off, 0, 16); }
DI u32x4 ld16_dev_s(rsrc_t r, unsigned off, unsigned soff) { return __builtin_amdgcn_raw_buffer_load_b128(r, off, soff, 16); }

DI void poll_flags(const char* ws, unsigned off, int n, unsigned tag, int lane, bool& dead) {
  gu32* f = (gu32*)(ws + off);
  unsigned spins = 0;
  while (!dead) {
    unsigned v = tag;
    if (lane < n) v = __hip_atomic_load(f + lane, RLX_AGENT);
    if (__all(v == tag)) break;
    if (++spins > SPIN_CAP) dead = true;
    __builtin_amdgcn_s_sleep(1);
  }
  asm volatile("" ::: "memory");
}
DI void publish(const char* ws, unsigned off, int nrep, unsigned rstride, unsigned tag, int wave, int lane) {
  wait_bar<0>();
  if (wave == 0 && lane < nrep) __hip_atomic_store((gu32*)(ws + off + lane * rstride), tag, RLX_AGENT);
}

DI float fmax3(float a, float b, float c) { return __builtin_fmaxf(__builtin_fmaxf(a, b), c); }
DI float rowmax16(const f32x16& s) {
  float a = fmax3(s[0], s[1], s[2]), b = fmax3(s[3], s[4], s[5]);
  a = fmax3(a, s[6], s[7]); b = fmax3(b, s[8], s[9]); a = fmax3(a, s[10], s[11]); b = fmax3(b, s[12], s[13]);
  a = fmax3(a, s[14], s[15]); a = __builtin_fmaxf(a, b);
  return __builtin_fmaxf(a, __shfl_xor(a, 32));
}
DI void pack_p(const f32x16& p, bf16x8& lo, bf16x8& hi) {
  u32x4 a, b;
#pragma unroll
  for (int i = 0; i < 4; ++i) { a[i] = pack_bf16(p[2 * i], p[2 * i + 1]); b[i] = pack_bf16(p[8 + 2 * i], p[9 + 2 * i]); }
  lo = __builtin_bit_cast(bf16x8, a); hi = __builtin_bit_cast(bf16x8, b);
}
DI float gelu_tanh(float u) {
  const float z = 0.7978845608028654f * (u + 0.044715f * u * u * u);
  const float e = __builtin_amdgcn_exp2f(-2.885390081777927f * z);
  return u * __builtin_amdgcn_rcpf(1.f + e);
}

DI void wslice_block(const u16* w, unsigned lds0, int lw, int lane, int blk) {
  const char* src = (const char*)w + lw * (160 * 1280);
  if (blk < 5) {
    const int r = lane >> 3, pos = lane & 7;
    const unsigned pe = (((pos >> 1) ^ ((r >> 1) & 1)) << 1 | (pos & 1)) * 16;
    const unsigned v0 = r * 1280 + pe, v1 = r * 1280 + (pe ^ 64) + 9216;
    glds4<false>(src + blk * (32 * 1280), lds0 + L_W + lw * 20480 + blk * 4096, v0, v1, v0 + 18432, v1 + 18432);
  } else {
    const unsigned t0 = (lane >> 1) * 1280 + 128 + (lane & 1) * 16;
    if (blk == 5) glds4<false>(src, lds0 + L_WT + lw * 5120, t0, t0 + 39936, t0 + 2 * 39936, t0 + 3 * 39936);
    else glds1<false>(src + 128 * 1280, lds0 + L_WT + lw * 5120 + 4096, t0);
  }
}

DI void touch(const void* sbase, unsigned voff, unsigned& sink) {
  asm volatile("s_nop 4\n\tglobal_load_dword %0, %1, %2" : "+v"(sink) : "v"(voff), "s"(sbase) : "memory");
}
DI float xsum16(float x) {
  auto r = __builtin_amdgcn_permlane16_swap(__float_as_uint(x), __float_as_uint(x), false, false);
  return __uint_as_float(r[0]) + __uint_as_float(r[1]);
}
DI float xsum32(float x) {
  auto r = __builtin_amdgcn_permlane32_swap(__float_as_uint(x), __float_as_uint(x), false, false);
  return __uint_as_float(r[0]) + __uint_as_float(r[1]);
}
DI f32x4 ldf4(lds_cptr p) { return *(const LAS f32x4*)p; }

DI f32x4 sum4(lds_cptr lds, int t, int nt, int lane) {
  f32x4 s = *(const LAS f32x4*)(lds + t * 1024 + lane * 16);
#pragma unroll
  for (int k = 1; k < 4; ++k) s += *(const LAS f32x4*)(lds + (k * nt + t) * 1024 + lane * 16);
  return s;
}
DI void ln_finish(float s1, float s2, float& mu, float& var) { mu = s1 * (1.f / D_); var = fmaxf(s2 * (1.f / D_) - mu * mu, 0.f); }

constexpr int ATT_CH = 4;
constexpr int ATT_CHB = ATT_CH * KV_TILE;
constexpr int ATT_SLOT = 10752;
constexpr int ATT_NCH = S_ / 32 / ATT_CH;
constexpr int w2_slot(int st) { return (st % 9) < 4 ? L_RING + (st % 9) * 16384 : ((st % 9) - 4) * 16384; }
template <int N> struct IC { static constexpr int v = N; };
template <int I, int N, class F> DI void static_for(F&& f) { if constexpr (I < N) { f(IC<I>{}); static_for<I + 1, N>(f); } }


__global__ __launch_bounds__(512, 2) void mega_kernel(const u16* __restrict__ x0, const u16* __restrict__ wq, const u16* __restrict__ wk,
                                                      const u16* __restrict__ wv, const u16* __restrict__ wo, const u16* __restrict__ w1,
                                                      const u16* __restrict__ b1, const u16* __restrict__ w2, const u16* __restrict__ b2,
                                                      u16* __restrict__ out, char* ws) {
  extern __shared__ __attribute__((aligned(16))) char smem[];
  const int tid = threadIdx.x;
  const unsigned lds0 = (unsigned)(uintptr_t)smem;
  const lds_cptr lds = (lds_cptr)smem;
  const lds_ptr ldw = (lds_ptr)smem;
#define FRESH() \
  int lane = tid & 63; asm volatile("" : "+v"(lane)); \
  int wave = UNI(tid >> 6); asm volatile("" : "+s"(wave)); \
  int bid = blockIdx.x; asm volatile("" : "+s"(bid)); \
  const int xcd = bid & 7, idx = bid >> 3, tb = xcd * 4 + (idx >> 3), j = idx & 7; \
  const int g = lane >> 4, c16 = lane & 15, kq = wave & 3, th = wave >> 2; \
  const int ea = wave & 3, ew0 = th ? 3 : 0, ewn = th ? 2 : 3; \
  const unsigned res_off = L_RES + (ea * 16 + c16) * 160 + 8 * g; \
  const int panel4 = (xcd & 1) * 4 + (idx & 3), ft = (xcd >> 1) * 7 + (idx >> 2); \
  const bool has4 = idx < 28 && ft < 27; \
  (void)xcd; (void)g; (void)c16; (void)kq; (void)th; (void)ea; (void)ew0; (void)ewn; (void)res_off; (void)tb; (void)j; (void)panel4; (void)has4
  const rsrc_t rws = __builtin_amdgcn_make_buffer_rsrc(ws, 0, WS_END, 0x00020000);
  unsigned tag = 0;
  bool dead = false;

#pragma unroll 1
  for (int layer = 0; layer < 2; ++layer) {
    const unsigned fl = WS_FLAGS + layer * FL_LAYER;
    const unsigned kvoff = WS_KV + layer * KV_LAYER;
    unsigned sink = 0;
    {
      FRESH();
      auto load_kvw = [&]() {
        const char* xs = (const char*)((j & 1) ? wv : wk);
        if (wave < 3) {
          glds_lin<4, false>(xs + wave * 8192, lds0 + L_ATT + wave * 8192, lane * 16);
          glds_lin<4, false>(xs + wave * 8192 + 4096, lds0 + L_ATT + wave * 8192 + 4096, lane * 16);
        } else glds1<false>(xs + 24576, lds0 + L_ATT + 24576, lane * 16);
      };
      u32x4 af[5][2];
      if (layer == 0) {
        const rsrc_t ra = __builtin_amdgcn_make_buffer_rsrc((void*)x0, 0, XB, 0x00020000);
        const unsigned abase = (tb * 64 + th * 32 + c16) * (D_ * 2) + kq * 64 + g * 16;
#pragma unroll
        for (int s = 0; s < 5; ++s)
#pragma unroll
          for (int t = 0; t < 2; ++t) af[s][t] = __builtin_amdgcn_raw_buffer_load_b128(ra, abase + s * 256, t * (16 * D_ * 2), 0);
        const unsigned epoch = __hip_atomic_load((gu32*)(ws + WS_EPOCH), RLX_AGENT);
        if (wave < 4) load_kvw();
        else touch(wq, min((idx * 4 + kq) * 50 + lane, 6399) * 128, sink);
        wait_vbar<0>();
        tag = UNI(epoch) + 1u;
        if (wave >= 4) {
#pragma unroll 1
          for (int blk = 0; blk < 7; ++blk) wslice_block(wq + j * 80, lds0, kq, lane, blk);
        } else {
#pragma unroll 1
          for (int pc = wave; pc < 10; pc += 4) {
            const int n = pc * 64 + lane, row = n / 10, ch = n - row * 10;
            glds1<false>(x0 + (long)tb * 64 * D_ + j * 80, lds0 + L_RES + pc * 1024, row * 1280 + ch * 16);
          }
        }
      } else {
        if (wave >= 4) {
#pragma unroll 1
          for (int blk = 0; blk < 7; ++blk) wslice_block(wq + j * 80, lds0, kq, lane, blk);
        } else load_kvw();
        if (wave == 0) poll_flags(ws, WS_FLAGS + FL_X + tb * 128, 8, tag, lane, dead);
        wait_vbar<0>();
        const unsigned abase = WS_X + tb * 81920 + (kq * 4 + th * 2) * 1024 + lane * 16;
#pragma unroll
        for (int s = 0; s < 5; ++s)
#pragma unroll
          for (int t = 0; t < 2; ++t) af[s][t] = ld16_dev_s(rws, abase + t * 1024, s * 16384);
      }
      bf16x8 ones;
#pragma unroll
      for (int i = 0; i < 8; ++i) ones[i] = (short)0x3f80;
      const f32x4 z4 = {0.f, 0.f, 0.f, 0.f};
      const int q = c16 >> 2, p = lane & 3;
      const int row = 32 * kq + 8 * g + q;
      const unsigned fsw = (((q >> 1) & 1) | ((g & 1) << 1)) * 32;
      const unsigned woff = L_W + row * 128 + 8 * p, toff = L_WT + row * 32 + 8 * p, xoff = L_ATT + row * 40 + 8 * p;
      LAS float* stat = (LAS float*)(ldw + L_DUMP);
      LAS float* csum = (LAS float*)(ldw + L_KVP + 20480);
      const int tk = j >> 1;
      const bool isv = j & 1;
      {
        f32x4 a1s[2] = {z4, z4}, a2s[2] = {z4, z4}, akv[2] = {z4, z4}, aok[2] = {z4, z4};
#pragma unroll
        for (int s = 0; s < 5; ++s) {
          const bf16x8 a0 = __builtin_bit_cast(bf16x8, af[s][0]), a1 = __builtin_bit_cast(bf16x8, af[s][1]);
          if (th == (tk >> 1)) {
            const bf16x8 a = (tk & 1) ? a1 : a0;
            const bf16x8 x0f = lds_tr8(lds + xoff + s * 5120, 160), x1f = lds_tr8(lds + xoff + s * 5120 + 32, 160);
            if (!isv) {
              akv[0] = MFMA16(x0f, a, akv[0]); akv[1] = MFMA16(x1f, a, akv[1]);
              aok[0] = MFMA16(x0f, ones, aok[0]); aok[1] = MFMA16(x1f, ones, aok[1]);
            } else {
              akv[0] = MFMA16(a, x0f, akv[0]); akv[1] = MFMA16(a, x1f, akv[1]);
              aok[0] = MFMA16(ones, x0f, aok[0]); aok[1] = MFMA16(ones, x1f, aok[1]);
            }
          }
          a1s[0] = MFMA16(ones, a0, a1s[0]); a1s[1] = MFMA16(ones, a1, a1s[1]);
          a2s[0] = MFMA16(a0, a0, a2s[0]); a2s[1] = MFMA16(a1, a1, a2s[1]);
        }
        if (g == q) {
#pragma unroll
          for (int t = 0; t < 2; ++t) {
            const float d = (p & 2) ? ((p & 1) ? a2s[t][3] : a2s[t][2]) : ((p & 1) ? a2s[t][1] : a2s[t][0]);
            *(LAS f32x2*)(stat + (kq * 64 + th * 32 + t * 16 + c16) * 2) = f32x2{a1s[t][0], d};
          }
        }
        if (th == (tk >> 1)) {
          const bool l2 = isv ? (c16 < 4) : (g == 0);
          const int i2 = isv ? g * 4 + c16 : c16;
          const int o = L_KVP + kq * 2560;
          *(LAS f32x4*)(ldw + o + lane * 16) = akv[0];
          *(LAS f32x4*)(ldw + o + 1280 + lane * 16) = aok[0];
          if (l2) { *(LAS f32x4*)(ldw + o + 1024 + i2 * 16) = akv[1]; *(LAS f32x4*)(ldw + o + 2304 + i2 * 16) = aok[1]; }
        }
      }
      wait_bar<0>();
      float mu, rs;
      {
        f32x2 sv[4];
#pragma unroll
        for (int k = 0; k < 4; ++k) sv[k] = *(const LAS f32x2*)(stat + (k * 64 + ea * 16 + c16) * 2);
        float var; ln_finish((sv[0][0] + sv[1][0]) + (sv[2][0] + sv[3][0]), (sv[0][1] + sv[1][1]) + (sv[2][1] + sv[3][1]), mu, var);
        mu = rnd1(mu); rs = ln_rsd(var);
      }
      if (ea == tk) {
        const int li = th == 0 ? lane : 64 + (isv ? g * 4 + (c16 & 3) : c16);
        f32x4 s = z4, cs = z4;
#pragma unroll
        for (int k = 0; k < 4; ++k) { s += ldf4(lds + L_KVP + k * 2560 + li * 16); cs += ldf4(lds + L_KVP + k * 2560 + 1280 + li * 16); }
        const unsigned tile = kvoff + (tb * 2 + (ea >> 1)) * KV_TILE;
        if (!isv) {
          u32x2 w; w[0] = pack_bf16((s[0] - mu * cs[0]) * rs, (s[1] - mu * cs[1]) * rs); w[1] = pack_bf16((s[2] - mu * cs[2]) * rs, (s[3] - mu * cs[3]) * rs);
          const int r32 = (ea & 1) * 16 + c16;
          if (th == 0) st8_dev(rws, tile + ((g >> 1) * 32 + r32) * 16 + (g & 1) * 8, w);
          else if (g == 0) st8_dev(rws, tile + KV_KT + r32 * 8, w);
        } else {
          float o[4];
#pragma unroll
          for (int e = 0; e < 4; ++e) o[e] = (s[e] - __shfl(mu, 4 * g + e) * cs[e]) * __shfl(rs, 4 * g + e);
          const int d = th * 16 + c16;
          u32x2 w; w[0] = pack_bf16(o[0], o[1]); w[1] = pack_bf16(o[2], o[3]);
          if (d == DH_) { w[0] = 0x3f803f80u; w[1] = 0x3f803f80u; }
          if (d == DH_ + 1) { w[0] = 0u; w[1] = 0u; }
          if (d <= DH_ + 1) st8_dev(rws, tile + KV_V + (ea & 1) * KV_VS + ((g & 1) * 22 + d) * 16 + (g >> 1) * 8, w);
        }
      }
      f32x4 acc[5][2], aon[3] = {z4, z4, z4};
#pragma unroll
      for (int i = 0; i < 5; ++i) { acc[i][0] = z4; acc[i][1] = z4; }
      bf16x8 wq2[2][5];
#pragma unroll
      for (int i = 0; i < 5; ++i) wq2[0][i] = i < 4 ? lds_tr8(lds + woff + ((i * 32) ^ fsw), 512) : lds_tr8(lds + toff, 128);
#pragma unroll
      for (int s = 0; s < 5; ++s) {
        const bf16x8 a0 = __builtin_bit_cast(bf16x8, af[s][0]), a1 = __builtin_bit_cast(bf16x8, af[s][1]);
        const bf16x8* wf = wq2[s & 1];
#pragma unroll
        for (int i = 0; i < 5; ++i) {
          if (s < 4) wq2[(s + 1) & 1][i] = i < 4 ? lds_tr8(lds + woff + (s + 1) * 16384 + ((i * 32) ^ fsw), 512) : lds_tr8(lds + toff + (s + 1) * 4096, 128);
          acc[i][0] = MFMA16(wf[i], a0, acc[i][0]); acc[i][1] = MFMA16(wf[i], a1, acc[i][1]);
          SBAR();
        }
        if (th == 0) { aon[0] = MFMA16(wf[0], ones, aon[0]); aon[1] = MFMA16(wf[1], ones, aon[1]); aon[2] = MFMA16(wf[2], ones, aon[2]); }
        else { aon[0] = MFMA16(wf[3], ones, aon[0]); aon[1] = MFMA16(wf[4], ones, aon[1]); }
        if (s == 1) {
          publish(ws, fl + FL_KV + (tb * 8 + j) * 4, 32, 1024, tag, wave, lane);
          if (wave >= 4) {
            touch((const char*)(w2 + tb * 64) + (long)(j * 216 + kq * 54) * (S_ * 2), (lane < 54 ? lane : 53) * (S_ * 2), sink);
          }
        }
      }
      bar_lds();
#pragma unroll
      for (int i = 0; i < 5; ++i)
#pragma unroll
        for (int t = 0; t < 2; ++t) *(LAS f32x4*)(ldw + ((kq * 5 + i) * 4 + th * 2 + t) * 1024 + lane * 16) = acc[i][t];
      if (c16 == 0) {
#pragma unroll
        for (int i = 0; i < 3; ++i) if (i < ewn) *(LAS f32x4*)(csum + (kq * 7 + ew0 + i) * 16 + 4 * g) = aon[i];
      }
      bar_lds();
      {
        const float r = rs * QSCALE;
        f32x4 s[3], cs[3];
#pragma unroll
        for (int i = 0; i < 3; ++i) {
          const int wt = min(ew0 + i, 4);
          s[i] = sum4(lds, wt * 4 + ea, 20, lane);
          cs[i] = z4;
#pragma unroll
          for (int k = 0; k < 4; ++k) cs[i] += *(const LAS f32x4*)(csum + (k * 7 + wt) * 16 + 4 * g);
        }
#pragma unroll
        for (int i = 0; i < 3; ++i) {
          if (i < ewn) {
            u32x2 w;
            w[0] = pack_bf16((s[i][0] - mu * cs[i][0]) * r, (s[i][1] - mu * cs[i][1]) * r);
            w[1] = pack_bf16((s[i][2] - mu * cs[i][2]) * r, (s[i][3] - mu * cs[i][3]) * r);
            *(LAS u32x2*)(ldw + L_QT + (ea * 16 + c16) * 160 + (ew0 + i) * 32 + 8 * g) = w;
          }
        }
      }
      bar_lds();
    }
    bf16x8 w1r[16][2];
    {
      FRESH();
      const int r32 = lane & 31, hi = lane >> 5;
      const int hh = wave & 3, trow = th * 32 + r32;
      bf16x8 q16; bf16x4 q4;
      {
        const lds_cptr qp = lds + L_QT + trow * 160 + hh * 40;
        const u32x2 a = *(const LAS u32x2*)(qp + hi * 16), b = *(const LAS u32x2*)(qp + hi * 16 + 8), c = *(const LAS u32x2*)(qp + 32);
        u32x4 v = {a[0], a[1], b[0], b[1]};
        q16 = __builtin_bit_cast(bf16x8, v);
        u32x2 t = {hi ? 0u : c[0], hi ? 0u : c[1]};
        q4 = __builtin_bit_cast(bf16x4, t);
        PIN(q16); PIN(q4);
      }
      if (wave == 0) {
        unsigned spins = 0;
        while (!dead) {
          const u32x4 v = ld16_dev(rws, fl + FL_KV + (bid & 31) * 1024 + lane * 16);
          if (__all(v[0] == tag && v[1] == tag && v[2] == tag && v[3] == tag)) break;
          if (++spins > SPIN_CAP) dead = true;
          __builtin_amdgcn_s_sleep(1);
          asm volatile("" ::: "memory");
        }
      }
      bar_lds();
      const unsigned koff = lane * 16;
      const char* KV = ws + kvoff + wave * 3072;
      auto issue_kv = [&](int ch) {
        const unsigned dst = lds0 + L_ATT + (ch % 3) * ATT_SLOT + wave * 3072;
        if (wave < 3) glds_lin<3, true>(KV + ch * ATT_CHB, dst, koff);
        else { glds1<true>(KV + ch * ATT_CHB, dst, koff); if (lane < 32) glds1<true>(KV + ch * ATT_CHB + 1024, dst + 1024, koff); }
      };
      const char* w1s = (const char*)(w1 + (has4 ? ft : 0) * 64) + (long)wave * 16 * (F_ * 2);
      unsigned sv0, sv1;
      {
        const int r = lane >> 3, pos = lane & 7;
        const unsigned pe = (((pos >> 1) ^ ((r >> 1) & 1)) << 1 | (pos & 1)) * 16;
        sv0 = r * (F_ * 2) + pe; sv1 = (r + 8) * (F_ * 2) + (pe ^ 64) - 1024;
      }
      const unsigned fsw = (((c16 >> 3) & 1) | ((g & 1) << 1)) * 32;
      const unsigned srow = (32 * kq + 8 * g + (c16 >> 2)) * 128 + 8 * (lane & 3);
      const unsigned sr0 = srow + ((64 * th) ^ fsw), sr1 = srow + ((64 * th + 32) ^ fsw);
      if (wave < 4) { issue_kv(0); issue_kv(1); }
      glds2(w1s, lds0 + L_STG + wave * 2048, sv0, sv1);
      const unsigned ktoff = KV_KT + r32 * 8, voff = KV_V + (hi * 22 + (r32 < 21 ? r32 : 21)) * 16;
      f32x16 o = {}, negm, sA, sB;
      bf16x8 k16, v0, v1; bf16x4 k4; u32x4 p0, p1;
      if (wave < 3) wait_bar<5>(); else if (wave == 3) wait_bar<4>(); else bar_lds();
      const lds_cptr ring = lds + L_ATT;
      {
        k16 = lds_b128(ring + koff);
        k4 = *(const LAS bf16x4*)(ring + ktoff);
        f32x16 z = {};
        z = MFMA32(k16, q16, z); z = MFMA32K8(k4, q4, z);
        k16 = lds_b128(ring + KV_TILE + koff);
        k4 = *(const LAS bf16x4*)(ring + KV_TILE + ktoff);
        v1 = lds_b128(ring + voff);
        const float m = rowmax16(z);
#pragma unroll
        for (int i = 0; i < 16; ++i) { negm[i] = -m; sA[i] = z[i] - m; }
        p1 = u32x4{0u, 0u, 0u, 0u};
      }
#define EX(x) __builtin_amdgcn_exp2f(x)
#define EXP4(X, i) do { X[i] = EX(X[i]); X[i + 1] = EX(X[i + 1]); X[i + 2] = EX(X[i + 2]); X[i + 3] = EX(X[i + 3]); } while (0)
#define BF8(x) __builtin_bit_cast(bf16x8, x)
#define PK4(P, X, i) do { P[0] = pack_bf16(X[i], X[i + 1]); P[1] = pack_bf16(X[i + 2], X[i + 3]); P[2] = pack_bf16(X[i + 4], X[i + 5]); P[3] = pack_bf16(X[i + 6], X[i + 7]); } while (0)
#define STEP(C, N, kn, vc) do { \
    N = MFMA32(k16, q16, negm); SBAR(); \
    v0 = lds_b128((vc) + voff); EXP4(C, 0); PIN(C); SBAR(); \
    o = MFMA32(v1, BF8(p1), o); SBAR(); \
    k16 = lds_b128((kn) + koff); EXP4(C, 4); PIN(C); SBAR(); PK4(p0, C, 0); PIN(p0); SBAR(); \
    N = MFMA32K8(k4, q4, N); SBAR(); \
    v1 = lds_b128((vc) + voff + KV_VS); EXP4(C, 8); PIN(C); SBAR(); \
    o = MFMA32(v0, BF8(p0), o); SBAR(); \
    k4 = *(const LAS bf16x4*)((kn) + ktoff); EXP4(C, 12); PIN(C); SBAR(); PK4(p1, C, 8); PIN(p1); SBAR(); \
  } while (0)
      static_for<0, ATT_NCH>([&](auto CH) {
        constexpr int ch = decltype(CH)::v;
        const lds_cptr cb = ring + (ch % 3) * ATT_SLOT;
        const lds_cptr nb = (ch + 1 < ATT_NCH) ? ring + ((ch + 1) % 3) * ATT_SLOT : cb;
        __builtin_amdgcn_s_setprio(1);
        STEP(sA, sB, cb + 2 * KV_TILE, cb);
        __builtin_amdgcn_s_setprio(0);
        STEP(sB, sA, cb + 3 * KV_TILE, cb + KV_TILE);
        if (wave < 4) wait_vbar<(ch >= 1 && ch <= 5) ? 4 : 0>(); else wait_vbar<0>();
        if (ch + 2 < ATT_NCH) { if (wave < 4) issue_kv(ch + 2); }
        if (ch + 1 < 16) glds2(w1s + (long)(ch + 1) * 128 * (F_ * 2), lds0 + ((ch & 1) ? L_STG : L_WT) + wave * 2048, sv0, sv1);
        if (ch < 5) { if (wave < 4) wslice_block(wo + j * 80, lds0, wave, lane, ch); }
        w1r[ch][0] = lds_tr8(lds + ((ch & 1) ? L_WT : L_STG) + sr0, 512); w1r[ch][1] = lds_tr8(lds + ((ch & 1) ? L_WT : L_STG) + sr1, 512);
        __builtin_amdgcn_s_setprio(3);
        SBAR();
        STEP(sA, sB, nb, cb + 2 * KV_TILE);
        PIN(w1r[ch][0]); PIN(w1r[ch][1]);
        __builtin_amdgcn_s_setprio(2);
        SBAR();
        STEP(sB, sA, nb + KV_TILE, cb + 3 * KV_TILE);
      });
      __builtin_amdgcn_s_setprio(0);
      o = MFMA32(v1, BF8(p1), o);
      float l = xsum32(hi ? o[8] : 0.f);
      bool bad = !(l < 1.8446744e19f);
#pragma unroll
      for (int i = 0; i < 12; ++i) bad |= !(__builtin_fabsf(o[i]) < 3.0e38f);
      if (__builtin_expect(__any(bad), 0)) {
        float m = -INFINITY;
        o = f32x16{};
#pragma unroll 1
        for (int t = 0; t < S_ / 32; ++t) {
          const unsigned tp = kvoff + t * KV_TILE;
          const bf16x8 a16 = BF8(ld16_dev(rws, tp + koff));
          const u32x2 a4 = __builtin_amdgcn_raw_buffer_load_b64(rws, tp + ktoff, 0, 16);
          const bf16x8 w0 = BF8(ld16_dev(rws, tp + voff)), w1f = BF8(ld16_dev(rws, tp + voff + KV_VS));
          f32x16 z = {};
          z = MFMA32(a16, q16, z); z = MFMA32K8(__builtin_bit_cast(bf16x4, a4), q4, z);
          const float mn = __builtin_fmaxf(m, rowmax16(z));
          const float alpha = __builtin_amdgcn_exp2f(m - mn);
          m = mn;
#pragma unroll
          for (int i = 0; i < 16; ++i) { z[i] = __builtin_amdgcn_exp2f(z[i] - m); o[i] *= alpha; }
          bf16x8 a0, a1; pack_p(z, a0, a1);
          o = MFMA32(w0, a0, o); o = MFMA32(w1f, a1, o);
        }
        l = xsum32(hi ? o[8] : 0.f);
      }
      const float rl = __builtin_amdgcn_rcpf(l);
      const unsigned ob = WS_O + layer * XB + tb * 81920 + ((trow >> 4) * 64 + (trow & 15)) * 16;
#pragma unroll
      for (int gq = 0; gq < 3; ++gq) {
        const int d0 = 8 * gq + 4 * hi;
        if (d0 < DH_) {
          const int k = (j * 4 + hh) * DH_ + d0;
          u32x2 w; w[0] = pack_bf16(o[4 * gq] * rl, o[4 * gq + 1] * rl); w[1] = pack_bf16(o[4 * gq + 2] * rl, o[4 * gq + 3] * rl);
          st8_dev(rws, ob + (k >> 5) * 4096 + ((k & 31) >> 3) * 256 + (k & 7) * 2, w);
        }
      }
      publish(ws, fl + FL_O + tb * 128 + j * 4, 1, 0, tag, wave, lane);
      asm volatile("" :: "v"(sink));
      if (wave >= 4) { wslice_block(wo + j * 80, lds0, kq, lane, 5); wslice_block(wo + j * 80, lds0, kq, lane, 6); }
    }
    {
      FRESH();
      if (wave == 0) poll_flags(ws, fl + FL_O + tb * 128, 8, tag, lane, dead);
      wait_vbar<0>();
      const unsigned abase = WS_O + layer * XB + tb * 81920 + (kq * 4 + th * 2) * 1024 + lane * 16;
      u32x4 af[5][2];
#pragma unroll
      for (int s = 0; s < 5; ++s)
#pragma unroll
        for (int t = 0; t < 2; ++t) af[s][t] = ld16_dev_s(rws, abase + t * 1024, s * 16384);
      f32x4 acc[5][2];
#pragma unroll
      for (int i = 0; i < 5; ++i) { acc[i][0] = f32x4{0.f, 0.f, 0.f, 0.f}; acc[i][1] = acc[i][0]; }
      const int q = c16 >> 2, p = lane & 3;
      const int row = 32 * kq + 8 * g + q;
      const unsigned fsw = (((q >> 1) & 1) | ((g & 1) << 1)) * 32;
      const unsigned woff = L_W + row * 128 + 8 * p, toff = L_WT + row * 32 + 8 * p;
      bf16x8 wf[2][5];
#pragma unroll
      for (int i = 0; i < 5; ++i) wf[0][i] = i < 4 ? lds_tr8(lds + woff + ((i * 32) ^ fsw), 512) : lds_tr8(lds + toff, 128);
#pragma unroll
      for (int s = 0; s < 5; ++s) {
        const bf16x8 a0 = __builtin_bit_cast(bf16x8, af[s][0]), a1 = __builtin_bit_cast(bf16x8, af[s][1]);
#pragma unroll
        for (int i = 0; i < 5; ++i) {
          if (s < 4) wf[(s + 1) & 1][i] = i < 4 ? lds_tr8(lds + woff + (s + 1) * 16384 + ((i * 32) ^ fsw), 512) : lds_tr8(lds + toff + (s + 1) * 4096, 128);
          acc[i][0] = MFMA16(wf[s & 1][i], a0, acc[i][0]); acc[i][1] = MFMA16(wf[s & 1][i], a1, acc[i][1]);
          SBAR();
        }
      }
      bar_lds();
#pragma unroll
      for (int i = 0; i < 5; ++i)
#pragma unroll
        for (int t = 0; t < 2; ++t) *(LAS f32x4*)(ldw + ((kq * 5 + i) * 4 + th * 2 + t) * 1024 + lane * 16) = acc[i][t];
      bar_lds();
      gu64* gr = (gu64*)(ws + WS_STAT + layer * ST_LAYER + (tb * 4 + ea) * 4096);
      {
        f32x4 s[3]; u32x2 r[3];
#pragma unroll
        for (int i = 0; i < 3; ++i) {
          const int wt = min(ew0 + i, 4);
          s[i] = sum4(lds, wt * 4 + ea, 20, lane);
          r[i] = *(const LAS u32x2*)(lds + res_off + wt * 32);
        }
        float s1 = 0.f, s2 = 0.f;
#pragma unroll
        for (int i = 0; i < 3; ++i) {
          if (i < ewn) {
            u32x2 w;
            w[0] = pack_bf16(bf_lo(r[i][0]) + s[i][0], bf_hi(r[i][0]) + s[i][1]);
            w[1] = pack_bf16(bf_lo(r[i][1]) + s[i][2], bf_hi(r[i][1]) + s[i][3]);
            *(LAS u32x2*)(ldw + res_off + (ew0 + i) * 32) = w;
            const float a = bf_lo(w[0]), b = bf_hi(w[0]), c = bf_lo(w[1]), d = bf_hi(w[1]);
            s1 += (a + b) + (c + d);
            s2 += (a * a + b * b) + (c * c + d * d);
          }
        }
        s1 = xsum32(xsum16(s1)); s2 = xsum32(xsum16(s2));
        if (lane < 32) __hip_atomic_store(gr + (2 * j + th) * 32 + lane, ((unsigned long long)tag << 32) | __float_as_uint(g ? s2 : s1), RLX_AGENT);
      }
      bar_lds();
      if (wave >= 4) {
        const int r = lane >> 3, pos = lane & 7;
        const unsigned pe = (((pos >> 1) ^ ((r >> 1) & 1)) << 1 | (pos & 1)) * 16;
        const unsigned v0 = r * (S_ * 2) + pe + 4096, v1 = (r + 8) * (S_ * 2) + (pe ^ 64) + 3072, vstep = 16 * (S_ * 2) - 2048;
        const char* wsrc = (const char*)(w2 + tb * 64) - 4096 + (long)kq * 32 * (S_ * 2);
#pragma unroll 1
        for (int st = 0; st < 4; ++st) glds4<false>(wsrc + (long)st * 128 * (S_ * 2), lds0 + L_RING + st * 16384 + kq * 4096, v0, v1, v0 + vstep, v1 + vstep);
        asm volatile("s_barrier" ::: "memory");
      } else {
        u32x2 xv[5];
#pragma unroll
        for (int wt = 0; wt < 5; ++wt) xv[wt] = __builtin_bit_cast(u32x2, lds_tr(lds + L_RES + (ea * 16 + 4 * g + q) * 160 + wt * 32 + 8 * p));
        float tot;
        {
          unsigned spins = 0;
          for (;;) {
            bool ok = true; tot = 0.f;
#pragma unroll
            for (int i = 0; i < 8; ++i) {
              const unsigned long long v = __hip_atomic_load(gr + i * 64 + lane, RLX_AGENT);
              tot += __uint_as_float((unsigned)v); ok &= (unsigned)(v >> 32) == tag;
            }
            if (__all(ok) || dead) break;
            if (++spins > SPIN_CAP) dead = true;
            __builtin_amdgcn_s_sleep(1);
          }
        }
        tot = xsum32(tot);
        float mu4[4], rr4[4], m24[4], r24[4];
#pragma unroll
        for (int e = 0; e < 4; ++e) { mu4[e] = __shfl(tot, 4 * g + e); rr4[e] = __shfl(tot, 16 + 4 * g + e); }
#pragma unroll
        for (int e = 0; e < 4; ++e) {
          float mu, var; ln_finish(mu4[e], rr4[e], mu, var);
          mu4[e] = rnd1(mu); rr4[e] = ln_rsd(var);
          m24[e] = rnd1((mu - mu4[e]) * rr4[e]); r24[e] = ln_rsd(var * rr4[e] * rr4[e]);
        }
        const unsigned hb = WS_H + layer * XB + j * 327680 + ((tb * 2 + (ea >> 1)) * 5) * 1024 + (c16 + 16 * ((ea & 1) * 2 + (g >> 1))) * 16 + (g & 1) * 8;
#pragma unroll
        for (int wt = 0; wt < 5; ++wt) {
          const u32x2 v = xv[wt];
          u32x2 w;
          w[0] = pack_bf16(bf_lo(v[0]) - mu4[0], bf_hi(v[0]) - mu4[1]); w[1] = pack_bf16(bf_lo(v[1]) - mu4[2], bf_hi(v[1]) - mu4[3]);
          w[0] = pack_bf16(bf_lo(w[0]) * rr4[0], bf_hi(w[0]) * rr4[1]); w[1] = pack_bf16(bf_lo(w[1]) * rr4[2], bf_hi(w[1]) * rr4[3]);
          w[0] = pack_bf16(bf_lo(w[0]) - m24[0], bf_hi(w[0]) - m24[1]); w[1] = pack_bf16(bf_lo(w[1]) - m24[2], bf_hi(w[1]) - m24[3]);
          w[0] = pack_bf16(bf_lo(w[0]) * r24[0], bf_hi(w[0]) * r24[1]); w[1] = pack_bf16(bf_lo(w[1]) * r24[2], bf_hi(w[1]) * r24[3]);
          st8_dev(rws, hb + wt * 1024, w);
        }
        publish(ws, fl + FL_H + j * 512 + tb * 4, 4, 128, tag, wave, lane);
      }
    }
    {
      FRESH();
      if (has4) {
        const int f = ft * 64 + ea * 16 + 4 * g;
        const u32x2 bias = *(const u32x2*)(b1 + f);
        if (wave == 0) poll_flags(ws, fl + FL_H + panel4 * 512 + ((idx >> 2) & 3) * 128, 32, tag, lane, dead);
        asm volatile("s_barrier" ::: "memory");
        const unsigned l16 = lane * 16;
        const int po = (th ? 12 + 2 * kq : 3 * kq) * 1024;
        const char* hsrc = ws + WS_H + layer * XB + panel4 * 327680 + po;
        auto issue = [&](int st) {
          if (th == 0) glds_lin<3, true>(hsrc + st * 20480, lds0 + (st & 3) * 20480 + po, l16);
          else glds_lin<2, true>(hsrc + st * 20480, lds0 + (st & 3) * 20480 + po, l16);
        };
        issue(0); issue(1); issue(2); issue(3);
        f32x4 acc[2][5];
#pragma unroll
        for (int i = 0; i < 2; ++i)
#pragma unroll
          for (int a = 0; a < 5; ++a) acc[i][a] = f32x4{0.f, 0.f, 0.f, 0.f};
        const lds_cptr ab = lds + kq * 5120 + l16;
        bf16x8 af[2][5];
        if (th == 0) wait_vbar<9>(); else wait_vbar<6>();
#pragma unroll
        for (int a = 0; a < 5; ++a) af[0][a] = lds_b128(ab + a * 1024);
        static_for<0, 16>([&](auto ST) {
          constexpr int st = decltype(ST)::v;
          if constexpr (st < 15) {
            constexpr int ahead = (14 - st) < 2 ? (14 - st) : 2;
            if (th == 0) wait_bar<3 * ahead>(); else wait_bar<2 * ahead>();
#pragma unroll
            for (int a = 0; a < 5; ++a) af[(st + 1) & 1][a] = lds_b128(ab + ((st + 1) & 3) * 20480 + a * 1024);
          }
          if (st + 4 < 16) { if (th == 0) issue(st + 4); }
          SBAR();
#pragma unroll
          for (int a = 0; a < 5; ++a) { acc[0][a] = MFMA16(w1r[st][0], af[st & 1][a], acc[0][a]); acc[1][a] = MFMA16(w1r[st][1], af[st & 1][a], acc[1][a]); }
          SBAR();
          if (st + 4 < 16) { if (th == 1) issue(st + 4); }
        });
        bar_lds();
#pragma unroll
        for (int i = 0; i < 2; ++i)
#pragma unroll
          for (int a = 0; a < 5; ++a) *(LAS f32x4*)(ldw + ((kq * 4 + 2 * th + i) * 5 + a) * 1024 + l16) = acc[i][a];
        bar_lds();
        const unsigned ub = WS_U + layer * UB + ((panel4 * U_KS + (f >> 5)) * 5) * 1024 + (c16 + 16 * ((f & 31) >> 3)) * 16 + (f & 7) * 2;
        f32x4 sv[3];
#pragma unroll
        for (int i = 0; i < 3; ++i) sv[i] = sum4(lds, ea * 5 + min(ew0 + i, 4), 20, lane);
#pragma unroll
        for (int i = 0; i < 3; ++i) {
          if (i < ewn) {
            const f32x4 s = sv[i];
            u32x2 w;
            w[0] = pack_bf16(gelu_tanh(s[0] + bf_lo(bias[0])), gelu_tanh(s[1] + bf_hi(bias[0])));
            w[1] = pack_bf16(gelu_tanh(s[2] + bf_lo(bias[1])), gelu_tanh(s[3] + bf_hi(bias[1])));
            st8_dev(rws, ub + (ew0 + i) * 1024, w);
          }
        }
        publish(ws, fl + FL_U + panel4 * 512 + ft * 4, 4, 128, tag, wave, lane);
      }
    }
    {
      FRESH();
      const int tok = tb * 64 + ea * 16 + c16;
      const float bias = __uint_as_float((unsigned)b2[tok] << 16);
      if (wave >= 4) {
        const int r = lane >> 3, pos = lane & 7;
        const unsigned pe = (((pos >> 1) ^ ((r >> 1) & 1)) << 1 | (pos & 1)) * 16;
        const unsigned v0 = r * (S_ * 2) + pe + 4096, v1 = (r + 8) * (S_ * 2) + (pe ^ 64) + 3072, vstep = 16 * (S_ * 2) - 2048;
        const char* wsrc = (const char*)(w2 + tb * 64) - 4096 + (long)kq * 32 * (S_ * 2);
#pragma unroll 1
        for (int st = 4; st < 9; ++st) glds4<false>(wsrc + (long)st * 128 * (S_ * 2), lds0 + (st - 4) * 16384 + kq * 4096, v0, v1, v0 + vstep, v1 + vstep);
        wait_vbar<32>();
        static_for<0, 13>([&](auto ST) {
          constexpr int st = decltype(ST)::v;
          constexpr int last = (st + 8 > 13) ? 13 : st + 8;
          wait_vbar<4 * (last - st - 1)>();
          if (st + 9 < 14) {
            const long o = (st + 9 == 13 && kq >= 2) ? (long)(2 * 13 - 1) * 64 * (S_ * 2) : (long)(st + 9) * 128 * (S_ * 2);
            glds4<false>(wsrc + o, lds0 + w2_slot(st + 9) + kq * 4096, v0, v1, v0 + vstep, v1 + vstep);
          }
        });
        asm volatile("s_barrier" ::: "memory");
      } else {
        if (wave == 0) poll_flags(ws, fl + FL_U + j * 512 + (tb & 3) * 128, 27, tag, lane, dead);
        asm volatile("s_barrier" ::: "memory");
        const unsigned abase = WS_U + layer * UB + j * (U_KS * 5 * 1024) + kq * 5120 + lane * 16;
        const int q = c16 >> 2, p = lane & 3;
        const unsigned fsw = (((q >> 1) & 1) | ((g & 1) << 1)) * 32;
        const lds_cptr wrow = lds + (32 * kq + 8 * g + q) * 128 + 8 * p;
        u32x4 af[3][5];
#pragma unroll
        for (int st = 0; st < 3; ++st)
#pragma unroll
          for (int a = 0; a < 5; ++a) af[st][a] = ld16_dev_s(rws, abase + (a & 3) * 1024, st * 20480 + (a >> 2) * 4096);
        f32x4 acc[4][5];
#pragma unroll
        for (int i = 0; i < 4; ++i)
#pragma unroll
          for (int a = 0; a < 5; ++a) acc[i][a] = f32x4{0.f, 0.f, 0.f, 0.f};
        bf16x8 wf[2][4];
#pragma unroll
        for (int i = 0; i < 4; ++i) wf[0][i] = lds_tr8(wrow + w2_slot(0) + ((i * 32) ^ fsw), 512);
        static_for<0, 14>([&](auto ST) {
          constexpr int st = decltype(ST)::v;
          if constexpr (st < 13) {
            bar_lds();
#pragma unroll
            for (int i = 0; i < 4; ++i) wf[(st + 1) & 1][i] = lds_tr8(wrow + w2_slot(st + 1) + ((i * 32) ^ fsw), 512);
          }
          if (st < 13 || kq < 2) {
            SBAR();
#pragma unroll
            for (int a = 0; a < 5; ++a) {
              const bf16x8 x = __builtin_bit_cast(bf16x8, af[st % 3][a]);
#pragma unroll
              for (int i = 0; i < 4; ++i) acc[i][a] = MFMA16(x, wf[st & 1][i], acc[i][a]);
              SBAR();
              if (st + 3 < 14) af[st % 3][a] = ld16_dev_s(rws, abase + (a & 3) * 1024, (st + 3) * 20480 + (a >> 2) * 4096);
              SBAR();
            }
          }
        });
        bar_lds();
#pragma unroll
        for (int i = 0; i < 4; ++i)
#pragma unroll
          for (int a = 0; a < 5; ++a) *(LAS f32x4*)(ldw + ((kq * 4 + i) * 5 + a) * 1024 + lane * 16) = acc[i][a];
      }
      bar_lds();
      f32x4 sv[3]; u32x2 rv[3];
#pragma unroll
      for (int i = 0; i < 3; ++i) { const int at = min(ew0 + i, 4); sv[i] = sum4(lds, ea * 5 + at, 20, lane); rv[i] = *(const LAS u32x2*)(lds + res_off + at * 32); }
#pragma unroll
      for (int i = 0; i < 3; ++i) {
        if (i < ewn) {
          const int at = ew0 + i;
          const f32x4 s = sv[i];
          const u32x2 r = rv[i];
          u32x2 w;
          w[0] = pack_bf16(bf_lo(r[0]) + (s[0] + bias), bf_hi(r[0]) + (s[1] + bias));
          w[1] = pack_bf16(bf_lo(r[1]) + (s[2] + bias), bf_hi(r[1]) + (s[3] + bias));
          const int d = j * 80 + at * 16 + 4 * g;
          *(LAS u32x2*)(ldw + res_off + at * 32) = w;
          if (layer == 0)
            st8_dev(rws, WS_X + tb * 81920 + (((d >> 5) * 4 + ea) * 64 + c16 + 16 * ((d & 31) >> 3)) * 16 + (d & 7) * 2, w);
        }
      }
      if (layer == 0) publish(ws, WS_FLAGS + FL_X + tb * 128 + j * 4, 1, 0, tag, wave, lane);
      else {
        bar_lds();
#pragma unroll
        for (int i = 0; i < 2; ++i) {
          const int n = i * 512 + wave * 64 + lane;
          if (n < 640) {
            const int row = n / 10, ch = n - row * 10;
            *(u32x4*)(out + (long)(tb * 64 + row) * D_ + j * 80 + ch * 8) = *(const LAS u32x4*)(lds + L_RES + n * 16);
          }
        }
      }
    }
  }
  if (blockIdx.x == 0 && tid == 0) __hip_atomic_store((gu32*)(ws + WS_EPOCH), tag, RLX_AGENT);
}

extern "C" void kernel_launch(void* const* d_in, const int* in_sizes, int n_in, void* d_out, int out_size, void* d_ws,
                              size_t ws_size, hipStream_t stream) {
  (void)hipFuncSetAttribute((const void*)mega_kernel, hipFuncAttributeMaxDynamicSharedMemorySize, LDS_BYTES);
  mega_kernel<<<256, 512, LDS_BYTES, stream>>>((const u16*)d_in[0], (const u16*)d_in[1], (const u16*)d_in[2], (const u16*)d_in[3],
                                               (const u16*)d_in[4], (const u16*)d_in[6], (const u16*)d_in[7], (const u16*)d_in[8],
                                               (const u16*)d_in[9], (u16*)d_out, (char*)d_ws);
}
```

```cpp
#include <hip/hip_runtime.h>
#include <hip/hip_bf16.h>
#include <stdint.h>

typedef __attribute__((ext_vector_type(8))) short bf16x8;
typedef __attribute__((ext_vector_type(4))) short bf16x4;
typedef __attribute__((ext_vector_type(16))) float f32x16;
typedef __attribute__((ext_vector_type(4))) float f32x4;
typedef __attribute__((ext_vector_type(2))) float f32x2;
typedef __attribute__((ext_vector_type(2))) __bf16 bf2_t;
typedef __attribute__((ext_vector_type(4))) unsigned u32x4;
typedef __attribute__((ext_vector_type(2))) unsigned u32x2;
typedef unsigned short u16;
#define LAS __attribute__((address_space(3)))
#define GAS __attribute__((address_space(1)))
typedef LAS const char* lds_cptr;
typedef LAS char* lds_ptr;
typedef GAS unsigned gu32;
typedef GAS unsigned long long gu64;
#define DI __device__ __forceinline__
#define RLX_AGENT __ATOMIC_RELAXED, __HIP_MEMORY_SCOPE_AGENT

constexpr int S_ = 2048, D_ = 640, DH_ = 20, F_ = 1728;
constexpr float LN_EPS = 1e-5f;
constexpr float QSCALE = 0.22360679774997896f * 1.4426950408889634f;

constexpr int KV_TILE = 2688, KV_KT = 1024, KV_V = 1280, KV_VS = 704;

constexpr unsigned WS_EPOCH = 0;
constexpr unsigned WS_FLAGS = 4096, FL_LAYER = 65536;
constexpr unsigned FL_KV = 0;
constexpr unsigned FL_O = 32768;
constexpr unsigned FL_X = 36864;
constexpr unsigned FL_H = 40960;
constexpr unsigned FL_U = 45056;
constexpr unsigned WS_STAT = 524288, ST_LAYER = 524288;
constexpr unsigned WS_KV = WS_STAT + 2 * ST_LAYER, KV_LAYER = 196608;
constexpr unsigned XB = S_ * D_ * 2;
constexpr unsigned WS_O = WS_KV + 2 * KV_LAYER;
constexpr unsigned WS_H = WS_O + 2 * XB;
constexpr int U_KS = 56;
constexpr unsigned UB = 8 * U_KS * 5 * 1024;
constexpr unsigned WS_U = WS_H + 2 * XB;
constexpr unsigned WS_X = WS_U + 2 * UB;
constexpr unsigned WS_END = WS_X + XB;

constexpr int L_W = 0;
constexpr int L_WT = 81920;
constexpr int L_RING = 81920;
constexpr int L_ATT = 102400;
constexpr int L_QT = 81920;
constexpr int L_KVP = 128064;
constexpr int L_STG = 134656;
constexpr int L_RES = 151040;
constexpr int L_DUMP = 161280;
constexpr int LDS_BYTES = 163840;
constexpr unsigned SPIN_CAP = 1u << 21;

DI unsigned pack_bf16(float lo, float hi) {
  f32x2 v = {lo, hi};
  bf2_t b = __builtin_convertvector(v, bf2_t);
  return __builtin_bit_cast(unsigned, b);
}
DI float bf_lo(unsigned u) { return __uint_as_float(u << 16); }
DI float bf_hi(unsigned u) { return __uint_as_float(u & 0xffff0000u); }
DI float rnd1(float v) { return bf_lo(pack_bf16(v, 0.f)); }
DI float ln_rsd(float var) { return __builtin_amdgcn_rcpf(rnd1(__builtin_amdgcn_sqrtf(rnd1(rnd1(var) + LN_EPS)))); }

#define GLDS_HEAD "s_mov_b32 m0, %1\n\ts_nop 4\n\t"
template <bool DEV> DI void glds1(const void* sbase, unsigned m0v, unsigned v0) {
  if (DEV) asm volatile(GLDS_HEAD "global_load_lds_dwordx4 %2, %0 sc1" ::"s"(sbase), "s"(m0v), "v"(v0) : "memory");
  else asm volatile(GLDS_HEAD "global_load_lds_dwordx4 %2, %0" ::"s"(sbase), "s"(m0v), "v"(v0) : "memory");
}
template <bool DEV> DI void glds4(const void* sbase, unsigned m0v, unsigned v0, unsigned v1, unsigned v2, unsigned v3) {
  if (DEV)
    asm volatile(GLDS_HEAD "global_load_lds_dwordx4 %2, %0 sc1\n\tglobal_load_lds_dwordx4 %3, %0 offset:1024 sc1\n\t"
                           "global_load_lds_dwordx4 %4, %0 offset:2048 sc1\n\tglobal_load_lds_dwordx4 %5, %0 offset:3072 sc1"
                 ::"s"(sbase), "s"(m0v), "v"(v0), "v"(v1), "v"(v2), "v"(v3) : "memory");
  else
    asm volatile(GLDS_HEAD "global_load_lds_dwordx4 %2, %0\n\tglobal_load_lds_dwordx4 %3, %0 offset:1024\n\t"
                           "global_load_lds_dwordx4 %4, %0 offset:2048\n\tglobal_load_lds_dwordx4 %5, %0 offset:3072"
                 ::"s"(sbase), "s"(m0v), "v"(v0), "v"(v1), "v"(v2), "v"(v3) : "memory");
}
template <int N, bool DEV> DI void glds_lin(const void* sbase, unsigned m0v, unsigned v) {
  if (N == 1) glds1<DEV>(sbase, m0v, v);
  else if (N == 4) glds4<DEV>(sbase, m0v, v, v, v, v);
  else if (N == 2) {
    if (DEV) asm volatile(GLDS_HEAD "global_load_lds_dwordx4 %2, %0 sc1\n\tglobal_load_lds_dwordx4 %2, %0 offset:1024 sc1" ::"s"(sbase), "s"(m0v), "v"(v) : "memory");
    else asm volatile(GLDS_HEAD "global_load_lds_dwordx4 %2, %0\n\tglobal_load_lds_dwordx4 %2, %0 offset:1024" ::"s"(sbase), "s"(m0v), "v"(v) : "memory");
  } else {
    if (DEV) asm volatile(GLDS_HEAD "global_load_lds_dwordx4 %2, %0 sc1\n\tglobal_load_lds_dwordx4 %2, %0 offset:1024 sc1\n\tglobal_load_lds_dwordx4 %2, %0 offset:2048 sc1" ::"s"(sbase), "s"(m0v), "v"(v) : "memory");
    else asm volatile(GLDS_HEAD "global_load_lds_dwordx4 %2, %0\n\tglobal_load_lds_dwordx4 %2, %0 offset:1024\n\tglobal_load_lds_dwordx4 %2, %0 offset:2048" ::"s"(sbase), "s"(m0v), "v"(v) : "memory");
  }
}
DI void glds2(const void* sbase, unsigned m0v, unsigned v0, unsigned v1) {
  asm volatile(GLDS_HEAD "global_load_lds_dwordx4 %2, %0\n\tglobal_load_lds_dwordx4 %3, %0 offset:1024" ::"s"(sbase), "s"(m0v), "v"(v0), "v"(v1) : "memory");
}
#define UNI(x) __builtin_amdgcn_readfirstlane(x)
template <int N> DI void wait_bar() { asm volatile("s_waitcnt vmcnt(%0) lgkmcnt(0)\n\ts_barrier" ::"i"(N) : "memory"); }
template <int N> DI void wait_vbar() { asm volatile("s_waitcnt vmcnt(%0)\n\ts_barrier" ::"i"(N) : "memory"); }
DI void bar_lds() { asm volatile("s_waitcnt lgkmcnt(0)\n\ts_barrier" ::: "memory"); }
typedef short v4i16_t __attribute__((ext_vector_type(4)));
DI bf16x4 lds_tr(lds_cptr p) {
  return __builtin_bit_cast(bf16x4, __builtin_amdgcn_ds_read_tr16_b64_v4i16((LAS v4i16_t*)p));
}
DI bf16x8 lds_tr8(lds_cptr p, int step) {
  const bf16x4 lo = lds_tr(p), hi = lds_tr(p + step);
  return __builtin_shufflevector(lo, hi, 0, 1, 2, 3, 4, 5, 6, 7);
}
DI bf16x8 lds_b128(lds_cptr p) { return *(const LAS bf16x8*)p; }
#define MFMA16(a, b, c) __builtin_amdgcn_mfma_f32_16x16x32_bf16((a), (b), (c), 0, 0, 0)
#define MFMA32(a, b, c) __builtin_amdgcn_mfma_f32_32x32x16_bf16((a), (b), (c), 0, 0, 0)
#define MFMA32K8(a, b, c) __builtin_amdgcn_mfma_f32_32x32x8bf16_1k((a), (b), (c), 0, 0, 0)
#define SBAR() __builtin_amdgcn_sched_barrier(0)
#define PIN(x) asm volatile("" : "+v"(x))
typedef __amdgpu_buffer_rsrc_t rsrc_t;
DI void st8_dev(rsrc_t r, unsigned off, u32x2 v) { __builtin_amdgcn_raw_buffer_store_b64(v, r, off, 0, 16); }
DI void st16_dev(rsrc_t r, unsigned off, u32x4 v) { __builtin_amdgcn_raw_buffer_store_b128(v, r, off, 0, 16); }
DI u32x4 merge_rows(u32x2 a, u32x2 b) {
  const auto x = __builtin_amdgcn_permlane16_swap(a[0], b[0], false, false);
  const auto y = __builtin_amdgcn_permlane16_swap(a[1], b[1], false, false);
  return u32x4{x[0], y[0], x[1], y[1]};
}
DI u32x4 ld16_dev(rsrc_t r, unsigned off) { return __builtin_amdgcn_raw_buffer_load_b128(r, off, 0, 16); }
DI u32x4 ld16_dev_s(rsrc_t r, unsigned off, unsigned soff) { return __builtin_amdgcn_raw_buffer_load_b128(r, off, soff, 16); }

DI void poll_flags(const char* ws, unsigned off, int n, unsigned tag, int lane, bool& dead) {
  gu32* f = (gu32*)(ws + off);
  unsigned spins = 0;
  while (!dead) {
    unsigned v = tag;
    if (lane < n) v = __hip_atomic_load(f + lane, RLX_AGENT);
    if (__all(v == tag)) break;
    if (++spins > SPIN_CAP) dead = true;
    __builtin_amdgcn_s_sleep(1);
  }
  asm volatile("" ::: "memory");
}
DI void publish(const char* ws, unsigned off, int nrep, unsigned rstride, unsigned tag, int wave, int lane) {
  wait_bar<0>();
  if (wave == 0 && lane < nrep) __hip_atomic_store((gu32*)(ws + off + lane * rstride), tag, RLX_AGENT);
}

DI float fmax3(float a, float b, float c) { return __builtin_fmaxf(__builtin_fmaxf(a, b), c); }
DI float rowmax16(const f32x16& s) {
  float a = fmax3(s[0], s[1], s[2]), b = fmax3(s[3], s[4], s[5]);
  a = fmax3(a, s[6], s[7]); b = fmax3(b, s[8], s[9]); a = fmax3(a, s[10], s[11]); b = fmax3(b, s[12], s[13]);
  a = fmax3(a, s[14], s[15]); a = __builtin_fmaxf(a, b);
  return __builtin_fmaxf(a, __shfl_xor(a, 32));
}
DI void pack_p(const f32x16& p, bf16x8& lo, bf16x8& hi) {
  u32x4 a, b;
#pragma unroll
  for (int i = 0; i < 4; ++i) { a[i] = pack_bf16(p[2 * i], p[2 * i + 1]); b[i] = pack_bf16(p[8 + 2 * i], p[9 + 2 * i]); }
  lo = __builtin_bit_cast(bf16x8, a); hi = __builtin_bit_cast(bf16x8, b);
}
DI float gelu_tanh(float u) {
  const float z = 0.7978845608028654f * (u + 0.044715f * u * u * u);
  const float e = __builtin_amdgcn_exp2f(-2.885390081777927f * z);
  return u * __builtin_amdgcn_rcpf(1.f + e);
}

DI void wslice_block(const u16* w, unsigned lds0, int lw, int lane, int blk) {
  const char* src = (const char*)w + lw * (160 * 1280);
  if (blk < 5) {
    const int r = lane >> 3, pos = lane & 7;
    const unsigned pe = (((pos >> 1) ^ ((r >> 1) & 1)) << 1 | (pos & 1)) * 16;
    const unsigned v0 = r * 1280 + pe, v1 = r * 1280 + (pe ^ 64) + 9216;
    glds4<false>(src + blk * (32 * 1280), lds0 + L_W + lw * 20480 + blk * 4096, v0, v1, v0 + 18432, v1 + 18432);
  } else {
    const unsigned t0 = (lane >> 1) * 1280 + 128 + (lane & 1) * 16;
    if (blk == 5) glds4<false>(src, lds0 + L_WT + lw * 5120, t0, t0 + 39936, t0 + 2 * 39936, t0 + 3 * 39936);
    else glds1<false>(src + 128 * 1280, lds0 + L_WT + lw * 5120 + 4096, t0);
  }
}

DI void touch(const void* sbase, unsigned voff, unsigned& sink) {
  asm volatile("s_nop 4\n\tglobal_load_dword %0, %1, %2" : "+v"(sink) : "v"(voff), "s"(sbase) : "memory");
}
DI float xsum16(float x) {
  auto r = __builtin_amdgcn_permlane16_swap(__float_as_uint(x), __float_as_uint(x), false, false);
  return __uint_as_float(r[0]) + __uint_as_float(r[1]);
}
DI float xsum32(float x) {
  auto r = __builtin_amdgcn_permlane32_swap(__float_as_uint(x), __float_as_uint(x), false, false);
  return __uint_as_float(r[0]) + __uint_as_float(r[1]);
}
DI void store_tile(rsrc_t rws, unsigned base, lds_cptr tile, int j, int wave, int lane) {
#pragma unroll
  for (int i = 0; i < 2; ++i) {
    const int n = (i * 8 + wave) * 4 + (lane >> 4);
    if (n < 40) {
      const int c8 = j * 10 + (n >> 2);
      const u32x4 v = *(const LAS u32x4*)(tile + ((n & 3) * 16 + (lane & 15)) * 160 + (n >> 2) * 16);
      st16_dev(rws, base + ((c8 >> 2) * 4 + (n & 3)) * 1024 + (c8 & 3) * 256 + (lane & 15) * 16, v);
    }
  }
}
DI f32x4 ldf4(lds_cptr p) { return *(const LAS f32x4*)p; }

DI f32x4 sum4(lds_cptr lds, int t, int nt, int lane) {
  f32x4 s = *(const LAS f32x4*)(lds + t * 1024 + lane * 16);
#pragma unroll
  for (int k = 1; k < 4; ++k) s += *(const LAS f32x4*)(lds + (k * nt + t) * 1024 + lane * 16);
  return s;
}
DI void ln_finish(float s1, float s2, float& mu, float& var) { mu = s1 * (1.f / D_); var = fmaxf(s2 * (1.f / D_) - mu * mu, 0.f); }

constexpr int ATT_CH = 4;
constexpr int ATT_CHB = ATT_CH * KV_TILE;
constexpr int ATT_SLOT = 10752;
constexpr int ATT_NCH = S_ / 32 / ATT_CH;
constexpr int w2_slot(int st) { return (st % 9) < 4 ? L_RING + (st % 9) * 16384 : ((st % 9) - 4) * 16384; }
template <int N> struct IC { static constexpr int v = N; };
template <int I, int N, class F> DI void static_for(F&& f) { if constexpr (I < N) { f(IC<I>{}); static_for<I + 1, N>(f); } }


__global__ __launch_bounds__(512, 2) void mega_kernel(const u16* __restrict__ x0, const u16* __restrict__ wq, const u16* __restrict__ wk,
                                                      const u16* __restrict__ wv, const u16* __restrict__ wo, const u16* __restrict__ w1,
                                                      const u16* __restrict__ b1, const u16* __restrict__ w2, const u16* __restrict__ b2,
                                                      u16* __restrict__ out, char* ws) {
  extern __shared__ __attribute__((aligned(16))) char smem[];
  const int tid = threadIdx.x;
  const unsigned lds0 = (unsigned)(uintptr_t)smem;
  const lds_cptr lds = (lds_cptr)smem;
  const lds_ptr ldw = (lds_ptr)smem;
#define FRESH() \
  int lane = tid & 63; asm volatile("" : "+v"(lane)); \
  int wave = UNI(tid >> 6); asm volatile("" : "+s"(wave)); \
  int bid = blockIdx.x; asm volatile("" : "+s"(bid)); \
  const int xcd = bid & 7, idx = bid >> 3, tb = xcd * 4 + (idx >> 3), j = idx & 7; \
  const int g = lane >> 4, c16 = lane & 15, kq = wave & 3, th = wave >> 2; \
  const int ea = wave & 3, ew0 = th ? 3 : 0, ewn = th ? 2 : 3; \
  const unsigned res_off = L_RES + (ea * 16 + c16) * 160 + 8 * g; \
  const int panel4 = (xcd & 1) * 4 + (idx & 3), ft = (xcd >> 1) * 7 + (idx >> 2); \
  const bool has4 = idx < 28 && ft < 27; \
  (void)xcd; (void)g; (void)c16; (void)kq; (void)th; (void)ea; (void)ew0; (void)ewn; (void)res_off; (void)tb; (void)j; (void)panel4; (void)has4
  const rsrc_t rws = __builtin_amdgcn_make_buffer_rsrc(ws, 0, WS_END, 0x00020000);
  unsigned tag = 0;
  bool dead = false;

#pragma unroll 1
  for (int layer = 0; layer < 2; ++layer) {
    const unsigned fl = WS_FLAGS + layer * FL_LAYER;
    const unsigned kvoff = WS_KV + layer * KV_LAYER;
    unsigned sink = 0;
    {
      FRESH();
      auto load_kvw = [&]() {
        const char* xs = (const char*)((j & 1) ? wv : wk);
        if (wave < 3) {
          glds_lin<4, false>(xs + wave * 8192, lds0 + L_ATT + wave * 8192, lane * 16);
          glds_lin<4, false>(xs + wave * 8192 + 4096, lds0 + L_ATT + wave * 8192 + 4096, lane * 16);
        } else glds1<false>(xs + 24576, lds0 + L_ATT + 24576, lane * 16);
      };
      u32x4 af[5][2];
      if (layer == 0) {
        const rsrc_t ra = __builtin_amdgcn_make_buffer_rsrc((void*)x0, 0, XB, 0x00020000);
        const unsigned abase = (tb * 64 + th * 32 + c16) * (D_ * 2) + kq * 64 + g * 16;
#pragma unroll
        for (int s = 0; s < 5; ++s)
#pragma unroll
          for (int t = 0; t < 2; ++t) af[s][t] = __builtin_amdgcn_raw_buffer_load_b128(ra, abase + s * 256, t * (16 * D_ * 2), 0);
        const unsigned epoch = __hip_atomic_load((gu32*)(ws + WS_EPOCH), RLX_AGENT);
        if (wave < 4) load_kvw();
        else touch(wq, min((idx * 4 + kq) * 50 + lane, 6399) * 128, sink);
        wait_vbar<0>();
        tag = UNI(epoch) + 1u;
        if (wave >= 4) {
#pragma unroll 1
          for (int blk = 0; blk < 7; ++blk) wslice_block(wq + j * 80, lds0, kq, lane, blk);
        } else {
#pragma unroll 1
          for (int pc = wave; pc < 10; pc += 4) {
            const int n = pc * 64 + lane, row = n / 10, ch = n - row * 10;
            glds1<false>(x0 + (long)tb * 64 * D_ + j * 80, lds0 + L_RES + pc * 1024, row * 1280 + ch * 16);
          }
        }
      } else {
        if (wave >= 4) {
#pragma unroll 1
          for (int blk = 0; blk < 7; ++blk) wslice_block(wq + j * 80, lds0, kq, lane, blk);
        } else load_kvw();
        if (wave == 0) poll_flags(ws, WS_FLAGS + FL_X + tb * 128, 8, tag, lane, dead);
        wait_vbar<0>();
        const unsigned abase = WS_X + tb * 81920 + (kq * 4 + th * 2) * 1024 + lane * 16;
#pragma unroll
        for (int s = 0; s < 5; ++s)
#pragma unroll
          for (int t = 0; t < 2; ++t) af[s][t] = ld16_dev_s(rws, abase + t * 1024, s * 16384);
      }
      bf16x8 ones;
#pragma unroll
      for (int i = 0; i < 8; ++i) ones[i] = (short)0x3f80;
      const f32x4 z4 = {0.f, 0.f, 0.f, 0.f};
      const int q = c16 >> 2, p = lane & 3;
      const int row = 32 * kq + 8 * g + q;
      const unsigned fsw = (((q >> 1) & 1) | ((g & 1) << 1)) * 32;
      const unsigned woff = L_W + row * 128 + 8 * p, toff = L_WT + row * 32 + 8 * p, xoff = L_ATT + row * 40 + 8 * p;
      LAS float* stat = (LAS float*)(ldw + L_DUMP);
      LAS float* csum = (LAS float*)(ldw + L_KVP + 20480);
      const int tk = j >> 1;
      const bool isv = j & 1;
      {
        f32x4 a1s[2] = {z4, z4}, a2s[2] = {z4, z4}, akv[2] = {z4, z4}, aok[2] = {z4, z4};
#pragma unroll
        for (int s = 0; s < 5; ++s) {
          const bf16x8 a0 = __builtin_bit_cast(bf16x8, af[s][0]), a1 = __builtin_bit_cast(bf16x8, af[s][1]);
          if (th == (tk >> 1)) {
            const bf16x8 a = (tk & 1) ? a1 : a0;
            const bf16x8 x0f = lds_tr8(lds + xoff + s * 5120, 160), x1f = lds_tr8(lds + xoff + s * 5120 + 32, 160);
            if (!isv) {
              akv[0] = MFMA16(x0f, a, akv[0]); akv[1] = MFMA16(x1f, a, akv[1]);
              aok[0] = MFMA16(x0f, ones, aok[0]); aok[1] = MFMA16(x1f, ones, aok[1]);
            } else {
              akv[0] = MFMA16(a, x0f, akv[0]); akv[1] = MFMA16(a, x1f, akv[1]);
              aok[0] = MFMA16(ones, x0f, aok[0]); aok[1] = MFMA16(ones, x1f, aok[1]);
            }
          }
          a1s[0] = MFMA16(ones, a0, a1s[0]); a1s[1] = MFMA16(ones, a1, a1s[1]);
          a2s[0] = MFMA16(a0, a0, a2s[0]); a2s[1] = MFMA16(a1, a1, a2s[1]);
        }
        if (g == q) {
#pragma unroll
          for (int t = 0; t < 2; ++t) {
            const float d = (p & 2) ? ((p & 1) ? a2s[t][3] : a2s[t][2]) : ((p & 1) ? a2s[t][1] : a2s[t][0]);
            *(LAS f32x2*)(stat + (kq * 64 + th * 32 + t * 16 + c16) * 2) = f32x2{a1s[t][0], d};
          }
        }
        if (th == (tk >> 1)) {
          const bool l2 = isv ? (c16 < 4) : (g == 0);
          const int i2 = isv ? g * 4 + c16 : c16;
          const int o = L_KVP + kq * 2560;
          *(LAS f32x4*)(ldw + o + lane * 16) = akv[0];
          *(LAS f32x4*)(ldw + o + 1280 + lane * 16) = aok[0];
          if (l2) { *(LAS f32x4*)(ldw + o + 1024 + i2 * 16) = akv[1]; *(LAS f32x4*)(ldw + o + 2304 + i2 * 16) = aok[1]; }
        }
      }
      wait_bar<0>();
      float mu, rs;
      {
        f32x2 sv[4];
#pragma unroll
        for (int k = 0; k < 4; ++k) sv[k] = *(const LAS f32x2*)(stat + (k * 64 + ea * 16 + c16) * 2);
        float var; ln_finish((sv[0][0] + sv[1][0]) + (sv[2][0] + sv[3][0]), (sv[0][1] + sv[1][1]) + (sv[2][1] + sv[3][1]), mu, var);
        mu = rnd1(mu); rs = ln_rsd(var);
      }
      if (ea == tk) {
        const int li = th == 0 ? lane : 64 + (isv ? g * 4 + (c16 & 3) : c16);
        f32x4 s = z4, cs = z4;
#pragma unroll
        for (int k = 0; k < 4; ++k) { s += ldf4(lds + L_KVP + k * 2560 + li * 16); cs += ldf4(lds + L_KVP + k * 2560 + 1280 + li * 16); }
        const unsigned tile = kvoff + (tb * 2 + (ea >> 1)) * KV_TILE;
        if (!isv) {
          u32x2 w; w[0] = pack_bf16((s[0] - mu * cs[0]) * rs, (s[1] - mu * cs[1]) * rs); w[1] = pack_bf16((s[2] - mu * cs[2]) * rs, (s[3] - mu * cs[3]) * rs);
          const int r32 = (ea & 1) * 16 + c16;
          if (th == 0) st8_dev(rws, tile + ((g >> 1) * 32 + r32) * 16 + (g & 1) * 8, w);
          else if (g == 0) st8_dev(rws, tile + KV_KT + r32 * 8, w);
        } else {
          float o[4];
#pragma unroll
          for (int e = 0; e < 4; ++e) o[e] = (s[e] - __shfl(mu, 4 * g + e) * cs[e]) * __shfl(rs, 4 * g + e);
          const int d = th * 16 + c16;
          u32x2 w; w[0] = pack_bf16(o[0], o[1]); w[1] = pack_bf16(o[2], o[3]);
          if (d == DH_) { w[0] = 0x3f803f80u; w[1] = 0x3f803f80u; }
          if (d == DH_ + 1) { w[0] = 0u; w[1] = 0u; }
          if (d <= DH_ + 1) st8_dev(rws, tile + KV_V + (ea & 1) * KV_VS + ((g & 1) * 22 + d) * 16 + (g >> 1) * 8, w);
        }
      }
      f32x4 acc[5][2], aon[3] = {z4, z4, z4};
#pragma unroll
      for (int i = 0; i < 5; ++i) { acc[i][0] = z4; acc[i][1] = z4; }
      bf16x8 wq2[2][5];
#pragma unroll
      for (int i = 0; i < 5; ++i) wq2[0][i] = i < 4 ? lds_tr8(lds + woff + ((i * 32) ^ fsw), 512) : lds_tr8(lds + toff, 128);
#pragma unroll
      for (int s = 0; s < 5; ++s) {
        const bf16x8 a0 = __builtin_bit_cast(bf16x8, af[s][0]), a1 = __builtin_bit_cast(bf16x8, af[s][1]);
        const bf16x8* wf = wq2[s & 1];
#pragma unroll
        for (int i = 0; i < 5; ++i) {
          if (s < 4) wq2[(s + 1) & 1][i] = i < 4 ? lds_tr8(lds + woff + (s + 1) * 16384 + ((i * 32) ^ fsw), 512) : lds_tr8(lds + toff + (s + 1) * 4096, 128);
          acc[i][0] = MFMA16(wf[i], a0, acc[i][0]); acc[i][1] = MFMA16(wf[i], a1, acc[i][1]);
          SBAR();
        }
        if (th == 0) { aon[0] = MFMA16(wf[0], ones, aon[0]); aon[1] = MFMA16(wf[1], ones, aon[1]); aon[2] = MFMA16(wf[2], ones, aon[2]); }
        else { aon[0] = MFMA16(wf[3], ones, aon[0]); aon[1] = MFMA16(wf[4], ones, aon[1]); }
        if (s == 1) {
          publish(ws, fl + FL_KV + (tb * 8 + j) * 4, 32, 1024, tag, wave, lane);
          if (wave >= 4) {
            touch((const char*)(w2 + tb * 64) + (long)(j * 216 + kq * 54) * (S_ * 2), (lane < 54 ? lane : 53) * (S_ * 2), sink);
          }
        }
      }
      bar_lds();
#pragma unroll
      for (int i = 0; i < 5; ++i)
#pragma unroll
        for (int t = 0; t < 2; ++t) *(LAS f32x4*)(ldw + ((kq * 5 + i) * 4 + th * 2 + t) * 1024 + lane * 16) = acc[i][t];
      if (c16 == 0) {
#pragma unroll
        for (int i = 0; i < 3; ++i) if (i < ewn) *(LAS f32x4*)(csum + (kq * 7 + ew0 + i) * 16 + 4 * g) = aon[i];
      }
      bar_lds();
      {
        const float r = rs * QSCALE;
        f32x4 s[3], cs[3];
#pragma unroll
        for (int i = 0; i < 3; ++i) {
          const int wt = min(ew0 + i, 4);
          s[i] = sum4(lds, wt * 4 + ea, 20, lane);
          cs[i] = z4;
#pragma unroll
          for (int k = 0; k < 4; ++k) cs[i] += *(const LAS f32x4*)(csum + (k * 7 + wt) * 16 + 4 * g);
        }
#pragma unroll
        for (int i = 0; i < 3; ++i) {
          if (i < ewn) {
            u32x2 w;
            w[0] = pack_bf16((s[i][0] - mu * cs[i][0]) * r, (s[i][1] - mu * cs[i][1]) * r);
            w[1] = pack_bf16((s[i][2] - mu * cs[i][2]) * r, (s[i][3] - mu * cs[i][3]) * r);
            *(LAS u32x2*)(ldw + L_QT + (ea * 16 + c16) * 160 + (ew0 + i) * 32 + 8 * g) = w;
          }
        }
      }
      bar_lds();
    }
    bf16x8 w1r[16][2];
    {
      FRESH();
      const int r32 = lane & 31, hi = lane >> 5;
      const int hh = wave & 3, trow = th * 32 + r32;
      bf16x8 q16; bf16x4 q4;
      {
        const lds_cptr qp = lds + L_QT + trow * 160 + hh * 40;
        const u32x2 a = *(const LAS u32x2*)(qp + hi * 16), b = *(const LAS u32x2*)(qp + hi * 16 + 8), c = *(const LAS u32x2*)(qp + 32);
        u32x4 v = {a[0], a[1], b[0], b[1]};
        q16 = __builtin_bit_cast(bf16x8, v);
        u32x2 t = {hi ? 0u : c[0], hi ? 0u : c[1]};
        q4 = __builtin_bit_cast(bf16x4, t);
        PIN(q16); PIN(q4);
      }
      if (wave == 0) {
        unsigned spins = 0;
        while (!dead) {
          const u32x4 v = ld16_dev(rws, fl + FL_KV + (bid & 31) * 1024 + lane * 16);
          if (__all(v[0] == tag && v[1] == tag && v[2] == tag && v[3] == tag)) break;
          if (++spins > SPIN_CAP) dead = true;
          __builtin_amdgcn_s_sleep(1);
          asm volatile("" ::: "memory");
        }
      }
      bar_lds();
      const unsigned koff = lane * 16;
      const char* KV = ws + kvoff + wave * 3072;
      auto issue_kv = [&](int ch) {
        const unsigned dst = lds0 + L_ATT + (ch % 3) * ATT_SLOT + wave * 3072;
        if (wave < 3) glds_lin<3, true>(KV + ch * ATT_CHB, dst, koff);
        else { glds1<true>(KV + ch * ATT_CHB, dst, koff); if (lane < 32) glds1<true>(KV + ch * ATT_CHB + 1024, dst + 1024, koff); }
      };
      const char* w1s = (const char*)(w1 + (has4 ? ft : 0) * 64) + (long)wave * 16 * (F_ * 2);
      unsigned sv0, sv1;
      {
        const int r = lane >> 3, pos = lane & 7;
        const unsigned pe = (((pos >> 1) ^ ((r >> 1) & 1)) << 1 | (pos & 1)) * 16;
        sv0 = r * (F_ * 2) + pe; sv1 = (r + 8) * (F_ * 2) + (pe ^ 64) - 1024;
      }
      const unsigned fsw = (((c16 >> 3) & 1) | ((g & 1) << 1)) * 32;
      const unsigned srow = (32 * kq + 8 * g + (c16 >> 2)) * 128 + 8 * (lane & 3);
      const unsigned sr0 = srow + ((64 * th) ^ fsw), sr1 = srow + ((64 * th + 32) ^ fsw);
      if (wave < 4) { issue_kv(0); issue_kv(1); }
      glds2(w1s, lds0 + L_STG + wave * 2048, sv0, sv1);
      const unsigned ktoff = KV_KT + r32 * 8, voff = KV_V + (hi * 22 + (r32 < 21 ? r32 : 21)) * 16;
      f32x16 o = {}, negm, sA, sB;
      bf16x8 k16, v0, v1; bf16x4 k4; u32x4 p0, p1;
      if (wave < 3) wait_bar<5>(); else if (wave == 3) wait_bar<4>(); else bar_lds();
      const lds_cptr ring = lds + L_ATT;
      {
        k16 = lds_b128(ring + koff);
        k4 = *(const LAS bf16x4*)(ring + ktoff);
        f32x16 z = {};
        z = MFMA32(k16, q16, z); z = MFMA32K8(k4, q4, z);
        k16 = lds_b128(ring + KV_TILE + koff);
        k4 = *(const LAS bf16x4*)(ring + KV_TILE + ktoff);
        v1 = lds_b128(ring + voff);
        const float m = rowmax16(z);
#pragma unroll
        for (int i = 0; i < 16; ++i) { negm[i] = -m; sA[i] = z[i] - m; }
        p1 = u32x4{0u, 0u, 0u, 0u};
      }
#define EX(x) __builtin_amdgcn_exp2f(x)
#define EXP4(X, i) do { X[i] = EX(X[i]); X[i + 1] = EX(X[i + 1]); X[i + 2] = EX(X[i + 2]); X[i + 3] = EX(X[i + 3]); } while (0)
#define BF8(x) __builtin_bit_cast(bf16x8, x)
#define PK4(P, X, i) do { P[0] = pack_bf16(X[i], X[i + 1]); P[1] = pack_bf16(X[i + 2], X[i + 3]); P[2] = pack_bf16(X[i + 4], X[i + 5]); P[3] = pack_bf16(X[i + 6], X[i + 7]); } while (0)
#define STEP(C, N, kn, vc) do { \
    N = MFMA32(k16, q16, negm); SBAR(); \
    v0 = lds_b128((vc) + voff); EXP4(C, 0); PIN(C); SBAR(); \
    o = MFMA32(v1, BF8(p1), o); SBAR(); \
    k16 = lds_b128((kn) + koff); EXP4(C, 4); PIN(C); SBAR(); PK4(p0, C, 0); PIN(p0); SBAR(); \
    N = MFMA32K8(k4, q4, N); SBAR(); \
    v1 = lds_b128((vc) + voff + KV_VS); EXP4(C, 8); PIN(C); SBAR(); \
    o = MFMA32(v0, BF8(p0), o); SBAR(); \
    k4 = *(const LAS bf16x4*)((kn) + ktoff); EXP4(C, 12); PIN(C); SBAR(); PK4(p1, C, 8); PIN(p1); SBAR(); \
  } while (0)
      static_for<0, ATT_NCH>([&](auto CH) {
        constexpr int ch = decltype(CH)::v;
        const lds_cptr cb = ring + (ch % 3) * ATT_SLOT;
        const lds_cptr nb = (ch + 1 < ATT_NCH) ? ring + ((ch + 1) % 3) * ATT_SLOT : cb;
        __builtin_amdgcn_s_setprio(1);
        STEP(sA, sB, cb + 2 * KV_TILE, cb);
        __builtin_amdgcn_s_setprio(0);
        STEP(sB, sA, cb + 3 * KV_TILE, cb + KV_TILE);
        if (wave < 4) wait_vbar<(ch >= 1 && ch <= 5) ? 4 : 0>(); else wait_vbar<0>();
        if (ch + 2 < ATT_NCH) { if (wave < 4) issue_kv(ch + 2); }
        if (ch + 1 < 16) glds2(w1s + (long)(ch + 1) * 128 * (F_ * 2), lds0 + ((ch & 1) ? L_STG : L_WT) + wave * 2048, sv0, sv1);
        if (ch < 5) { if (wave < 4) wslice_block(wo + j * 80, lds0, wave, lane, ch); }
        w1r[ch][0] = lds_tr8(lds + ((ch & 1) ? L_WT : L_STG) + sr0, 512); w1r[ch][1] = lds_tr8(lds + ((ch & 1) ? L_WT : L_STG) + sr1, 512);
        __builtin_amdgcn_s_setprio(3);
        SBAR();
        STEP(sA, sB, nb, cb + 2 * KV_TILE);
        PIN(w1r[ch][0]); PIN(w1r[ch][1]);
        __builtin_amdgcn_s_setprio(2);
        SBAR();
        STEP(sB, sA, nb + KV_TILE, cb + 3 * KV_TILE);
      });
      __builtin_amdgcn_s_setprio(0);
      o = MFMA32(v1, BF8(p1), o);
      float l = xsum32(hi ? o[8] : 0.f);
      bool bad = !(l < 1.8446744e19f);
#pragma unroll
      for (int i = 0; i < 12; ++i) bad |= !(__builtin_fabsf(o[i]) < 3.0e38f);
      if (__builtin_expect(__any(bad), 0)) {
        float m = -INFINITY;
        o = f32x16{};
#pragma unroll 1
        for (int t = 0; t < S_ / 32; ++t) {
          const unsigned tp = kvoff + t * KV_TILE;
          const bf16x8 a16 = BF8(ld16_dev(rws, tp + koff));
          const u32x2 a4 = __builtin_amdgcn_raw_buffer_load_b64(rws, tp + ktoff, 0, 16);
          const bf16x8 w0 = BF8(ld16_dev(rws, tp + voff)), w1f = BF8(ld16_dev(rws, tp + voff + KV_VS));
          f32x16 z = {};
          z = MFMA32(a16, q16, z); z = MFMA32K8(__builtin_bit_cast(bf16x4, a4), q4, z);
          const float mn = __builtin_fmaxf(m, rowmax16(z));
          const float alpha = __builtin_amdgcn_exp2f(m - mn);
          m = mn;
#pragma unroll
          for (int i = 0; i < 16; ++i) { z[i] = __builtin_amdgcn_exp2f(z[i] - m); o[i] *= alpha; }
          bf16x8 a0, a1; pack_p(z, a0, a1);
          o = MFMA32(w0, a0, o); o = MFMA32(w1f, a1, o);
        }
        l = xsum32(hi ? o[8] : 0.f);
      }
      const float rl = __builtin_amdgcn_rcpf(l);
#pragma unroll
      for (int gq = 0; gq < 3; ++gq) {
        const int d0 = 8 * gq + 4 * hi;
        if (d0 < DH_) {
          u32x2 w; w[0] = pack_bf16(o[4 * gq] * rl, o[4 * gq + 1] * rl); w[1] = pack_bf16(o[4 * gq + 2] * rl, o[4 * gq + 3] * rl);
          *(LAS u32x2*)(ldw + L_STG + trow * 160 + hh * 40 + d0 * 2) = w;
        }
      }
      bar_lds();
      store_tile(rws, WS_O + layer * XB + tb * 81920, lds + L_STG, j, wave, lane);
      publish(ws, fl + FL_O + tb * 128 + j * 4, 1, 0, tag, wave, lane);
      asm volatile("" :: "v"(sink));
      if (wave >= 4) { wslice_block(wo + j * 80, lds0, kq, lane, 5); wslice_block(wo + j * 80, lds0, kq, lane, 6); }
    }
    {
      FRESH();
      if (wave == 0) poll_flags(ws, fl + FL_O + tb * 128, 8, tag, lane, dead);
      wait_vbar<0>();
      const unsigned abase = WS_O + layer * XB + tb * 81920 + (kq * 4 + th * 2) * 1024 + lane * 16;
      u32x4 af[5][2];
#pragma unroll
      for (int s = 0; s < 5; ++s)
#pragma unroll
        for (int t = 0; t < 2; ++t) af[s][t] = ld16_dev_s(rws, abase + t * 1024, s * 16384);
      f32x4 acc[5][2];
#pragma unroll
      for (int i = 0; i < 5; ++i) { acc[i][0] = f32x4{0.f, 0.f, 0.f, 0.f}; acc[i][1] = acc[i][0]; }
      const int q = c16 >> 2, p = lane & 3;
      const int row = 32 * kq + 8 * g + q;
      const unsigned fsw = (((q >> 1) & 1) | ((g & 1) << 1)) * 32;
      const unsigned woff = L_W + row * 128 + 8 * p, toff = L_WT + row * 32 + 8 * p;
      bf16x8 wf[2][5];
#pragma unroll
      for (int i = 0; i < 5; ++i) wf[0][i] = i < 4 ? lds_tr8(lds + woff + ((i * 32) ^ fsw), 512) : lds_tr8(lds + toff, 128);
#pragma unroll
      for (int s = 0; s < 5; ++s) {
        const bf16x8 a0 = __builtin_bit_cast(bf16x8, af[s][0]), a1 = __builtin_bit_cast(bf16x8, af[s][1]);
#pragma unroll
        for (int i = 0; i < 5; ++i) {
          if (s < 4) wf[(s + 1) & 1][i] = i < 4 ? lds_tr8(lds + woff + (s + 1) * 16384 + ((i * 32) ^ fsw), 512) : lds_tr8(lds + toff + (s + 1) * 4096, 128);
          acc[i][0] = MFMA16(wf[s & 1][i], a0, acc[i][0]); acc[i][1] = MFMA16(wf[s & 1][i], a1, acc[i][1]);
          SBAR();
        }
      }
      bar_lds();
#pragma unroll
      for (int i = 0; i < 5; ++i)
#pragma unroll
        for (int t = 0; t < 2; ++t) *(LAS f32x4*)(ldw + ((kq * 5 + i) * 4 + th * 2 + t) * 1024 + lane * 16) = acc[i][t];
      bar_lds();
      gu64* gr = (gu64*)(ws + WS_STAT + layer * ST_LAYER + (tb * 4 + ea) * 4096);
      {
        f32x4 s[3]; u32x2 r[3];
#pragma unroll
        for (int i = 0; i < 3; ++i) {
          const int wt = min(ew0 + i, 4);
          s[i] = sum4(lds, wt * 4 + ea, 20, lane);
          r[i] = *(const LAS u32x2*)(lds + res_off + wt * 32);
        }
        float s1 = 0.f, s2 = 0.f;
#pragma unroll
        for (int i = 0; i < 3; ++i) {
          if (i < ewn) {
            u32x2 w;
            w[0] = pack_bf16(bf_lo(r[i][0]) + s[i][0], bf_hi(r[i][0]) + s[i][1]);
            w[1] = pack_bf16(bf_lo(r[i][1]) + s[i][2], bf_hi(r[i][1]) + s[i][3]);
            *(LAS u32x2*)(ldw + res_off + (ew0 + i) * 32) = w;
            const float a = bf_lo(w[0]), b = bf_hi(w[0]), c = bf_lo(w[1]), d = bf_hi(w[1]);
            s1 += (a + b) + (c + d);
            s2 += (a * a + b * b) + (c * c + d * d);
          }
        }
        s1 = xsum32(xsum16(s1)); s2 = xsum32(xsum16(s2));
        if (lane < 32) __hip_atomic_store(gr + (2 * j + th) * 32 + lane, ((unsigned long long)tag << 32) | __float_as_uint(g ? s2 : s1), RLX_AGENT);
      }
      bar_lds();
      if (wave >= 4) {
        const int r = lane >> 3, pos = lane & 7;
        const unsigned pe = (((pos >> 1) ^ ((r >> 1) & 1)) << 1 | (pos & 1)) * 16;
        const unsigned v0 = r * (S_ * 2) + pe + 4096, v1 = (r + 8) * (S_ * 2) + (pe ^ 64) + 3072, vstep = 16 * (S_ * 2) - 2048;
        const char* wsrc = (const char*)(w2 + tb * 64) - 4096 + (long)kq * 32 * (S_ * 2);
#pragma unroll 1
        for (int st = 0; st < 4; ++st) glds4<false>(wsrc + (long)st * 128 * (S_ * 2), lds0 + L_RING + st * 16384 + kq * 4096, v0, v1, v0 + vstep, v1 + vstep);
        asm volatile("s_barrier" ::: "memory");
      } else {
        u32x2 xv[5];
#pragma unroll
        for (int wt = 0; wt < 5; ++wt) xv[wt] = __builtin_bit_cast(u32x2, lds_tr(lds + L_RES + (ea * 16 + 4 * g + q) * 160 + wt * 32 + 8 * p));
        float tot;
        {
          unsigned spins = 0;
          for (;;) {
            bool ok = true; tot = 0.f;
#pragma unroll
            for (int i = 0; i < 8; ++i) {
              const unsigned long long v = __hip_atomic_load(gr + i * 64 + lane, RLX_AGENT);
              tot += __uint_as_float((unsigned)v); ok &= (unsigned)(v >> 32) == tag;
            }
            if (__all(ok) || dead) break;
            if (++spins > SPIN_CAP) dead = true;
            __builtin_amdgcn_s_sleep(1);
          }
        }
        tot = xsum32(tot);
        float mu4[4], rr4[4], m24[4], r24[4];
#pragma unroll
        for (int e = 0; e < 4; ++e) { mu4[e] = __shfl(tot, 4 * g + e); rr4[e] = __shfl(tot, 16 + 4 * g + e); }
#pragma unroll
        for (int e = 0; e < 4; ++e) {
          float mu, var; ln_finish(mu4[e], rr4[e], mu, var);
          mu4[e] = rnd1(mu); rr4[e] = ln_rsd(var);
          m24[e] = rnd1((mu - mu4[e]) * rr4[e]); r24[e] = ln_rsd(var * rr4[e] * rr4[e]);
        }
        const unsigned hb = WS_H + layer * XB + j * 327680 + ((tb * 2 + (ea >> 1)) * 5) * 1024 + (c16 + 16 * ((ea & 1) * 2 + (g >> 1))) * 16;
        u32x2 hw[5];
#pragma unroll
        for (int wt = 0; wt < 5; ++wt) {
          const u32x2 v = xv[wt];
          u32x2 w;
          w[0] = pack_bf16(bf_lo(v[0]) - mu4[0], bf_hi(v[0]) - mu4[1]); w[1] = pack_bf16(bf_lo(v[1]) - mu4[2], bf_hi(v[1]) - mu4[3]);
          w[0] = pack_bf16(bf_lo(w[0]) * rr4[0], bf_hi(w[0]) * rr4[1]); w[1] = pack_bf16(bf_lo(w[1]) * rr4[2], bf_hi(w[1]) * rr4[3]);
          w[0] = pack_bf16(bf_lo(w[0]) - m24[0], bf_hi(w[0]) - m24[1]); w[1] = pack_bf16(bf_lo(w[1]) - m24[2], bf_hi(w[1]) - m24[3]);
          w[0] = pack_bf16(bf_lo(w[0]) * r24[0], bf_hi(w[0]) * r24[1]); w[1] = pack_bf16(bf_lo(w[1]) * r24[2], bf_hi(w[1]) * r24[3]);
          hw[wt] = w;
        }
        st16_dev(rws, hb + (g & 1) * 1024, merge_rows(hw[0], hw[1]));
        st16_dev(rws, hb + (2 + (g & 1)) * 1024, merge_rows(hw[2], hw[3]));
        st8_dev(rws, hb + 4096 + (g & 1) * 8, hw[4]);
        publish(ws, fl + FL_H + j * 512 + tb * 4, 4, 128, tag, wave, lane);
      }
    }
    {
      FRESH();
      if (has4) {
        const int f = ft * 64 + ea * 16 + 4 * g;
        const u32x2 bias = *(const u32x2*)(b1 + f);
        if (wave == 0) poll_flags(ws, fl + FL_H + panel4 * 512 + ((idx >> 2) & 3) * 128, 32, tag, lane, dead);
        asm volatile("s_barrier" ::: "memory");
        const unsigned l16 = lane * 16;
        const int po = (th ? 12 + 2 * kq : 3 * kq) * 1024;
        const char* hsrc = ws + WS_H + layer * XB + panel4 * 327680 + po;
        auto issue = [&](int st) {
          if (th == 0) glds_lin<3, true>(hsrc + st * 20480, lds0 + (st & 3) * 20480 + po, l16);
          else glds_lin<2, true>(hsrc + st * 20480, lds0 + (st & 3) * 20480 + po, l16);
        };
        issue(0); issue(1); issue(2); issue(3);
        f32x4 acc[2][5];
#pragma unroll
        for (int i = 0; i < 2; ++i)
#pragma unroll
          for (int a = 0; a < 5; ++a) acc[i][a] = f32x4{0.f, 0.f, 0.f, 0.f};
        const lds_cptr ab = lds + kq * 5120 + l16;
        bf16x8 af[2][5];
        if (th == 0) wait_vbar<9>(); else wait_vbar<6>();
#pragma unroll
        for (int a = 0; a < 5; ++a) af[0][a] = lds_b128(ab + a * 1024);
        static_for<0, 16>([&](auto ST) {
          constexpr int st = decltype(ST)::v;
          if constexpr (st < 15) {
            constexpr int ahead = (14 - st) < 2 ? (14 - st) : 2;
            if (th == 0) wait_bar<3 * ahead>(); else wait_bar<2 * ahead>();
#pragma unroll
            for (int a = 0; a < 5; ++a) af[(st + 1) & 1][a] = lds_b128(ab + ((st + 1) & 3) * 20480 + a * 1024);
          }
          if (st + 4 < 16) { if (th == 0) issue(st + 4); }
          SBAR();
#pragma unroll
          for (int a = 0; a < 5; ++a) { acc[0][a] = MFMA16(w1r[st][0], af[st & 1][a], acc[0][a]); acc[1][a] = MFMA16(w1r[st][1], af[st & 1][a], acc[1][a]); }
          SBAR();
          if (st + 4 < 16) { if (th == 1) issue(st + 4); }
        });
        bar_lds();
#pragma unroll
        for (int i = 0; i < 2; ++i)
#pragma unroll
          for (int a = 0; a < 5; ++a) *(LAS f32x4*)(ldw + ((kq * 4 + 2 * th + i) * 5 + a) * 1024 + l16) = acc[i][a];
        bar_lds();
        const unsigned ub = WS_U + layer * UB + ((panel4 * U_KS + (f >> 5)) * 5) * 1024 + (c16 + 16 * ((f & 31) >> 3)) * 16;
        u32x2 uw[3];
        f32x4 sv[3];
#pragma unroll
        for (int i = 0; i < 3; ++i) sv[i] = sum4(lds, ea * 5 + min(ew0 + i, 4), 20, lane);
#pragma unroll
        for (int i = 0; i < 3; ++i) {
          if (i < ewn) {
            const f32x4 s = sv[i];
            u32x2 w;
            w[0] = pack_bf16(gelu_tanh(s[0] + bf_lo(bias[0])), gelu_tanh(s[1] + bf_hi(bias[0])));
            w[1] = pack_bf16(gelu_tanh(s[2] + bf_lo(bias[1])), gelu_tanh(s[3] + bf_hi(bias[1])));
            uw[i] = w;
          }
        }
        st16_dev(rws, ub + (ew0 + (g & 1)) * 1024, merge_rows(uw[0], uw[1]));
        if (th == 0) st8_dev(rws, ub + 2048 + (g & 1) * 8, uw[2]);
        publish(ws, fl + FL_U + panel4 * 512 + ft * 4, 4, 128, tag, wave, lane);
      }
    }
    {
      FRESH();
      const int tok = tb * 64 + ea * 16 + c16;
      const float bias = __uint_as_float((unsigned)b2[tok] << 16);
      if (wave >= 4) {
        const int r = lane >> 3, pos = lane & 7;
        const unsigned pe = (((pos >> 1) ^ ((r >> 1) & 1)) << 1 | (pos & 1)) * 16;
        const unsigned v0 = r * (S_ * 2) + pe + 4096, v1 = (r + 8) * (S_ * 2) + (pe ^ 64) + 3072, vstep = 16 * (S_ * 2) - 2048;
        const char* wsrc = (const char*)(w2 + tb * 64) - 4096 + (long)kq * 32 * (S_ * 2);
#pragma unroll 1
        for (int st = 4; st < 9; ++st) glds4<false>(wsrc + (long)st * 128 * (S_ * 2), lds0 + (st - 4) * 16384 + kq * 4096, v0, v1, v0 + vstep, v1 + vstep);
        wait_vbar<32>();
        static_for<0, 13>([&](auto ST) {
          constexpr int st = decltype(ST)::v;
          constexpr int last = (st + 8 > 13) ? 13 : st + 8;
          wait_vbar<4 * (last - st - 1)>();
          if (st + 9 < 14) {
            const long o = (st + 9 == 13 && kq >= 2) ? (long)(2 * 13 - 1) * 64 * (S_ * 2) : (long)(st + 9) * 128 * (S_ * 2);
            glds4<false>(wsrc + o, lds0 + w2_slot(st + 9) + kq * 4096, v0, v1, v0 + vstep, v1 + vstep);
          }
        });
        asm volatile("s_barrier" ::: "memory");
      } else {
        if (wave == 0) poll_flags(ws, fl + FL_U + j * 512 + (tb & 3) * 128, 27, tag, lane, dead);
        asm volatile("s_barrier" ::: "memory");
        const unsigned abase = WS_U + layer * UB + j * (U_KS * 5 * 1024) + kq * 5120 + lane * 16;
        const int q = c16 >> 2, p = lane & 3;
        const unsigned fsw = (((q >> 1) & 1) | ((g & 1) << 1)) * 32;
        const lds_cptr wrow = lds + (32 * kq + 8 * g + q) * 128 + 8 * p;
        u32x4 af[3][5];
#pragma unroll
        for (int st = 0; st < 3; ++st)
#pragma unroll
          for (int a = 0; a < 5; ++a) af[st][a] = ld16_dev_s(rws, abase + (a & 3) * 1024, st * 20480 + (a >> 2) * 4096);
        f32x4 acc[4][5];
#pragma unroll
        for (int i = 0; i < 4; ++i)
#pragma unroll
          for (int a = 0; a < 5; ++a) acc[i][a] = f32x4{0.f, 0.f, 0.f, 0.f};
        bf16x8 wf[2][4];
#pragma unroll
        for (int i = 0; i < 4; ++i) wf[0][i] = lds_tr8(wrow + w2_slot(0) + ((i * 32) ^ fsw), 512);
        static_for<0, 14>([&](auto ST) {
          constexpr int st = decltype(ST)::v;
          if constexpr (st < 13) {
            bar_lds();
#pragma unroll
            for (int i = 0; i < 4; ++i) wf[(st + 1) & 1][i] = lds_tr8(wrow + w2_slot(st + 1) + ((i * 32) ^ fsw), 512);
          }
          if (st < 13 || kq < 2) {
            SBAR();
#pragma unroll
            for (int a = 0; a < 5; ++a) {
              const bf16x8 x = __builtin_bit_cast(bf16x8, af[st % 3][a]);
#pragma unroll
              for (int i = 0; i < 4; ++i) acc[i][a] = MFMA16(x, wf[st & 1][i], acc[i][a]);
              SBAR();
              if (st + 3 < 14) af[st % 3][a] = ld16_dev_s(rws, abase + (a & 3) * 1024, (st + 3) * 20480 + (a >> 2) * 4096);
              SBAR();
            }
          }
        });
        bar_lds();
#pragma unroll
        for (int i = 0; i < 4; ++i)
#pragma unroll
          for (int a = 0; a < 5; ++a) *(LAS f32x4*)(ldw + ((kq * 4 + i) * 5 + a) * 1024 + lane * 16) = acc[i][a];
      }
      bar_lds();
      f32x4 sv[3]; u32x2 rv[3];
#pragma unroll
      for (int i = 0; i < 3; ++i) { const int at = min(ew0 + i, 4); sv[i] = sum4(lds, ea * 5 + at, 20, lane); rv[i] = *(const LAS u32x2*)(lds + res_off + at * 32); }
#pragma unroll
      for (int i = 0; i < 3; ++i) {
        if (i < ewn) {
          const int at = ew0 + i;
          const f32x4 s = sv[i];
          const u32x2 r = rv[i];
          u32x2 w;
          w[0] = pack_bf16(bf_lo(r[0]) + (s[0] + bias), bf_hi(r[0]) + (s[1] + bias));
          w[1] = pack_bf16(bf_lo(r[1]) + (s[2] + bias), bf_hi(r[1]) + (s[3] + bias));
          *(LAS u32x2*)(ldw + res_off + at * 32) = w;
        }
      }
      bar_lds();
      if (layer == 0) {
        store_tile(rws, WS_X + tb * 81920, lds + L_RES, j, wave, lane);
        publish(ws, WS_FLAGS + FL_X + tb * 128 + j * 4, 1, 0, tag, wave, lane);
      } else {
#pragma unroll
        for (int i = 0; i < 2; ++i) {
          const int n = i * 512 + wave * 64 + lane;
          if (n < 640) {
            const int row = n / 10, ch = n - row * 10;
            *(u32x4*)(out + (long)(tb * 64 + row) * D_ + j * 80 + ch * 8) = *(const LAS u32x4*)(lds + L_RES + n * 16);
          }
        }
      }
    }
  }
  if (blockIdx.x == 0 && tid == 0) __hip_atomic_store((gu32*)(ws + WS_EPOCH), tag, RLX_AGENT);
}

extern "C" void kernel_launch(void* const* d_in, const int* in_sizes, int n_in, void* d_out, int out_size, void* d_ws,
                              size_t ws_size, hipStream_t stream) {
  (void)hipFuncSetAttribute((const void*)mega_kernel, hipFuncAttributeMaxDynamicSharedMemorySize, LDS_BYTES);
  mega_kernel<<<256, 512, LDS_BYTES, stream>>>((const u16*)d_in[0], (const u16*)d_in[1], (const u16*)d_in[2], (const u16*)d_in[3],
                                               (const u16*)d_in[4], (const u16*)d_in[6], (const u16*)d_in[7], (const u16*)d_in[8],
                                               (const u16*)d_in[9], (u16*)d_out, (char*)d_ws);
}
```
